```python
import torch

def _softmax(x, axis=-1):
    return torch.softmax(x, dim=axis)
def _gelu(x):
    return torch.nn.functional.gelu(x, approximate="tanh")
def _silu(x):
    return torch.nn.functional.silu(x)
def _sigmoid(x):
    return torch.sigmoid(x)
def _layernorm(x, axis=-1, eps=1e-5):
    m = x.mean(dim=axis, keepdim=True)
    v = x.var(dim=axis, keepdim=True, unbiased=False)
    return (x - m) / torch.sqrt(v + eps)
def _scatter_add(tgt, idx, src, axis=0):
    return tgt.clone().index_add_(axis, idx, src)
def _segment_sum(src, seg, n):
    out = torch.zeros((n,) + src.shape[1:], dtype=src.dtype)
    return out.index_add_(0, seg, src)
def _pad(x, pw):
    flat = tuple(v for pair in reversed(pw) for v in pair)
    return torch.nn.functional.pad(x, flat)

kernel_name = 'parallel_sum__linear_attn_block__geglu_ffn__prompt'

def setup_inputs():

    _w = (lambda a: (lambda t: t.to(torch.bfloat16) if t.is_floating_point() else t)(torch.as_tensor(a)))
    def _A_setup():
        B, S, D = 1, 2048, 640
        return {
            "x": _w((lambda *s: torch.randn(*s, dtype=torch.bfloat16) * 0.05)(B, S, D)),
            "wqkv": _w((lambda *s: torch.randn(*s, dtype=torch.bfloat16) * 0.2)(D, 3 * D)),
            "wo": _w((lambda *s: torch.randn(*s, dtype=torch.bfloat16) * 0.2)(D, D)),
        }
    _A_in = _A_setup()

    def _B_setup():
        B, S, D, F = 1, 2048, 640, 1728
        return {
            "x": _w((lambda *s: torch.randn(*s, dtype=torch.bfloat16) * 0.05)(B, S, D)),
            "w_gate": _w((lambda *s: torch.randn(*s, dtype=torch.bfloat16) * 0.05)(D, F)),
            "w_up": _w((lambda *s: torch.randn(*s, dtype=torch.bfloat16) * 0.05)(D, F)),
            "w_down": _w((lambda *s: torch.randn(*s, dtype=torch.bfloat16) * 0.05)(F, D)),
        }
    _B_in = _B_setup()

    return {
        "A_x": _A_in["x"],
        "A_wqkv": _A_in["wqkv"],
        "A_wo": _A_in["wo"],
        "B_x": _B_in["x"],
        "B_w_gate": _B_in["w_gate"],
        "B_w_up": _B_in["w_up"],
        "B_w_down": _B_in["w_down"],
    }

def reference(A_x, A_wqkv, A_wo, B_x, B_w_gate, B_w_up, B_w_down):
    def _A_ref(x, wqkv, wo):
        B, S, D = x.shape
        H = 32; Dh = D // H
        qkv = (lambda x, *d: x.permute(*d))((x @ wqkv).reshape(B, S, 3, H, Dh), 2, 0, 3, 1, 4)
        q, k, v = qkv[0], qkv[1], qkv[2]
        phi = lambda t: torch.where(t > 0, t + 1, torch.exp(t)) + 1e-6
        pq, pk = phi(q), phi(k)
        kv = torch.einsum("bhsd,bhse->bhde", pk, v)
        z = torch.einsum("bhsd->bhd", pk)
        num = torch.einsum("bhqd,bhde->bhqe", pq, kv)
        den = torch.einsum("bhqd,bhd->bhq", pq, z)[..., None] + 1e-6
        o = num / den
        return (lambda x, *d: x.permute(*d))(o, 0, 2, 1, 3).reshape(B, S, D) @ wo

    def _B_ref(x, w_gate, w_up, w_down):
        return (_gelu(x @ w_gate) * (x @ w_up)) @ w_down

    x = A_x
    xn = _layernorm(x, axis=-1)
    return x + _A_ref(xn, A_wqkv, A_wo) + _B_ref(xn, B_w_gate, B_w_up, B_w_down)
```

```cpp
#include <hip/hip_runtime.h>
#include <hip/hip_bf16.h>
typedef __hip_bfloat16 bf16;
#define S_ 2048
#define D_ 640
#define F_ 1728
#define H_ 32
#define DH_ 20
__device__ inline float b2f(bf16 v){return __bfloat162float(v);}
__global__ void nv_ln(const bf16* x, float* xn){
  int r=blockIdx.x; int t=threadIdx.x;
  float s=0; for(int i=t;i<D_;i+=64) s+=b2f(x[r*D_+i]);
  for(int o=32;o;o>>=1) s+=__shfl_xor(s,o);
  float m=s/D_; float v=0; for(int i=t;i<D_;i+=64){float d=b2f(x[r*D_+i])-m; v+=d*d;}
  for(int o=32;o;o>>=1) v+=__shfl_xor(v,o);
  float rs=1.0f/sqrtf(v/D_+1e-5f);
  for(int i=t;i<D_;i+=64) xn[r*D_+i]=(b2f(x[r*D_+i])-m)*rs;
}
__global__ void nv_gemm(const float* A, const bf16* B, float* C, int M,int N,int K){
  int n=blockIdx.x*64+threadIdx.x; int m0=blockIdx.y*8;
  if(n>=N) return;
  float acc[8]={0,0,0,0,0,0,0,0};
  for(int k=0;k<K;k++){ float b=b2f(B[(size_t)k*N+n]);
    #pragma unroll
    for(int i=0;i<8;i++) acc[i]+=A[(size_t)(m0+i)*K+k]*b; }
  #pragma unroll
  for(int i=0;i<8;i++) C[(size_t)(m0+i)*N+n]=acc[i];
}
__device__ inline float phi(float t){ return (t>0? t+1.f: expf(t))+1e-6f; }
__global__ void nv_kv(const float* qkv, float* kv, float* z){
  int h=blockIdx.x; int t=threadIdx.x;
  if(t<400){ int d=t/20,e=t%20; float s=0; for(int r=0;r<S_;r++) s+=phi(qkv[(size_t)r*1920+640+h*20+d])*qkv[(size_t)r*1920+1280+h*20+e]; kv[h*400+t]=s; }
  else if(t<420){ int d=t-400; float s=0; for(int r=0;r<S_;r++) s+=phi(qkv[(size_t)r*1920+640+h*20+d]); z[h*20+d]=s; }
}
__global__ void nv_o(const float* qkv, const float* kv, const float* z, float* o){
  int r=blockIdx.x; int c=threadIdx.x;
  int h=c/20,e=c%20; float num=0,den=0;
  for(int d=0;d<20;d++){ float p=phi(qkv[(size_t)r*1920+h*20+d]); num+=p*kv[h*400+d*20+e]; den+=p*z[h*20+d]; }
  o[(size_t)r*D_+c]=num/(den+1e-6f);
}
__global__ void nv_h(const float* g, const float* u, float* h, int n){
  int i=blockIdx.x*256+threadIdx.x; if(i>=n) return;
  float x=g[i]; float t=tanhf(0.7978845608028654f*(x+0.044715f*x*x*x));
  h[i]=0.5f*x*(1.f+t)*u[i];
}
__global__ void nv_out(const bf16* x,const float* a,const float* b, bf16* out,int n){
  int i=blockIdx.x*256+threadIdx.x; if(i>=n) return;
  out[i]=__float2bfloat16(b2f(x[i])+a[i]+b[i]);
}
extern "C" void kernel_launch(void* const* d_in, const int* in_sizes, int n_in,
                              void* d_out, int out_size, void* d_ws, size_t ws_size,
                              hipStream_t stream) {
  const bf16* x=(const bf16*)d_in[0]; const bf16* wqkv=(const bf16*)d_in[1]; const bf16* wo=(const bf16*)d_in[2];
  const bf16* wg=(const bf16*)d_in[4]; const bf16* wu=(const bf16*)d_in[5]; const bf16* wd=(const bf16*)d_in[6];
  float* ws=(float*)d_ws;
  float* xn=ws; float* qkv=xn+S_*D_; float* g=qkv+S_*1920; float* u=g+S_*F_; float* h=u+S_*F_;
  float* o=h+S_*F_; float* a=o+S_*D_; float* b=a+S_*D_; float* kv=b+S_*D_; float* z=kv+32*400;
  nv_ln<<<S_,64,0,stream>>>(x,xn);
  nv_gemm<<<dim3(30,S_/8),64,0,stream>>>(xn,wqkv,qkv,S_,1920,D_);
  nv_gemm<<<dim3(27,S_/8),64,0,stream>>>(xn,wg,g,S_,F_,D_);
  nv_gemm<<<dim3(27,S_/8),64,0,stream>>>(xn,wu,u,S_,F_,D_);
  nv_kv<<<32,448,0,stream>>>(qkv,kv,z);
  nv_o<<<S_,640,0,stream>>>(qkv,kv,z,o);
  nv_h<<<(S_*F_+255)/256,256,0,stream>>>(g,u,h,S_*F_);
  nv_gemm<<<dim3(10,S_/8),64,0,stream>>>(o,wo,a,S_,D_,D_);
  nv_gemm<<<dim3(10,S_/8),64,0,stream>>>(h,wd,b,S_,D_,F_);
  nv_out<<<(S_*D_+255)/256,256,0,stream>>>(x,a,b,(bf16*)d_out,S_*D_);
}
```

```cpp
#include <hip/hip_runtime.h>
#include <stdint.h>

#define GAS __attribute__((address_space(1)))
#define LAS __attribute__((address_space(3)))
typedef __attribute__((ext_vector_type(2))) __bf16 bf16x2;
typedef __attribute__((ext_vector_type(8))) __bf16 bf16x8;
typedef __attribute__((ext_vector_type(4))) short s16x4;
typedef __attribute__((ext_vector_type(4))) float f32x4;
typedef __attribute__((ext_vector_type(2))) float f32x2;
typedef __attribute__((ext_vector_type(4))) unsigned u32x4;
typedef __attribute__((ext_vector_type(2))) unsigned u32x2;
typedef unsigned long long u64;

#define DEVI __device__ __forceinline__
#define WAIT_V(n) asm volatile("s_waitcnt vmcnt(" #n ")" ::: "memory")
#define WAIT_L0() do { __builtin_amdgcn_s_waitcnt(0xC07F); asm volatile("" ::: "memory"); } while (0)
#define SB() __builtin_amdgcn_sched_barrier(0)
#define PIN(x) asm volatile("" : "+v"(x))
#define BAR() __builtin_amdgcn_s_barrier()
#define RLX_AGENT __ATOMIC_RELAXED, __HIP_MEMORY_SCOPE_AGENT

#define STAMP(kern, i)

constexpr int kS = 2048, kD = 640, kF = 1728, kQKV = 1920;
constexpr unsigned OFF_CNT = 0;
constexpr unsigned OFF_H   = 4096;
constexpr unsigned OFF_Q   = OFF_H + 32 * 54 * 4096;
constexpr unsigned OFF_WD  = OFF_Q + 32 * 20 * 4096;
constexpr unsigned OFF_WO  = OFF_WD + 8 * 54 * 5 * 1024;
constexpr unsigned OFF_KVP = OFF_WO + 32 * 40 * 1024;
constexpr unsigned OFF_MP  = OFF_KVP + 32 * 16 * 420 * 4;
constexpr unsigned OFF_Z   = OFF_MP + 8 * 20 * 5 * 1024;
constexpr unsigned WS_END  = OFF_Z + 8 * 640 * 2;

constexpr int L1_B = 0, L1_BST = 24576;
constexpr int L1_A = 4 * L1_BST, L1_AST = 16384;
constexpr int L1_ST = L1_A + 3 * L1_AST;
constexpr int L1_CS = L1_ST + 1024;
constexpr int L1_SIZE = L1_CS + 4 * 6 * 16 * 4;
constexpr int L2_PA = 0;
constexpr int L2_PF = 4 * 20 * 1024;
constexpr int L2_RED = L2_PF + 4 * 15 * 1024;
constexpr int L2_FLAG = L2_RED + 2 * 420 * 4;
constexpr int L2_SIZE = L2_FLAG + 16;

DEVI f32x4 mfma(u32x4 a, u32x4 b, f32x4 c) {
  return __builtin_amdgcn_mfma_f32_16x16x32_bf16(__builtin_bit_cast(bf16x8, a), __builtin_bit_cast(bf16x8, b), c, 0, 0, 0);
}
DEVI unsigned pk2(float lo, float hi) {
  f32x2 f = {lo, hi};
  return __builtin_bit_cast(unsigned, __builtin_convertvector(f, bf16x2));
}
DEVI float dot2(unsigned a, unsigned b, float c) {
  return __builtin_amdgcn_fdot2_f32_bf16(__builtin_bit_cast(bf16x2, a), __builtin_bit_cast(bf16x2, b), c, false);
}
DEVI float bflo(unsigned u) { return __builtin_bit_cast(float, u << 16); }
DEVI float bfhi(unsigned u) { return __builtin_bit_cast(float, u & 0xffff0000u); }
DEVI float phi(float t) { return (t > 0.f ? t + 1.f : __builtin_amdgcn_exp2f(t * 1.4426950408889634f)) + 1e-6f; }
DEVI float sum_g(float v) {
  v += __shfl_xor(v, 16);
  v += __shfl_xor(v, 32);
  return v;
}
template <int IMM>
DEVI void dma16(const char* sbase, unsigned voff, unsigned ldsaddr) {
  asm volatile("s_mov_b32 m0, %2\n\ts_nop 0\n\tglobal_load_lds_dwordx4 %0, %1 offset:%3"
               :: "v"(voff), "s"(sbase), "s"(ldsaddr - IMM), "i"(IMM) : "memory");
}
DEVI u32x4 lds_b128(LAS char* p) { return *(LAS u32x4*)p; }
DEVI u32x2 lds_tr(LAS char* p) {
  return __builtin_bit_cast(u32x2, __builtin_amdgcn_ds_read_tr16_b64_v4i16((LAS s16x4*)p));
}

struct K1Ctx {
  LAS char* lds;
  const char* wbase;
  const char* xbase;
  unsigned ldsb;
  unsigned pitch;
  unsigned voffB, voffA;
  int w, rot;
};

DEVI int rotblk(int b, int rot) { int k = b + rot; return k >= 10 ? k - 10 : k; }
DEVI const char* k1_srcB(const K1Ctx& c, int s) {
  return c.wbase + (size_t)((unsigned)(rotblk(s >> 1, c.rot) * 64 + (s & 1) * 32) * c.pitch);
}
DEVI unsigned k1_dstB(const K1Ctx& c, int s) { return c.ldsb + L1_B + (s & 3) * L1_BST + c.w * 3072; }
DEVI void k1_issueB(const K1Ctx& c, int s) {
  const char* g = k1_srcB(c, s);
  const unsigned l = k1_dstB(c, s);
  dma16<0>(g, c.voffB, l);
  dma16<128>(g, c.voffB, l + 1024);
  dma16<256>(g, c.voffB, l + 2048);
}
DEVI void k1_issueA(const K1Ctx& c, int b, int slot, int hp) {
  const char* g = c.xbase + (size_t)(unsigned)(hp * 64 * 1280 + rotblk(b, c.rot) * 128);
  dma16<0>(g, c.voffA, c.ldsb + L1_A + slot * L1_AST + hp * 8192 + c.w * 1024);
}
DEVI void k1_prologue(const K1Ctx& c) {
  k1_issueA(c, 0, 0, 0);
  k1_issueB(c, 0); k1_issueA(c, 0, 0, 1);
  k1_issueB(c, 1); k1_issueA(c, 1, 1, 0);
  k1_issueB(c, 2); k1_issueA(c, 1, 1, 1);
  k1_issueB(c, 3); k1_issueA(c, 2, 2, 0);
}

template <bool SWAP>
DEVI void k1_stage(const K1Ctx& c, const int (&boff)[6], const int (&aoff)[4], f32x4 (&acc)[4][6],
                   const u32x4 (&ca)[4], const u32x4 (&cb)[6], u32x4 (&na)[4], u32x4 (&nb)[6],
                   float& rsum, float& rsq, float (&cs)[3], int rdB, int rdA, int sub,
                   bool isB, int sB, bool isA, int bA, int slotA, int hpA) {
  LAS char* pb = c.lds + L1_B + rdB * L1_BST;
  LAS char* pa = c.lds + L1_A + rdA * L1_AST;
#pragma unroll
  for (int k = 0; k < 24; ++k) {
    const int i = k >> 2, m = k & 3;
    acc[m][i] = SWAP ? mfma(ca[m], cb[i], acc[m][i]) : mfma(cb[i], ca[m], acc[m][i]);
#define ABL 0
    if (k < 4) { if (!(ABL & 2)) na[k] = lds_b128(pa + (aoff[k] ^ (sub * 64))); else na[k] = ca[k]; }
    else if (k < 16 && (ABL & 4)) { nb[(k - 4) >> 1] = cb[(k - 4) >> 1]; }
    else if (k < 16) {
      const int t = (k - 4) >> 1, hh = (k - 4) & 1;
      const u32x2 v = lds_tr(pb + boff[t] + hh * 512);
      nb[t][2 * hh] = v[0]; nb[t][2 * hh + 1] = v[1];
    } else if (k == 16) { if (isB && !(ABL & 1)) k1_issueB(c, sB); }
    else if (k == 18) { if (isA && !(ABL & 1)) k1_issueA(c, bA, slotA, hpA); }
    if (k >= 2 && k < 22 && !(ABL & 8)) {
      const int q = k - 2, d = q / 5, e = q % 5;
      if (e == 0) { rsum = dot2(ca[0][d], 0x3f803f80u, rsum); PIN(rsum); }
      else if (e == 1) { rsq = dot2(ca[0][d], ca[0][d], rsq); PIN(rsq); }
      else { cs[e - 2] = dot2(cb[e - 2][d], 0x3f803f80u, cs[e - 2]); PIN(cs[e - 2]); }
    }
    SB();
  }
}

template <bool SWAP>
DEVI void k1_mainloop(const K1Ctx& c, const int (&boff)[6], const int (&aoff)[4], f32x4 (&acc)[4][6],
                      float& rsum, float& rsq, float (&cs)[3]) {
  u32x4 fa0[4], fb0[6], fa1[4], fb1[6];
  WAIT_V(12); BAR();
#pragma unroll
  for (int i = 0; i < 4; ++i) fa0[i] = lds_b128(c.lds + L1_A + aoff[i]);
#pragma unroll
  for (int i = 0; i < 6; ++i) {
    const u32x2 lo = lds_tr(c.lds + L1_B + boff[i]), hi = lds_tr(c.lds + L1_B + boff[i] + 512);
    fb0[i] = u32x4{lo[0], lo[1], hi[0], hi[1]};
  }
  int a0 = 0;
#pragma unroll 1
  for (int b = 0; b < 10; ++b) {
    const int a1 = a0 == 2 ? 0 : a0 + 1, a2 = a1 == 2 ? 0 : a1 + 1;
    const int s = 2 * b;
    if (b < 8) WAIT_V(8); else if (b == 8) WAIT_V(7); else WAIT_V(0);
    WAIT_L0(); BAR(); SB();
    k1_stage<SWAP>(c, boff, aoff, acc, fa0, fb0, fa1, fb1, rsum, rsq, cs, (s + 1) & 3, a0, 1, b < 8, s + 4, b < 8, b + 2, a2, 1);
    if (b < 8) WAIT_V(8); else WAIT_V(3);
    WAIT_L0(); BAR(); SB();
    k1_stage<SWAP>(c, boff, aoff, acc, fa1, fb1, fa0, fb0, rsum, rsq, cs, (s + 2) & 3, a1, 0, b < 8, s + 5, b < 7, b + 3, a0, 0);
    a0 = a1;
  }
}

template <int MW>
DEVI void k1_epi_ffn(LAS char* lds, f32x4 (&acc)[4][6], char* ws, int Mt, int cg, int nw, int lane) {
  const int r = lane & 15, g = lane >> 4;
  LAS float* st = (LAS float*)(lds + L1_ST);
  f32x4 cs[6];
#pragma unroll
  for (int i = 0; i < 6; ++i) cs[i] = *(LAS f32x4*)(lds + L1_CS + ((nw * 6 + i) * 16 + 4 * g) * 4);
  char* hb = ws + OFF_H + (size_t)(unsigned)((Mt * 2 + MW) * 54 + 6 * cg) * 4096 + lane * 16;
#pragma unroll
  for (int mi = 0; mi < 4; ++mi) {
    const int mtl = (mi + nw) & 3, row = MW * 64 + mtl * 16 + r;
    const float mean = st[row], rs = st[128 + row];
    const float c0 = -2.0f * 0.7978845608028654f * 1.4426950408889634f;
    const float k1 = c0 * rs, k2 = k1 * 0.044715f * rs * rs, iv = 1.0f / (rs * rs);
    unsigned o[6];
#pragma unroll
    for (int i = 0; i < 3; ++i) {
      float h[4];
#pragma unroll
      for (int e = 0; e < 4; ++e) {
        const float G = __builtin_fmaf(-mean, cs[i][e], acc[mi][(i + 3 * MW) % 6][e]);
        const float U = __builtin_fmaf(-mean, cs[3 + i][e], acc[mi][(i + 3 + 3 * MW) % 6][e]);
        const float z = G * __builtin_fmaf(G * G, k2, k1);
        const float ex = __builtin_amdgcn_exp2f(z);
        h[e] = G * U * __builtin_amdgcn_rcpf(__builtin_fmaf(ex, iv, iv));
      }
      o[2 * i] = pk2(h[0], h[1]);
      o[2 * i + 1] = pk2(h[2], h[3]);
    }
    *(u32x4*)(hb + nw * 4096 + mtl * 1024) = u32x4{o[0], o[1], o[2], o[3]};
    *(u32x2*)(hb + (4 + (nw >> 1)) * 4096 + mtl * 1024 + 8 * (nw & 1)) = u32x2{o[4], o[5]};
  }
}

template <int MW>
DEVI void k1_epi_q(LAS char* lds, f32x4 (&acc)[4][6], char* ws, int Mt, int qi, int nw, int lane) {
  const int r = lane & 15, g = lane >> 4;
  LAS float* st = (LAS float*)(lds + L1_ST);
  f32x4 cs[6];
#pragma unroll
  for (int i = 0; i < 6; ++i) cs[i] = *(LAS f32x4*)(lds + L1_CS + ((nw * 6 + i) * 16 + 4 * g) * 4);
  const int blk = 2 * qi + (nw >> 1), s0 = (nw & 1) * 3;
  char* qb = ws + OFF_Q + (size_t)(unsigned)((Mt * 2 + MW) * 20 + blk * 5 + s0) * 4096 + lane * 16;
#pragma unroll
  for (int mi = 0; mi < 4; ++mi) {
    const int mtl = (mi + nw) & 3, row = MW * 64 + mtl * 16 + r;
    const float mean = st[row], rs = st[128 + row];
#pragma unroll
    for (int p = 0; p < 3; ++p) {
      unsigned o[4];
#pragma unroll
      for (int hh = 0; hh < 2; ++hh) {
        const int i = 2 * p + hh;
        float v[4];
#pragma unroll
        for (int e = 0; e < 4; ++e) v[e] = phi(rs * __builtin_fmaf(-mean, cs[i][e], acc[mi][(i + 3 * MW) % 6][e]));
        o[2 * hh] = pk2(v[0], v[1]);
        o[2 * hh + 1] = pk2(v[2], v[3]);
      }
      if (s0 + p < 5) *(u32x4*)(qb + p * 4096 + mtl * 1024) = u32x4{o[0], o[1], o[2], o[3]};
    }
  }
}

template <int MW>
DEVI void k1_epi_kv(LAS char* lds, f32x4 (&acc)[4][6], char* ws, int Mt, int kj, int nw, int lane) {
  const int cl = lane & 15, g = lane >> 4;
  float cs[6];
#pragma unroll
  for (int i = 0; i < 6; ++i) cs[i] = *(LAS float*)(lds + L1_CS + ((nw * 6 + i) * 16 + cl) * 4);
  u32x4 fr[2][6];
#pragma unroll
  for (int mi = 0; mi < 4; ++mi) {
    const int mtl = (mi + nw) & 3, row = MW * 64 + mtl * 16 + 4 * g;
    const f32x4 mean = *(LAS f32x4*)(lds + L1_ST + row * 4), rs = *(LAS f32x4*)(lds + L1_ST + 512 + row * 4);
#pragma unroll
    for (int i = 0; i < 6; ++i) {
      float v[4];
#pragma unroll
      for (int e = 0; e < 4; ++e) {
        float t = rs[e] * __builtin_fmaf(-mean[e], cs[i], acc[mi][(i + 3 * MW) % 6][e]);
        if (i < 3) t = phi(t);
        if (i == 5) t = cl >= 8 ? 1.0f : t;
        v[e] = t;
      }
      fr[mi >> 1][i][(mi & 1) * 2] = pk2(v[0], v[1]);
      fr[mi >> 1][i][(mi & 1) * 2 + 1] = pk2(v[2], v[3]);
    }
  }
  constexpr int KI[7] = {0, 0, 2, 1, 1, 2, 2}, VI[7] = {3, 5, 3, 4, 5, 4, 5};
  f32x4 p[7];
#pragma unroll
  for (int q = 0; q < 7; ++q) {
    p[q] = mfma(fr[0][KI[q]], fr[0][VI[q]], f32x4{0.f, 0.f, 0.f, 0.f});
    p[q] = mfma(fr[1][KI[q]], fr[1][VI[q]], p[q]);
  }
  LAS char* ex = lds + nw * 7168 + lane * 16;
  if (MW == 1) {
#pragma unroll
    for (int q = 0; q < 7; ++q) *(LAS f32x4*)(ex + q * 1024) = p[q];
  }
  WAIT_L0(); BAR();
  if (MW == 0) {
#pragma unroll
    for (int q = 0; q < 7; ++q) p[q] += *(LAS f32x4*)(ex + q * 1024);
    const int h0 = kj * 8 + 2 * nw;
    float* o0 = (float*)(ws + OFF_KVP) + (size_t)(unsigned)(h0 * 16 + Mt) * 420;
    float* o1 = o0 + 16 * 420;
    *(f32x4*)(o0 + cl * 20 + 4 * g) = p[0];
    *(f32x4*)(o1 + cl * 20 + 4 * g) = p[3];
    if (cl < 4 || cl == 8) *(f32x4*)(o0 + (cl == 8 ? 20 : 16 + cl) * 20 + 4 * g) = p[1];
    if (cl >= 4 && cl <= 8) *(f32x4*)(o1 + (12 + cl) * 20 + 4 * g) = p[4];
    if (g == 0) *(f32x4*)(o0 + cl * 20 + 16) = p[2];
    if (g == 1) *(f32x4*)(o1 + cl * 20 + 16) = p[5];
    if (g == 0 && (cl < 4 || cl == 8)) *(f32x4*)(o0 + (cl == 8 ? 20 : 16 + cl) * 20 + 16) = p[6];
    if (g == 1 && cl >= 4 && cl <= 8) *(f32x4*)(o1 + (12 + cl) * 20 + 16) = p[6];
  }
}

DEVI void k1_retile(const char* wd, const char* wo, char* ws, int id, int w, int lane) {
  const int c4 = lane & 15, g = lane >> 4;
  for (int it = id * 8 + w; it < 860; it += 128) {
    const bool isd = it < 540;
    const int u = isd ? it : it - 540, ks = u / 10, cgp = u - ks * 10;
    u32x2 v[8];
#pragma unroll
    for (int jj = 0; jj < 8; ++jj) {
      int row;
      bool ok = true;
      if (isd) {
        const int cg = ks / 6, uu = ks - cg * 6;
        row = cg * 192 + (uu < 4 ? 64 * (jj >> 2) + 16 * uu : 128 + 32 * (uu - 4) + 16 * (jj >> 2)) + 4 * g + (jj & 3);
      } else {
        const int e = 8 * g + jj;
        ok = e < 20;
        row = ks * 20 + (ok ? e : 0);
      }
      v[jj] = *(const u32x2*)((isd ? wd : wo) + (size_t)(unsigned)(row * 1280 + cgp * 128 + c4 * 8));
      if (!ok) v[jj] = u32x2{0u, 0u};
    }
    const int nt = cgp * 4 + (c4 >> 2);
    char* dst;
    if (isd) { const int j = nt / 5, t = nt - j * 5; dst = ws + OFF_WD + (size_t)(unsigned)((j * 54 + ks) * 5 + t) * 1024; }
    else dst = ws + OFF_WO + (size_t)(unsigned)(ks * 40 + nt) * 1024;
    dst += (g * 16 + (c4 & 3) * 4) * 16;
#pragma unroll
    for (int cc = 0; cc < 4; ++cc) {
      u32x4 o;
#pragma unroll
      for (int p = 0; p < 4; ++p) {
        const unsigned a = v[2 * p][cc >> 1], b = v[2 * p + 1][cc >> 1];
        o[p] = (cc & 1) ? __builtin_amdgcn_perm(b, a, 0x07060302u) : __builtin_amdgcn_perm(b, a, 0x05040100u);
      }
      *(u32x4*)(dst + cc * 16) = o;
    }
  }
}

__global__ __launch_bounds__(512, 2) void k1(const char* __restrict__ x, const char* __restrict__ wqkv,
                                             const char* __restrict__ wo, const char* __restrict__ wg,
                                             const char* __restrict__ wu, const char* __restrict__ wd, char* ws) {
  extern __shared__ __attribute__((aligned(16))) char smem[];
  LAS char* lds = (LAS char*)smem;
  const int bid = blockIdx.x, xcd = bid & 7, idx = bid >> 3;
  const int Mt = (xcd & 1) * 8 + (idx & 7), cslot = (xcd >> 1) * 4 + (idx >> 3);
  const int w = __builtin_amdgcn_readfirstlane(threadIdx.x >> 6), lane = threadIdx.x & 63;
  STAMP(0, 0);
  if (cslot == 15) {
    if (Mt == 0 && threadIdx.x < 8) *(unsigned*)(ws + OFF_CNT + threadIdx.x * 256) = 0u;
    k1_retile(wd, wo, ws, Mt, w, lane);
    WAIT_V(0);
    STAMP(0, 4);
    return;
  }
  const int type = cslot < 9 ? 0 : cslot < 13 ? 1 : 2;
  const int mw = w >> 2, nw = (w + mw) & 3;
  K1Ctx c;
  c.lds = lds; c.w = w; c.ldsb = (unsigned)(size_t)lds;
  c.rot = (Mt + cslot) % 10;
  {
    const int seg = w >> 2;
    if (type == 0) { c.pitch = kF * 2; c.wbase = (seg ? wu : wg) + cslot * 384; }
    else {
      c.pitch = kQKV * 2;
      const int startb = type == 1 ? (640 * (1 + seg) + 160 * (cslot - 9)) * 2 : 320 * (2 * (cslot - 13) + seg);
      c.wbase = wqkv + (startb & ~127);
    }
    const int i = lane >> 3, cp = lane & 7, pg = w & 3;
    c.voffB = (unsigned)(8 * pg + i) * c.pitch + 16 * (cp ^ ((i & 2) | ((pg & 1) << 2)));
    c.voffA = (unsigned)(8 * w + i) * 1280 + 16 * (cp ^ ((4 * w + (i >> 1)) & 7));
    c.xbase = x + (size_t)Mt * 128 * 1280;
  }
  k1_prologue(c);
  SB();
  int boff[6], aoff[4];
  {
    const int g = lane >> 4, q = (lane & 15) >> 2, p = lane & 3, r = lane & 15;
    const int swz = (q & 2) | ((g & 1) << 2);
#pragma unroll
    for (int pos = 0; pos < 6; ++pos) {
      int i = pos + 3 * mw; i = i >= 6 ? i - 6 : i;
      const int i3 = i >= 3 ? i - 3 : i;
      int seg = i >= 3, col;
      if (type == 0) col = 64 * i3 + 16 * nw + 4 * p;
      else if (type == 1) {
        const int hb = ((cslot - 9) & 1) * 32 + 40 * nw;
        col = i3 == 0 ? hb + 4 * p : i3 == 1 ? hb + 20 + 4 * p : hb + 16 + (p == 1 ? 20 : 0);
      } else {
        seg = nw >> 1;
        int tau = (nw & 1) * 6 + i; tau = tau > 9 ? 9 : tau;
        col = seg * 32 + 40 * p + 4 * tau;
      }
      boff[pos] = seg * 12288 + (g * 3 + (col >> 6)) * 1024 + q * 128 + 16 * (((col & 63) >> 3) ^ swz) + 8 * ((col >> 2) & 1);
    }
#pragma unroll
    for (int mi = 0; mi < 4; ++mi) aoff[mi] = (mw * 64 + ((mi + nw) & 3) * 16 + r) * 128 + 16 * (g ^ ((r >> 1) & 7));
  }
  f32x4 acc[4][6];
#pragma unroll
  for (int m = 0; m < 4; ++m)
#pragma unroll
    for (int i = 0; i < 6; ++i) acc[m][i] = f32x4{0.f, 0.f, 0.f, 0.f};
  float rsum = 0.f, rsq = 0.f, cs[3] = {0.f, 0.f, 0.f};
  if (type == 1) k1_mainloop<true>(c, boff, aoff, acc, rsum, rsq, cs);
  else k1_mainloop<false>(c, boff, aoff, acc, rsum, rsq, cs);
  STAMP(0, 2);
  rsum = sum_g(rsum); rsq = sum_g(rsq);
#pragma unroll
  for (int i = 0; i < 3; ++i) cs[i] = sum_g(cs[i]);
  if (lane < 16) {
    const float mean = rsum * (1.0f / kD);
    const float var = fmaxf(rsq * (1.0f / kD) - mean * mean, 0.f);
    const int row = mw * 64 + nw * 16 + lane;
    *(LAS float*)(lds + L1_ST + row * 4) = mean;
    *(LAS float*)(lds + L1_ST + 512 + row * 4) = 1.0f / __builtin_sqrtf(var + 1e-5f);
#pragma unroll
    for (int i = 0; i < 3; ++i) *(LAS float*)(lds + L1_CS + ((nw * 6 + 3 * mw + i) * 16 + lane) * 4) = cs[i];
  }
  WAIT_L0(); BAR();
  if (type == 0) { if (mw) k1_epi_ffn<1>(lds, acc, ws, Mt, cslot, nw, lane); else k1_epi_ffn<0>(lds, acc, ws, Mt, cslot, nw, lane); }
  else if (type == 1) { if (mw) k1_epi_kv<1>(lds, acc, ws, Mt, cslot - 9, nw, lane); else k1_epi_kv<0>(lds, acc, ws, Mt, cslot - 9, nw, lane); }
  else { if (mw) k1_epi_q<1>(lds, acc, ws, Mt, cslot - 13, nw, lane); else k1_epi_q<0>(lds, acc, ws, Mt, cslot - 13, nw, lane); }
  STAMP(0, 3);
  STAMP(0, 4);
}

__global__ __launch_bounds__(512, 2) void k2(const char* __restrict__ x, char* ws, char* __restrict__ out) {
  extern __shared__ __attribute__((aligned(16))) char smem[];
  LAS char* lds = (LAS char*)smem;
  const int bid = blockIdx.x, xcd = bid & 7, idx = bid >> 3;
  const int mt2 = (xcd >> 1) * 8 + (idx & 7), j = (xcd & 1) * 4 + (idx >> 3);
  const int w = __builtin_amdgcn_readfirstlane(threadIdx.x >> 6), lane = threadIdx.x & 63;
  const int cl = lane & 15, g = lane >> 4;
  STAMP(1, 0);
  f32x4 acc[4][5];
#pragma unroll
  for (int m = 0; m < 4; ++m)
#pragma unroll
    for (int t = 0; t < 5; ++t) acc[m][t] = f32x4{0.f, 0.f, 0.f, 0.f};
  if (w < 4) {
    const char* ap = ws + OFF_H + (size_t)(unsigned)(mt2 * 54 + w) * 4096 + lane * 16;
    const char* bp = ws + OFF_WD + (size_t)(unsigned)(j * 54 + w) * 5120 + lane * 16;
    unsigned ao[4];
#pragma unroll
    for (int m = 0; m < 4; ++m) ao[m] = ((m + w) & 3) * 1024;
    u32x4 fa[4][4], fb[4][5];
    auto ld = [&](int i) {
#pragma unroll
      for (int m = 0; m < 4; ++m) fa[i & 3][m] = *(const u32x4*)(ap + i * 16384 + ao[m]);
#pragma unroll
      for (int t = 0; t < 5; ++t) fb[i & 3][t] = *(const u32x4*)(bp + i * 20480 + t * 1024);
    };
    auto mm = [&](int i) {
#pragma unroll
      for (int m = 0; m < 4; ++m)
#pragma unroll
        for (int t = 0; t < 5; ++t) acc[m][t] = mfma(fb[i & 3][t], fa[i & 3][m], acc[m][t]);
    };
#pragma unroll
    for (int i = 0; i < 4; ++i) ld(i);
    BAR();
#pragma unroll
    for (int i = 0; i < 13; ++i) {
      mm(i);
      if (i + 4 < 13) ld(i + 4);
      else if (i + 4 == 13 && w < 2) ld(13);
    }
    const size_t orow = (size_t)(mt2 * 64 + w * 16 + cl) * 1280 + j * 160 + g * 8;
    u32x2 xr[5];
#pragma unroll
    for (int t = 0; t < 5; ++t) xr[t] = *(const u32x2*)(x + orow + t * 32);
    if (w < 2) mm(13);
    STAMP(1, 2);
#pragma unroll
    for (int m = 1; m < 4; ++m)
#pragma unroll
      for (int t = 0; t < 5; ++t)
        *(LAS f32x4*)(lds + L2_PF + ((((m + w) & 3) * 3 + (m - 1)) * 5 + t) * 1024 + lane * 16) = acc[m][t];
    WAIT_L0(); BAR();
    STAMP(1, 3);
#pragma unroll
    for (int t = 0; t < 5; ++t) {
      f32x4 s = acc[0][t];
#pragma unroll
      for (int q = 0; q < 3; ++q) s += *(LAS f32x4*)(lds + L2_PF + ((w * 3 + q) * 5 + t) * 1024 + lane * 16);
#pragma unroll
      for (int q = 0; q < 4; ++q) s += *(LAS f32x4*)(lds + L2_PA + ((q * 4 + w) * 5 + t) * 1024 + lane * 16);
      *(u32x2*)(out + orow + t * 32) =
          u32x2{pk2(s[0] + bflo(xr[t][0]), s[1] + bfhi(xr[t][0])), pk2(s[2] + bflo(xr[t][1]), s[3] + bfhi(xr[t][1]))};
    }
    STAMP(1, 4);
  } else {
    const int blk = w - 4, h = mt2;
    const int ti = threadIdx.x - 256, q4 = ti & 127, half = ti >> 7;
    f32x4 part[8];
    {
      const char* pp = ws + OFF_KVP + (size_t)(unsigned)((h * 16 + half * 8) * 420 + 4 * (q4 < 105 ? q4 : 0)) * 4;
#pragma unroll
      for (int p = 0; p < 8; ++p) part[p] = *(const f32x4*)(pp + p * 1680);
    }
    u32x4 wof[5];
#pragma unroll
    for (int t = 0; t < 5; ++t) wof[t] = *(const u32x4*)(ws + OFF_WO + (size_t)(unsigned)(h * 40 + j * 5 + t) * 1024 + lane * 16);
    u32x4 pq[5][4];
    {
      const char* qp = ws + OFF_Q + (size_t)(unsigned)(mt2 * 20 + blk * 5) * 4096 + lane * 16;
#pragma unroll
      for (int s = 0; s < 5; ++s)
#pragma unroll
        for (int m = 0; m < 4; ++m) pq[s][m] = *(const u32x4*)(qp + s * 4096 + m * 1024);
    }
    {
      f32x4 s = ((part[0] + part[1]) + (part[2] + part[3])) + ((part[4] + part[5]) + (part[6] + part[7]));
      if (q4 < 105) *(LAS f32x4*)(lds + L2_RED + (half * 420 + q4 * 4) * 4) = s;
      if (ti == 0) *(LAS unsigned*)(lds + L2_FLAG) = 0u;
    }
    WAIT_L0(); BAR();
    STAMP(1, 1);
    GAS unsigned* cnt = (GAS unsigned*)(ws + OFF_CNT + j * 256);
    if (w == 4) {
      LAS float* red = (LAS float*)(lds + L2_RED);
      u32x4 kf[2];
#pragma unroll
      for (int dt = 0; dt < 2; ++dt) {
        float v[8];
#pragma unroll
        for (int jj = 0; jj < 8; ++jj) {
          const int dd = 16 * dt + cl, e = 8 * g + jj;
          const bool ok = dd < 20 && e < 20;
          const int o = ok ? e * 20 + dd : 0;
          const float t = red[o] + red[420 + o];
          v[jj] = ok ? t : 0.f;
        }
        kf[dt] = u32x4{pk2(v[0], v[1]), pk2(v[2], v[3]), pk2(v[4], v[5]), pk2(v[6], v[7])};
      }
      char* mp = ws + OFF_MP + (size_t)(unsigned)((j * 20 + (h >> 3) * 5) * 5) * 1024 + ((((h & 7) >> 1) * 16 + cl) * 16);
#pragma unroll
      for (int dt = 0; dt < 2; ++dt) {
        const int u = 20 * (h & 1) + 16 * dt + 4 * g;
        char* dst = mp + (u >> 3) * 5120 + ((u >> 2) & 1) * 8;
#pragma unroll
        for (int t = 0; t < 5; ++t) {
          const f32x4 d = mfma(kf[dt], wof[t], f32x4{0.f, 0.f, 0.f, 0.f});
          const u64 val = (u64)pk2(d[0], d[1]) | ((u64)pk2(d[2], d[3]) << 32);
          if (dt == 0 || g == 0) __hip_atomic_store((GAS u64*)(dst + t * 1024), val, RLX_AGENT);
        }
      }
      if (lane < 5) {
        const f32x4 z = *(LAS f32x4*)(red + 400 + 4 * lane) + *(LAS f32x4*)(red + 820 + 4 * lane);
        const u64 val = (u64)pk2(z[0], z[1]) | ((u64)pk2(z[2], z[3]) << 32);
        __hip_atomic_store((GAS u64*)(ws + OFF_Z + (j * 640 + h * 20 + 4 * lane) * 2), val, RLX_AGENT);
      }
      WAIT_V(0);
      if (lane == 0) __hip_atomic_fetch_add(cnt, 1u, RLX_AGENT);
      STAMP(1, 5);
      for (unsigned spins = 0; spins < (1u << 20); ++spins) {
        if ((unsigned)__builtin_amdgcn_readfirstlane(__hip_atomic_load(cnt, RLX_AGENT)) >= 32u) break;
        __builtin_amdgcn_s_sleep(1);
      }
      asm volatile("" ::: "memory");
      if (lane == 0) __hip_atomic_store((LAS unsigned*)(lds + L2_FLAG), 1u, __ATOMIC_RELAXED, __HIP_MEMORY_SCOPE_WORKGROUP);
    } else {
      while (__builtin_amdgcn_readfirstlane(__hip_atomic_load((LAS unsigned*)(lds + L2_FLAG), __ATOMIC_RELAXED, __HIP_MEMORY_SCOPE_WORKGROUP)) == 0u)
        __builtin_amdgcn_s_sleep(1);
    }
    asm volatile("" ::: "memory");
    STAMP(1, 2);
    const __amdgpu_buffer_rsrc_t rs = __builtin_amdgcn_make_buffer_rsrc(ws + OFF_MP, 0, WS_END - OFF_MP, 0x00020000);
    u32x4 zf[5];
#pragma unroll
    for (int s = 0; s < 5; ++s)
      zf[s] = __builtin_amdgcn_raw_buffer_load_b128(rs, (OFF_Z - OFF_MP) + (j * 640 + blk * 160 + g * 40 + s * 8) * 2, 0, 16);
    u32x4 mf[5][5];
    const unsigned mo = (unsigned)((j * 20 + blk * 5) * 5) * 1024 + lane * 16;
#pragma unroll
    for (int s = 0; s < 3; ++s)
#pragma unroll
      for (int t = 0; t < 5; ++t) mf[s][t] = __builtin_amdgcn_raw_buffer_load_b128(rs, mo + (s * 5 + t) * 1024, 0, 16);
#pragma unroll
    for (int m = 0; m < 4; ++m) {
      float d0 = 0.f, d1 = 0.f;
#pragma unroll
      for (int u = 0; u < 10; ++u) {
        d0 = dot2(pq[u >> 2][m][u & 3], zf[u >> 2][u & 3], d0);
        d1 = dot2(pq[(u + 10) >> 2][m][(u + 10) & 3], zf[(u + 10) >> 2][(u + 10) & 3], d1);
      }
      const float i0 = 1.0f / (d0 + 1e-6f), i1 = 1.0f / (d1 + 1e-6f);
#pragma unroll
      for (int u = 0; u < 20; ++u) {
        const unsigned v = pq[u >> 2][m][u & 3];
        const float sc = u < 10 ? i0 : i1;
        pq[u >> 2][m][u & 3] = pk2(bflo(v) * sc, bfhi(v) * sc);
      }
    }
#pragma unroll
    for (int s = 3; s < 5; ++s)
#pragma unroll
      for (int t = 0; t < 5; ++t) mf[s][t] = __builtin_amdgcn_raw_buffer_load_b128(rs, mo + (s * 5 + t) * 1024, 0, 16);
#pragma unroll
    for (int s = 0; s < 5; ++s)
#pragma unroll
      for (int m = 0; m < 4; ++m)
#pragma unroll
        for (int t = 0; t < 5; ++t) acc[m][t] = mfma(mf[s][t], pq[s][m], acc[m][t]);
#pragma unroll
    for (int m = 0; m < 4; ++m)
#pragma unroll
      for (int t = 0; t < 5; ++t) *(LAS f32x4*)(lds + L2_PA + ((blk * 4 + m) * 5 + t) * 1024 + lane * 16) = acc[m][t];
    STAMP(1, 3);
    WAIT_L0(); BAR();
    STAMP(1, 4);
  }
}

extern "C" void kernel_launch(void* const* d_in, const int* in_sizes, int n_in, void* d_out, int out_size,
                              void* d_ws, size_t ws_size, hipStream_t stream) {
  (void)in_sizes; (void)n_in; (void)out_size; (void)ws_size;
  (void)hipFuncSetAttribute((const void*)k1, hipFuncAttributeMaxDynamicSharedMemorySize, L1_SIZE);
  (void)hipFuncSetAttribute((const void*)k2, hipFuncAttributeMaxDynamicSharedMemorySize, L2_SIZE);
  k1<<<256, 512, L1_SIZE, stream>>>((const char*)d_in[0], (const char*)d_in[1], (const char*)d_in[2], (const char*)d_in[4],
                                    (const char*)d_in[5], (const char*)d_in[6], (char*)d_ws);
  k2<<<256, 512, L2_SIZE, stream>>>((const char*)d_in[0], (char*)d_ws, (char*)d_out);
}
```

```cpp
#include <hip/hip_runtime.h>
#include <stdint.h>

#define GAS __attribute__((address_space(1)))
#define LAS __attribute__((address_space(3)))
typedef __attribute__((ext_vector_type(2))) __bf16 bf16x2;
typedef __attribute__((ext_vector_type(8))) __bf16 bf16x8;
typedef __attribute__((ext_vector_type(4))) short s16x4;
typedef __attribute__((ext_vector_type(4))) float f32x4;
typedef __attribute__((ext_vector_type(2))) float f32x2;
typedef __attribute__((ext_vector_type(4))) unsigned u32x4;
typedef __attribute__((ext_vector_type(2))) unsigned u32x2;
typedef unsigned long long u64;
typedef __amdgpu_buffer_rsrc_t rsrc_t;

#define DEVI __device__ __forceinline__
#define WAIT_V(n) asm volatile("s_waitcnt vmcnt(" #n ")" ::: "memory")
#define WAIT_L0() do { __builtin_amdgcn_s_waitcnt(0xC07F); asm volatile("" ::: "memory"); } while (0)
#define SB() __builtin_amdgcn_sched_barrier(0)
#define PIN(x) asm volatile("" : "+v"(x))
#define PINS(x) asm volatile("" : "+s"(x))
#define BAR() do { asm volatile("" ::: "memory"); __builtin_amdgcn_s_barrier(); asm volatile("" ::: "memory"); } while (0)
#define RLX_AGENT __ATOMIC_RELAXED, __HIP_MEMORY_SCOPE_AGENT
#define RLX_WG __ATOMIC_RELAXED, __HIP_MEMORY_SCOPE_WORKGROUP
#define SC1 16

#define STAMP(i)

constexpr int kD = 640, kF = 1728, kQKV = 1920;
constexpr unsigned OFF_E   = 0;
constexpr unsigned OFF_F1  = 256;
constexpr unsigned OFF_F2  = OFF_F1 + 2048;
constexpr unsigned OFF_F3  = OFF_F2 + 2048;
constexpr unsigned OFF_Z   = 6144;
constexpr unsigned OFF_SC  = 8192;
constexpr unsigned OFF_SR  = OFF_SC + 15 * 10 * 384 * 4;
constexpr unsigned OFF_KVP = OFF_SR + 16 * 10 * 256 * 4;
constexpr unsigned OFF_MP  = OFF_KVP + 32 * 16 * 420 * 4;
constexpr unsigned OFF_WD  = OFF_MP + 8 * 20 * 5 * 1024;
constexpr unsigned OFF_Q   = OFF_WD + 8 * 54 * 5 * 1024;
constexpr unsigned OFF_H   = OFF_Q + 32 * 20 * 4096;
constexpr unsigned WS_END  = OFF_H + 32 * 54 * 4096;
constexpr unsigned OOB = 0x80000000u;
static_assert(OFF_MP % 1024 == 0 && OFF_KVP % 128 == 0, "align");

constexpr int L1_B = 0, L1_BST = 24576;
constexpr int L1_A = 4 * L1_BST, L1_AST = 16384;
constexpr int L1_ST = L1_A + 3 * L1_AST;
constexpr int L1_CS = L1_ST + 1024;
constexpr int L1_EX = 72 * 1024;
constexpr int L2_RG = 35840;
constexpr int L2_RED = L1_CS + 1536;
constexpr int L2_FLAG = L2_RED + 2 * 420 * 4;
constexpr int LDS_SIZE = L2_FLAG + 64;
enum { LF_RED = 0, LF_ST = 1, LF_H = 2, LF_Q = 3, LF_M = 4, LF_FREE = 8, LF_PA = 12 };

DEVI f32x4 mfma(u32x4 a, u32x4 b, f32x4 c) {
  return __builtin_amdgcn_mfma_f32_16x16x32_bf16(__builtin_bit_cast(bf16x8, a), __builtin_bit_cast(bf16x8, b), c, 0, 0, 0);
}
DEVI void mfma_acc(f32x4& c, u32x4 a, u32x4 b) { asm volatile("v_mfma_f32_16x16x32_bf16 %0, %1, %2, %0" : "+v"(c) : "v"(a), "v"(b)); }
DEVI void mfma_new(f32x4& c, u32x4 a, u32x4 b) { asm volatile("v_mfma_f32_16x16x32_bf16 %0, %1, %2, 0" : "=&v"(c) : "v"(a), "v"(b)); }
DEVI unsigned pk2(f32x2 f) { return __builtin_bit_cast(unsigned, __builtin_convertvector(f, bf16x2)); }
DEVI unsigned pk2(float lo, float hi) { return pk2(f32x2{lo, hi}); }
DEVI float dot2(unsigned a, unsigned b, float c) {
  return __builtin_amdgcn_fdot2_f32_bf16(__builtin_bit_cast(bf16x2, a), __builtin_bit_cast(bf16x2, b), c, false);
}
DEVI f32x2 unpk(unsigned u) { return f32x2{__builtin_bit_cast(float, u << 16), __builtin_bit_cast(float, u & 0xffff0000u)}; }
DEVI f32x2 fma2(f32x2 a, f32x2 b, f32x2 c) { return __builtin_elementwise_fma(a, b, c); }
DEVI f32x2 bc2(float v) { return f32x2{v, v}; }
DEVI f32x2 lo2(f32x4 v) { return f32x2{v[0], v[1]}; }
DEVI f32x2 hi2(f32x4 v) { return f32x2{v[2], v[3]}; }
DEVI f32x2 phi2(f32x2 g, f32x2 rs) {
  const f32x2 t = fma2(g, rs, bc2(1e-6f)), l = g * (rs * 1.4426950408889634f);
  return f32x2{fmaxf(t[0], 1e-6f), fmaxf(t[1], 1e-6f)} +
         f32x2{__builtin_amdgcn_fmed3f(__builtin_amdgcn_exp2f(l[0]), 0.f, 1.f), __builtin_amdgcn_fmed3f(__builtin_amdgcn_exp2f(l[1]), 0.f, 1.f)};
}
DEVI u32x4 lds_b128(unsigned a) { return *(LAS u32x4*)(size_t)a; }
DEVI u32x4 lds_trfrag(unsigned a) {
  const u32x2 lo = __builtin_bit_cast(u32x2, __builtin_amdgcn_ds_read_tr16_b64_v4i16((LAS s16x4*)(size_t)a));
  const u32x2 hi = __builtin_bit_cast(u32x2, __builtin_amdgcn_ds_read_tr16_b64_v4i16((LAS s16x4*)(size_t)(a + 512)));
  return u32x4{lo[0], lo[1], hi[0], hi[1]};
}
DEVI u32x4 srd(const void* p, unsigned bytes) {
  const u64 a = (u64)p;
  return u32x4{(unsigned)a, (unsigned)(a >> 32) & 0xffffu, bytes, 0x00020000u};
}
DEVI void setm0(unsigned v) { asm volatile("s_mov_b32 m0, %0" :: "s"(v) : "memory"); }
template <int IMM>
DEVI void dma_n(u32x4 r, unsigned voff, unsigned soff) {
  asm volatile("buffer_load_dwordx4 %0, %1, %2 offen offset:%3 lds" :: "v"(voff), "s"(r), "s"(soff), "i"(IMM) : "memory");
}
template <int IMM>
DEVI void dma(u32x4 r, unsigned voff, unsigned soff, unsigned lds) {
  asm volatile("s_mov_b32 m0, %4\n\ts_nop 0\n\tbuffer_load_dwordx4 %0, %1, %2 offen offset:%3 lds"
               :: "v"(voff), "s"(r), "s"(soff), "i"(IMM), "s"(lds - IMM) : "memory");
}
DEVI void dma_sc1(u32x4 r, unsigned voff, unsigned soff, unsigned lds) {
  asm volatile("s_mov_b32 m0, %3\n\ts_nop 0\n\tbuffer_load_dwordx4 %0, %1, %2 offen sc1 lds"
               :: "v"(voff), "s"(r), "s"(soff), "s"(lds) : "memory");
}
DEVI u32x2 ld8_hidden(u32x4 r, unsigned voff, unsigned soff) {
  u32x2 v;
  asm volatile("buffer_load_dwordx2 %0, %1, %2, %3 offen sc1" : "=v"(v) : "v"(voff), "s"(r), "s"(soff) : "memory");
  return v;
}
template <int N0, int N1>
DEVI void wd_prefetch(u32x4 rws, unsigned ldsb, int j, int w, int lane) {
#pragma unroll
  for (int n = N0; n < N1; ++n) {
    const unsigned a = (unsigned)(w + 8 * n + (n == 9 ? 40 : (n >= 10 && n < 15) ? -8 : 0)) * 1024;
    if (n < 17 || w < 4) dma_sc1(rws, lane * 16, OFF_WD + j * 276480 + a, ldsb + a);
  }
}
DEVI void bar_if(int f) {
  asm volatile("s_cmp_eq_u32 %0, 0\n\ts_cbranch_scc1 .Lnb%=\n\ts_barrier\n.Lnb%=:" :: "s"(f) : "scc", "memory");
}
DEVI void wait_flags(char* ws, unsigned off, u64 tag) {
#pragma unroll 1
  for (unsigned spins = 0; spins < (1u << 20); ++spins) {
    const u64 v = __hip_atomic_load((GAS u64*)(ws + off), RLX_AGENT);
    if (__all(v == tag)) break;
    __builtin_amdgcn_s_sleep(1);
  }
  asm volatile("" ::: "memory");
}
DEVI u64 make_tag(const char* ws) {
  return *(const __attribute__((address_space(4))) u64*)(ws + OFF_E) * 0x9E3779B97F4A7C15ull + 0xD1B54A32D192ED03ull;
}
DEVI void set_flag(char* ws, unsigned off, u64 tag) { __hip_atomic_store((GAS u64*)(ws + off), tag, RLX_AGENT); }
DEVI void lds_set(LAS unsigned* p, int lane) { if (lane == 0) __hip_atomic_store(p, 1u, RLX_WG); }
DEVI void lds_wait(LAS unsigned* p, unsigned want) {
  while ((unsigned)__builtin_amdgcn_readfirstlane(__hip_atomic_load(p, RLX_WG)) < want) __builtin_amdgcn_s_sleep(1);
  asm volatile("" ::: "memory");
}
DEVI u32x4 ld16(rsrc_t r, unsigned voff, unsigned soff) { return __builtin_amdgcn_raw_buffer_load_b128(r, voff, soff, SC1); }
DEVI void st16(rsrc_t r, unsigned voff, unsigned soff, u32x4 v) { __builtin_amdgcn_raw_buffer_store_b128(v, r, voff + soff, 0, SC1); }
DEVI void st8(rsrc_t r, unsigned voff, unsigned soff, u32x2 v) { __builtin_amdgcn_raw_buffer_store_b64(v, r, voff, soff, SC1); }

struct P1 {
  u32x4 rw, rx;
  unsigned ldsb, pitch;
  unsigned voffB, voffA;
  unsigned oB, oA;
  int w, mw, nw, nt6, bstar, bflag;
  int Mt, cslot;
};

template <bool SWAP, bool LATE>
DEVI void p1_loop(P1& c, unsigned (&vb)[6], unsigned va0, f32x4 (&acc)[4][6], u32x2 (&sv)[10], char* ws, rsrc_t rws, u64 tag, int lane) {
  u32x4 fa[4], fb[6];
  unsigned va1 = va0 ^ 64;
  const unsigned stepB = 32 * c.pitch, limB = 640 * c.pitch;
  const unsigned lB0 = c.ldsb + L1_B + c.w * 3072, lA0 = c.ldsb + L1_A + c.w * 1024;
  unsigned lB = lB0 + 3 * L1_BST, lA = lA0 + 2 * L1_AST;
  int dA = L1_AST, dB = 2 * L1_BST;
  f32x4 st[5];
  fb[5] = u32x4{0u, 0u, 0u, 0u};
  auto rdA = [&](int i, int sub) { fa[i] = lds_b128((sub ? va1 : va0) + i * 2048); };
  auto rdB = [&](int i, int sub) { fb[i] = lds_trfrag(vb[i] + sub * L1_BST); };
  auto rdBh = [&](int i, int hh, int sub) {
    const u32x2 v = __builtin_bit_cast(u32x2, __builtin_amdgcn_ds_read_tr16_b64_v4i16((LAS s16x4*)(size_t)(vb[i] + sub * L1_BST + hh * 512)));
    fb[i][2 * hh] = v[0]; fb[i][2 * hh + 1] = v[1];
  };
  auto L = [&](int sub, bool isdma) {
    if (isdma) setm0(lB);
    SB(); rdA(0, sub); rdA(1, sub); SB();
    if (isdma) { dma_n<0>(c.rw, c.voffB, c.oB); setm0(lB + 1024 - 128); }
    SB(); rdA(2, sub); rdA(3, sub); SB();
    if (isdma) { dma_n<128>(c.rw, c.voffB, c.oB); setm0(lB + 2048 - 256); }
    SB(); rdB(LATE ? 4 : 0, sub); SB();
    if (isdma) { dma_n<256>(c.rw, c.voffB, c.oB); setm0(lA + sub * 8192); }
    SB(); if (!LATE) rdB(1, sub); else if (!SWAP) rdB(5, sub); else asm volatile("s_nop 0"); SB();
    if (isdma) dma_n<0>(c.rx, c.voffA, c.oA + sub * 81920);
    SB();
  };
  auto stats = [&](int sub) {
    const u32x4 one = {0x3f803f80u, 0x3f803f80u, 0x3f803f80u, 0x3f803f80u};
    const u32x4 sa = lds_b128((sub ? va1 : va0) + c.nw * 2048);
    u32x4 sb[3];
#pragma unroll
    for (int i = 0; i < 3; ++i) sb[i] = lds_trfrag(vb[LATE ? i : 3 + i] + sub * L1_BST);
    SB();
    if (sub == 0) {
#pragma unroll
      for (int i = 0; i < 5; ++i) st[i] = f32x4{0.f, 0.f, 0.f, 0.f};
    }
    st[0] = mfma(one, sa, st[0]);
    st[1] = mfma(sa, sa, st[1]);
#pragma unroll
    for (int i = 0; i < 3; ++i) st[2 + i] = mfma(one, sb[i], st[2 + i]);
    if (sub == 1) {
      const int cl = lane & 15, g = lane >> 4;
      const float sq = (cl & 2) ? ((cl & 1) ? st[1][3] : st[1][2]) : ((cl & 1) ? st[1][1] : st[1][0]);
      st8(rws, (cl >> 2) == g ? (c.mw * 64 + c.nw * 16 + cl) * 8 : OOB, c.cslot < 10 ? OFF_SR + (c.Mt * 10 + c.cslot) * 1024 : WS_END,
          u32x2{__builtin_bit_cast(unsigned, st[0][0]), __builtin_bit_cast(unsigned, sq)});
      const float v = g == 0 ? st[2][0] : g == 1 ? st[3][0] : st[4][0];
      __builtin_amdgcn_raw_buffer_store_b32(__builtin_bit_cast(unsigned, v), rws, g < 3 ? ((c.nw * 6 + c.mw * 3) * 16 + lane) * 4 : OOB,
                                            c.Mt < 10 ? OFF_SC + (c.cslot * 10 + c.Mt) * 1536 : WS_END, SC1);
    }
    SB();
  };
  auto C = [&](int sub, bool more) {
    if (LATE) { WAIT_L0(); BAR(); }
    SB();
#pragma unroll
    for (int k = 0; k < 20; ++k) {
      const int i = k >> 2, m = k & 3;
      acc[m][i] = SWAP ? mfma(fa[m], fb[i], acc[m][i]) : mfma(fb[i], fa[m], acc[m][i]);
      if (LATE) {
        if (sub && m == 3) { vb[i] += dB; PIN(vb[i]); }
        if (more && i >= 1 && m < 2) rdBh(i - 1, m, sub ^ 1);
        if (sub && k == 2) { vb[5] += dB; PIN(vb[5]); }
      } else {
        if (k < (SWAP ? 6 : 8)) rdBh(2 + (k >> 1), k & 1, sub);
        if (sub && k >= 8 && k < 14) { vb[k - 8] += dB; PIN(vb[k - 8]); }
      }
      constexpr int S0 = LATE ? 0 : 8;
      const int q = LATE ? (m == 2 ? i : k < 2 ? 5 + k : -1) : k - S0 - 4;
      if (q == 0) { c.oB += stepB; lB += L1_BST; PINS(c.oB); PINS(lB); }
      if (q == 1) { c.oB = c.oB == limB ? 0u : c.oB; PINS(c.oB); }
      if (q == 2) { lB = lB == lB0 + 4 * L1_BST ? lB0 : lB; PINS(lB); }
      if (sub) {
        if (q == 3) { va0 += dA; PIN(va0); c.oA += 128; PINS(c.oA); }
        if (q == 4) { va1 += dA; PIN(va1); lA += L1_AST; PINS(lA); }
      } else {
        if (q == 3) { dA = lA == lA0 + L1_AST ? -2 * L1_AST : L1_AST; PINS(dA); }
      }
      SB();
    }
    if (!SWAP) {
      PINS(c.nt6);
      if (__builtin_expect(c.nt6 > 0, 1)) {
#pragma unroll
        for (int m = 0; m < 4; ++m) acc[m][5] = mfma(fb[5], fa[m], acc[m][5]);
      }
    }
    if (sub) {
      c.oA = c.oA == 1280u ? 0u : c.oA; lA = lA == lA0 + 3 * L1_AST ? lA0 : lA; dB = -dB;
      PINS(c.oA); PINS(lA); PINS(dB);
    }
    SB();
    if (!LATE) { WAIT_L0(); BAR(); }
  };
  WAIT_V(8); BAR();
  if (LATE) { rdB(0, 0); rdB(1, 0); rdB(2, 0); rdB(3, 0); }
#pragma unroll 1
  for (int b = 0; b < 8; ++b) {
    L(0, true);
    if (__builtin_expect(b == c.bstar, 0)) stats(0);
    WAIT_V(8);
    C(0, true);
    L(1, true);
    if (__builtin_expect(b == c.bstar, 0)) stats(1);
    if (LATE) { if (__builtin_expect(b == c.bflag, 0)) { if (lane == 0) set_flag(ws, OFF_F1 + (c.Mt * 16 + c.cslot) * 8, tag); } }
    WAIT_V(8);
    C(1, true);
  }
  const u32x4 rw4 = srd(ws, WS_END);
  const unsigned foff = lane < 10 ? OFF_F1 + (lane * 16 + c.cslot) * 8 : lane < 20 ? OFF_F1 + (c.Mt * 16 + lane - 10) * 8
                                                                                  : OFF_F2 + ((lane & 15) * 16 + 15) * 8;
  u32x2 fl = ld8_hidden(rw4, foff, 0);
  L(0, false);
  dma<0>(c.rw, c.voffB, c.oB, lB); dma<128>(c.rw, c.voffB, c.oB, lB + 1024); dma<256>(c.rw, c.voffB, c.oB, lB + 2048);
  WAIT_V(8);
  C(0, true);
  L(1, false);
  WAIT_V(3);
  PIN(fl);
  if (__builtin_expect(!__all(__builtin_bit_cast(u64, fl) == tag), 0)) wait_flags(ws, foff, tag);
  {
    const unsigned base = c.w < 3 ? OFF_SC + c.cslot * 15360 : c.w < 5 ? OFF_SR + c.Mt * 10240 - 1536 : OOB, str = c.w < 3 ? 1536 : 1024;
#pragma unroll
    for (int p = 0; p < 10; ++p) sv[p] = ld8_hidden(rw4, (c.w * 64 + lane) * 8, base + p * str);
  }
  SB();
  C(1, true);
  L(0, false);
  wd_prefetch<0, 5>(rw4, c.ldsb, c.cslot & 7, c.w, lane);
  WAIT_V(15);
  C(0, true);
  L(1, false);
  wd_prefetch<5, 10>(rw4, c.ldsb, c.cslot & 7, c.w, lane);
  WAIT_V(10);
#pragma unroll
  for (int p = 0; p < 10; ++p) PIN(sv[p]);
  if (LATE || c.w == 4) {
    f32x2 s = __builtin_bit_cast(f32x2, sv[0]);
#pragma unroll
    for (int p = 1; p < 10; ++p) s += __builtin_bit_cast(f32x2, sv[p]);
    if (c.w >= 3) {
      const float mean = s[0] * (1.0f / kD);
      const float var = fmaxf(s[1] * (1.0f / kD) - mean * mean, 0.f);
      s = f32x2{mean, __builtin_amdgcn_rsqf(var + 1e-5f)};
    }
    *(LAS f32x2*)(size_t)(c.ldsb + (c.w < 3 ? L1_CS : L1_ST - 1536) + (c.w * 64 + lane) * 8) = s;
  }
  SB();
  C(1, false);
}

DEVI void epi_ffn(LAS char* lds, f32x4 (&acc)[4][6], rsrc_t rws, const P1& c, int lane) {
  const int r = lane & 15, g = lane >> 4;
  f32x4 cs[6];
#pragma unroll
  for (int i = 0; i < 6; ++i) cs[i] = *(LAS f32x4*)(lds + L1_CS + ((c.nw * 6 + i) * 16 + 4 * g) * 4);
  const unsigned hb = OFF_H + ((c.Mt * 2 + c.mw) * 54 + 6 * c.cslot) * 4096;
  unsigned keep[2];
#pragma unroll
  for (int mi = 0; mi < 4; ++mi) {
    const f32x2 ms = *(LAS f32x2*)(lds + L1_ST + (c.mw * 64 + mi * 16 + r) * 8);
    const float rs = ms[1], c0 = -2.0f * 0.7978845608028654f * 1.4426950408889634f;
    const f32x2 nm = bc2(-ms[0]), k1 = bc2(c0 * rs), k2 = bc2(c0 * 0.044715f * rs * rs * rs), iv = bc2(__builtin_amdgcn_rcpf(rs * rs));
    unsigned o[6];
#pragma unroll
    for (int p = 0; p < 6; ++p) {
      const int i = p >> 1;
      const f32x2 G = fma2(nm, (p & 1) ? hi2(cs[i]) : lo2(cs[i]), (p & 1) ? hi2(acc[mi][i]) : lo2(acc[mi][i]));
      const f32x2 U = fma2(nm, (p & 1) ? hi2(cs[3 + i]) : lo2(cs[3 + i]), (p & 1) ? hi2(acc[mi][3 + i]) : lo2(acc[mi][3 + i]));
      const f32x2 z = G * fma2(G * G, k2, k1);
      const f32x2 d = fma2(f32x2{__builtin_amdgcn_exp2f(z[0]), __builtin_amdgcn_exp2f(z[1])}, iv, iv);
      o[p] = pk2(G * U * f32x2{__builtin_amdgcn_rcpf(d[0]), __builtin_amdgcn_rcpf(d[1])});
    }
    st16(rws, lane * 16 + mi * 1024, hb + c.nw * 4096, u32x4{o[0], o[1], o[2], o[3]});
    if (mi & 1) {
      const auto s0 = __builtin_amdgcn_permlane32_swap(keep[0], o[4], false, false);
      const auto s1 = __builtin_amdgcn_permlane32_swap(keep[1], o[5], false, false);
      st16(rws, (lane & 31) * 16 + (lane >> 5) * 1024 + (mi - 1) * 1024, hb + (4 + (c.nw >> 1)) * 4096 + (c.nw & 1) * 512,
           u32x4{s0[0], s1[0], s0[1], s1[1]});
    } else { keep[0] = o[4]; keep[1] = o[5]; }
  }
}

DEVI void epi_q(LAS char* lds, f32x4 (&acc)[4][6], rsrc_t rws, const P1& c, int lane) {
  const int r = lane & 15, g = lane >> 4;
  f32x4 cs[5];
#pragma unroll
  for (int i = 0; i < 5; ++i) cs[i] = *(LAS f32x4*)(lds + L1_CS + ((c.nw * 6 + i) * 16 + 4 * g) * 4);
  const unsigned qb = OFF_Q + ((c.Mt * 2 + c.mw) * 20 + (2 * (c.cslot - 13) + (c.nw >> 1)) * 5) * 4096;
#pragma unroll
  for (int mi = 0; mi < 4; ++mi) {
    const f32x2 ms = *(LAS f32x2*)(lds + L1_ST + (c.mw * 64 + mi * 16 + r) * 8);
    const f32x2 nm = bc2(-ms[0]), rs = bc2(ms[1]);
    unsigned o[10];
#pragma unroll
    for (int p = 0; p < 10; ++p) {
      const int i = p >> 1;
      o[p] = pk2(phi2(fma2(nm, (p & 1) ? hi2(cs[i]) : lo2(cs[i]), (p & 1) ? hi2(acc[mi][i]) : lo2(acc[mi][i])), rs));
    }
    st16(rws, lane * 16 + mi * 1024, qb + (c.nw & 1) * 12288, u32x4{o[0], o[1], o[2], o[3]});
    st16(rws, lane * 16 + mi * 1024, qb + (c.nw & 1) * 12288 + 4096, u32x4{o[4], o[5], o[6], o[7]});
    st8(rws, lane * 16 + mi * 1024, qb + 8192 + 8 * (c.nw & 1), u32x2{o[8], o[9]});
  }
}

DEVI void epi_kv(LAS char* lds, f32x4 (&acc)[4][6], rsrc_t rws, const P1& c, int lane) {
  const int cl = lane & 15, g = lane >> 4;
  float cs[5];
#pragma unroll
  for (int i = 0; i < 5; ++i) cs[i] = *(LAS float*)(lds + L1_CS + ((c.nw * 6 + i) * 16 + cl) * 4);
  u32x4 fr[2][6];
#pragma unroll
  for (int mi = 0; mi < 4; ++mi) {
    const f32x4 s0 = *(LAS f32x4*)(lds + L1_ST + (c.mw * 64 + mi * 16 + 4 * g) * 8);
    const f32x4 s1 = *(LAS f32x4*)(lds + L1_ST + (c.mw * 64 + mi * 16 + 4 * g) * 8 + 16);
#pragma unroll
    for (int i = 0; i < 5; ++i) {
#pragma unroll
      for (int hh = 0; hh < 2; ++hh) {
        const f32x4 s = hh ? s1 : s0;
        const f32x2 rs = {s[1], s[3]}, t = fma2(f32x2{-s[0], -s[2]}, bc2(cs[i]), hh ? hi2(acc[mi][i]) : lo2(acc[mi][i]));
        fr[mi >> 1][i][(mi & 1) * 2 + hh] = pk2((i == 2 || i == 3) ? t * rs : phi2(t, rs));
        if (i == 4) fr[mi >> 1][5][(mi & 1) * 2 + hh] = cl < 8 ? 0x3f803f80u : pk2(t * rs);
      }
    }
  }
  constexpr int KI[7] = {0, 0, 4, 1, 1, 4, 4}, VI[7] = {2, 5, 2, 3, 5, 3, 5};
  f32x4 p[7];
#pragma unroll
  for (int q = 0; q < 7; ++q) {
    p[q] = mfma(fr[0][KI[q]], fr[0][VI[q]], f32x4{0.f, 0.f, 0.f, 0.f});
    p[q] = mfma(fr[1][KI[q]], fr[1][VI[q]], p[q]);
  }
  LAS char* ex = lds + L1_EX + c.nw * 7168 + lane * 16;
  if (c.mw) {
#pragma unroll
    for (int q = 0; q < 7; ++q) *(LAS f32x4*)(ex + q * 1024) = p[q];
  }
  WAIT_L0(); BAR();
  if (!c.mw) {
#pragma unroll
    for (int q = 0; q < 7; ++q) p[q] += *(LAS f32x4*)(ex + q * 1024);
    const unsigned o0 = OFF_KVP + (((c.cslot - 9) * 8 + 2 * c.nw) * 16 + c.Mt) * 1680, o1 = o0 + 16 * 1680;
    const bool u0 = cl == 0 || (cl >> 2) == 2, u1 = cl == 0 || (cl >> 2) == 3;
    const int eh = cl == 0 ? 20 : 16 + (cl & 3);
    auto put = [&](unsigned o, int e, int d, f32x4 v) { st16(rws, (e * 20 + d) * 4, o, __builtin_bit_cast(u32x4, v)); };
    put(o0, cl, 4 * g, p[0]);
    put(o1, cl, 4 * g, p[3]);
    if (u0) put(o0, eh, 4 * g, p[1]);
    if (u1) put(o1, eh, 4 * g, p[4]);
    if (g == 0) put(o0, cl, 16, p[2]);
    if (g == 1) put(o1, cl, 16, p[5]);
    if (g == 0 && u0) put(o0, eh, 16, p[6]);
    if (g == 1 && u1) put(o1, eh, 16, p[6]);
  }
}

DEVI void retile(const char* wd, rsrc_t rws, unsigned ldsw, int id, int w, int lane) {
  const u32x4 rd = srd(wd, kF * 1280);
  const int jj = lane >> 3, g = lane >> 4;
  const unsigned vlo = ((jj >> 2) * 64 + (jj & 3)) * 1280 + (lane & 7) * 16, vhi = ((jj >> 2) * 8 + (jj & 3)) * 1280 + (lane & 7) * 16;
  const unsigned ra = ldsw + g * 1024 + ((lane & 15) >> 2) * 128 + (lane & 3) * 8;
  auto fetch = [&](int n) {
    const int it = id * 8 + w + n * 128, ks = it / 10, ln = it - ks * 10, cg = ks / 6, uu = ks - cg * 6;
#pragma unroll
    for (int q = 0; q < 4; ++q) {
      const int row = cg * 192 + (uu < 4 ? 16 * uu + 4 * q : 32 * uu + 16 * (q >> 1) + 4 * (q & 1));
      dma<0>(rd, uu < 4 ? vlo : vhi, it < 540 ? row * 1280 + ln * 128 : OOB, ldsw + (n & 3) * 4096 + q * 1024);
    }
  };
  auto put = [&](int n) {
    const int it = id * 8 + w + n * 128, ks = it / 10, ln = it - ks * 10;
#pragma unroll
    for (int q = 0; q < 4; ++q) {
      const int nt = ln * 4 + q, j = nt / 5, t = nt - j * 5;
      st16(rws, lane * 16, it < 540 ? OFF_WD + ((j * 54 + ks) * 5 + t) * 1024 : OOB, lds_trfrag(ra + (n & 3) * 4096 + q * 32));
    }
  };
#pragma unroll 1
  for (int n = 0; n < 4; ++n) fetch(n);
  WAIT_V(0);
  put(0);
  WAIT_L0();
  fetch(4);
#pragma unroll 1
  for (int n = 1; n < 5; ++n) {
    if (n == 4) WAIT_V(0);
    put(n);
  }
}

DEVI void fold_kv(LAS char* lds, char* ws, rsrc_t rws, u64 tag, const u32x4 (&wf)[2][10], int hp, int a, int lane) {
  const int cl = lane & 15, g = lane >> 4;
  LAS unsigned* lf = (LAS unsigned*)(lds + L2_FLAG);
  __builtin_amdgcn_s_setprio(3);
  wait_flags(ws, OFF_F2 + (cl * 16 + 9 + (hp >> 2)) * 8, tag);
  STAMP(11);
  {
    const int q4 = (a & 1) * 64 + lane, hh = a >> 1;
    f32x4 part[16];
#pragma unroll
    for (int p = 0; p < 16; ++p)
      part[p] = __builtin_bit_cast(f32x4, ld16(rws, q4 < 105 ? q4 * 16 : OOB, OFF_KVP + ((2 * hp + hh) * 16 + p) * 1680));
    SB();
#pragma unroll
    for (int d = 8; d; d >>= 1)
#pragma unroll
      for (int p = 0; p < d; ++p) part[p] += part[p + d];
    if (q4 < 105) *(LAS f32x4*)(lds + L2_RED + (hh * 420 + q4 * 4) * 4) = part[0];
    WAIT_L0();
    if (lane == 0) __hip_atomic_fetch_add(lf + LF_RED, 1u, RLX_WG);
    lds_wait(lf + LF_RED, 4u);
  }
  STAMP(12);
  LAS float* red = (LAS float*)(lds + L2_RED);
  u32x4 kf[6];
#pragma unroll
  for (int f = 0; f < 6; ++f) {
    const int hh = f < 4 ? (f & 1) : 1;
    const int d = (f < 4 ? 8 * (cl >> 2) + 4 * (f >> 1) : 32 + 4 * (f - 4)) + (cl & 3) - 20 * hh;
    float v[8];
#pragma unroll
    for (int jj = 0; jj < 8; ++jj) {
      const int e = 8 * g + jj;
      const bool ok = d >= 0 && d < 20 && e < 20;
      const float t = red[ok ? hh * 420 + e * 20 + d : 0];
      v[jj] = ok ? t : 0.f;
    }
    kf[f] = u32x4{pk2(v[0], v[1]), pk2(v[2], v[3]), pk2(v[4], v[5]), pk2(v[6], v[7])};
  }
  const f32x4 zero = {0.f, 0.f, 0.f, 0.f};
  const unsigned vo = ((hp & 3) * 16 + cl) * 16;
#pragma unroll
  for (int i = 0; i < 10; ++i) {
    const f32x4 d1 = mfma(kf[1], wf[1][i], mfma(kf[0], wf[0][i], zero)), d2 = mfma(kf[3], wf[1][i], mfma(kf[2], wf[0][i], zero));
    const f32x4 d3 = mfma(kf[4], wf[1][i], zero), d4 = mfma(kf[5], wf[1][i], zero);
    const unsigned so = OFF_MP + (((2 * a + i / 5) * 20 + (hp >> 2) * 5) * 5 + i % 5) * 1024;
    st16(rws, vo + g * 5120, so, u32x4{pk2(lo2(d1)), pk2(hi2(d1)), pk2(lo2(d2)), pk2(hi2(d2))});
    st16(rws, g == 0 ? vo + 4 * 5120 : OOB, so, u32x4{pk2(lo2(d3)), pk2(hi2(d3)), pk2(lo2(d4)), pk2(hi2(d4))});
  }
  if (a == 0 && lane < 10) {
    const f32x4 z = *(LAS f32x4*)(red + (lane >= 5 ? 800 : 400) + 4 * lane);
    st8(rws, lane * 8, OFF_Z + hp * 80, u32x2{pk2(lo2(z)), pk2(hi2(z))});
  }
  WAIT_V(0);
  unsigned last = 0;
  if (lane == 0) last = __hip_atomic_fetch_add(lf + LF_ST, 1u, RLX_WG);
  if (__builtin_amdgcn_readfirstlane(last) == 3u && lane < 8) set_flag(ws, OFF_F3 + lane * 128 + hp * 8, tag);
  STAMP(13);
}

DEVI void p2_ffn(LAS char* lds, const char* x, char* ws, rsrc_t rws, char* out, u64 tag, int Mt, int mt2, int j, int w, int lane) {
  const int cl = lane & 15, g = lane >> 4;
  LAS unsigned* lf = (LAS unsigned*)(lds + L2_FLAG);
  const unsigned ab = OFF_H + mt2 * 54 * 4096, bb = OFF_WD + (j * 54 + 28 + w) * 5120;
  const unsigned last = w < 2 ? 0u : OOB;
  unsigned ao[4];
#pragma unroll
  for (int m = 0; m < 4; ++m) ao[m] = ((m + w) & 3) * 1024 + lane * 16;
  u32x4 fa[14][4], br[7][5], bl[2][5];
  auto ldA = [&](int i) {
    const unsigned so = i < 4 ? ab + (28 + w + 4 * i) * 4096 : i < 11 ? ab + (7 * w + i - 4) * 4096 : (ab + (w + 4 * i) * 4096) | (i == 13 ? last : 0u);
#pragma unroll
    for (int m = 0; m < 4; ++m) fa[i][m] = ld16(rws, ao[m], so);
  };
  auto ldB = [&](int r) {
    const unsigned so = (bb + r * 20480) | (r == 6 ? last : 0u);
#pragma unroll
    for (int t = 0; t < 4; ++t) br[r][t] = ld16(rws, lane * 16 + t * 1024, so);
    br[r][4] = ld16(rws, lane * 16, so + 4096);
  };
  auto rdL = [&](int i) {
#pragma unroll
    for (int t = 0; t < 5; ++t) bl[i & 1][t] = *(LAS u32x4*)(lds + w * L2_RG + ((i - 4) * 5 + t) * 1024 + lane * 16);
  };
#pragma unroll
  for (int r = 0; r < 4; ++r) ldB(r);
  SB();
  if (w == 0) { wait_flags(ws, OFF_F2 + (Mt * 16 + (cl < 9 ? cl : 0)) * 8, tag); lds_set(lf + LF_H, lane); }
  else lds_wait(lf + LF_H, 1u);
  STAMP(6);
#pragma unroll
  for (int i = 0; i < 4; ++i) ldA(i);
  f32x4 acc[4][5];
  const size_t orow = (size_t)(mt2 * 64 + w * 16 + cl) * 1280 + j * 160 + g * 8;
  u32x2 xr[5];
  SB();
#pragma unroll
  for (int i = 0; i < 14; ++i) {
    if (i >= 3 && i < 10) rdL(i + 1);
    SB();
    if (i < 13 || w < 2) {
#pragma unroll
      for (int m = 0; m < 4; ++m)
#pragma unroll
        for (int t = 0; t < 5; ++t) {
          if (i == 0) mfma_new(acc[m][t], br[0][t], fa[0][m]);
          else mfma_acc(acc[m][t], i < 4 ? br[i][t] : i < 11 ? bl[i & 1][t] : br[i - 7][t], fa[i][m]);
        }
    }
    SB();
    if (i < 2) { ldA(4 + 2 * i); ldA(5 + 2 * i); }
    else if (i < 6) ldA(i + 6);
    else if (i == 6) ldB(4);
    else if (i == 7) ldA(12);
    else if (i == 8) ldB(5);
    else if (i == 9) { ldA(13); ldB(6); }
    if (i == 10) {
      lds_set(lf + LF_FREE + w, lane);
#pragma unroll
      for (int t = 0; t < 5; ++t) xr[t] = *(const u32x2*)(x + orow + t * 32);
    }
    SB();
  }
  asm volatile("s_nop 7" ::: "memory");
  SB();
  STAMP(7);
  LAS char* rg = lds + w * L2_RG + lane * 16;
  lds_wait(lf + LF_PA + w, 1u);
#pragma unroll
  for (int m = 0; m < 4; ++m)
#pragma unroll
    for (int t = 0; t < 5; ++t) acc[m][t] += *(LAS f32x4*)(rg + (m * 5 + t) * 1024);
#pragma unroll
  for (int m = 1; m < 4; ++m)
#pragma unroll
    for (int t = 0; t < 5; ++t) *(LAS f32x4*)(rg + (m * 5 + t) * 1024) = acc[m][t];
  WAIT_L0(); BAR();
  STAMP(8);
#pragma unroll
  for (int t = 0; t < 5; ++t) {
    f32x4 s = acc[0][t];
#pragma unroll
    for (int q = 1; q < 4; ++q) s += *(LAS f32x4*)(lds + ((w - q) & 3) * L2_RG + (q * 5 + t) * 1024 + lane * 16);
    *(u32x2*)(out + orow + t * 32) = u32x2{pk2(lo2(s) + unpk(xr[t][0])), pk2(hi2(s) + unpk(xr[t][1]))};
  }
}

DEVI void p2_att(LAS char* lds, char* ws, rsrc_t rws, u64 tag, int Mt, int mt2, int j, int w, int lane) {
  const int g = lane >> 4, blk = w - 4;
  LAS unsigned* lf = (LAS unsigned*)(lds + L2_FLAG);
  __builtin_amdgcn_s_setprio(3);
  if (w == 4) { wait_flags(ws, OFF_F2 + (Mt * 16 + 13 + (lane & 1)) * 8, tag); lds_set(lf + LF_Q, lane); }
  else lds_wait(lf + LF_Q, 1u);
  STAMP(6);
  u32x4 pq[5][4];
#pragma unroll
  for (int s = 0; s < 5; ++s)
#pragma unroll
    for (int m = 0; m < 4; ++m) pq[s][m] = ld16(rws, ((m + blk) & 3) * 1024 + lane * 16, OFF_Q + (mt2 * 20 + blk * 5 + s) * 4096);
  SB();
  if (w == 4) { wait_flags(ws, OFF_F3 + (blockIdx.x & 7) * 128 + (lane & 15) * 8, tag); lds_set(lf + LF_M, lane); }
  else lds_wait(lf + LF_M, 1u);
  STAMP(7);
  u32x4 zf[5];
#pragma unroll
  for (int s = 0; s < 5; ++s) zf[s] = ld16(rws, g * 80 + s * 16, OFF_Z + blk * 320);
  SB();
  u32x4 mf[5][5];
  const unsigned mo = OFF_MP + (j * 20 + blk * 5) * 5120;
#pragma unroll
  for (int s = 0; s < 5; ++s)
#pragma unroll
    for (int t = 0; t < 5; ++t) mf[s][t] = ld16(rws, lane * 16 + (t & 3) * 1024, mo + s * 5120 + (t >> 2) * 4096);
  SB();
#pragma unroll
  for (int m = 0; m < 4; ++m) {
    float d0 = 0.f, d1 = 0.f;
#pragma unroll
    for (int u = 0; u < 10; ++u) {
      d0 = dot2(pq[u >> 2][m][u & 3], zf[u >> 2][u & 3], d0);
      d1 = dot2(pq[(u + 10) >> 2][m][(u + 10) & 3], zf[(u + 10) >> 2][(u + 10) & 3], d1);
    }
    const f32x2 i0 = bc2(1.0f / (d0 + 1e-6f)), i1 = bc2(1.0f / (d1 + 1e-6f));
#pragma unroll
    for (int u = 0; u < 20; ++u) pq[u >> 2][m][u & 3] = pk2(unpk(pq[u >> 2][m][u & 3]) * (u < 10 ? i0 : i1));
  }
  SB();
  STAMP(9);
  lds_wait(lf + LF_FREE + blk, 1u);
  LAS char* rg = lds + blk * L2_RG + lane * 16;
#pragma unroll
  for (int m = 0; m < 4; ++m) {
    f32x4 acc[5];
#pragma unroll
    for (int t = 0; t < 5; ++t) acc[t] = f32x4{0.f, 0.f, 0.f, 0.f};
#pragma unroll
    for (int s = 0; s < 5; ++s)
#pragma unroll
      for (int t = 0; t < 5; ++t) acc[t] = mfma(mf[s][t], pq[s][m], acc[t]);
#pragma unroll
    for (int t = 0; t < 5; ++t) *(LAS f32x4*)(rg + (m * 5 + t) * 1024) = acc[t];
  }
  WAIT_L0();
  lds_set(lf + LF_PA + blk, lane);
  STAMP(8);
  WAIT_L0(); BAR();
}

__global__ __launch_bounds__(512, 2) void fused(const char* __restrict__ x, const char* __restrict__ wqkv,
                                                const char* __restrict__ wo, const char* __restrict__ wg,
                                                const char* __restrict__ wu, const char* __restrict__ wd,
                                                char* ws, char* __restrict__ out) {
  extern __shared__ __attribute__((aligned(16))) char smem[];
  LAS char* lds = (LAS char*)smem;
  const unsigned ldsb = (unsigned)(size_t)lds;
  const int bid = blockIdx.x, xcd = bid & 7, idx = bid >> 3;
  const int Mt = (xcd & 1) * 8 + (idx & 7), cslot = (xcd >> 1) * 4 + (idx >> 3);
  const int w = __builtin_amdgcn_readfirstlane(threadIdx.x >> 6), lane = threadIdx.x & 63;
  STAMP(0);
  const rsrc_t rws = __builtin_amdgcn_make_buffer_rsrc(ws, 0, WS_END, 0x00020000);
  u64 tag;
  if (cslot == 15) {
    tag = make_tag(ws);
    PINS(tag);
    if (threadIdx.x < 16) ((LAS unsigned*)(lds + L2_FLAG))[threadIdx.x] = 0u;
    retile(wd, rws, ldsb + w * 16384, Mt, w, lane);
    u32x4 wf[2][10];
    if (w >= 4) {
      const int a = w - 4;
      const u32x4 ro = srd(wo, kD * 1280);
      WAIT_L0();
#pragma unroll 1
      for (int q = 0; q < 15; ++q)
        dma<0>(ro, (lane >> 3) * 1280 + (lane & 7) * 16, (Mt * 40 + (q / 3) * 8) * 1280 + ((a * 320) & ~127) + (q % 3) * 128, ldsb + w * 16384 + q * 1024);
      WAIT_V(0);
#pragma unroll
      for (int hh = 0; hh < 2; ++hh) {
        unsigned ra[2];
#pragma unroll
        for (int h2 = 0; h2 < 2; ++h2) {
          int row = hh * 20 + (lane >> 4) * 8 + h2 * 4 + ((lane & 15) >> 2);
          row = row < 40 ? row : 39;
          ra[h2] = ldsb + w * 16384 + (row >> 3) * 3072 + (row & 7) * 128 + (lane & 3) * 8;
        }
#pragma unroll
        for (int i = 0; i < 10; ++i) {
          const int cb = (a & 1) * 64 + i * 32, o = (cb >> 7) * 1024 + (cb & 127);
          const u32x2 lo = __builtin_bit_cast(u32x2, __builtin_amdgcn_ds_read_tr16_b64_v4i16((LAS s16x4*)(size_t)(ra[0] + o)));
          const u32x2 hi = __builtin_bit_cast(u32x2, __builtin_amdgcn_ds_read_tr16_b64_v4i16((LAS s16x4*)(size_t)(ra[1] + o)));
          wf[hh][i] = u32x4{lo[0], lo[1], hi[0], hi[1]};
        }
      }
    }
    WAIT_V(0); WAIT_L0(); BAR();
    if (threadIdx.x == 0) { set_flag(ws, OFF_F2 + (Mt * 16 + 15) * 8, tag); set_flag(ws, OFF_F1 + (Mt * 16 + 15) * 8, tag); }
    STAMP(5);
    wait_flags(ws, OFF_F2 + ((lane & 15) * 16 + 15) * 8, tag);
    wd_prefetch<0, 18>(srd(ws, WS_END), ldsb, 7, w, lane);
    WAIT_V(0); BAR();
    if (w >= 4) {
      if (Mt == 0 && w == 7) {
#pragma unroll 1
        for (int q = 0; q < 4; ++q) wait_flags(ws, OFF_F1 + (q * 64 + lane) * 8, tag);
        if (lane == 0) set_flag(ws, OFF_E, tag);
      }
      fold_kv(lds, ws, rws, tag, wf, Mt, w - 4, lane);
    }
  } else {
    const int type = cslot < 9 ? 0 : cslot < 13 ? 1 : 2;
    P1 c;
    c.w = w; c.mw = w >> 2; c.nw = w & 3; c.Mt = Mt; c.cslot = cslot; c.nt6 = 9 - cslot;
    c.ldsb = ldsb;
    {
      const int kb = (Mt + cslot) % 10, rot = kb >= 5 ? 5 : 0;
      c.bstar = kb - rot;
      c.bflag = w == 0 ? c.bstar + 2 : -1;
      const int seg = w >> 2;
      c.pitch = type == 0 ? kF * 2 : kQKV * 2;
      const int startb = type == 0 ? cslot * 384
                       : ((type == 1 ? (640 * (1 + seg) + 160 * (cslot - 9)) * 2 : 320 * (2 * (cslot - 13) + seg)) & ~127);
      const char* wb = type == 0 ? (seg ? wu : wg) : wqkv;
      c.rw = srd(wb + startb, 640 * c.pitch - startb);
      c.rx = srd(x + (size_t)Mt * 128 * 1280, 128 * 1280);
      const int i = lane >> 3, cp = lane & 7, pg = w & 3;
      c.voffB = (unsigned)(8 * pg + i) * c.pitch + 16 * (cp ^ ((i & 2) | ((pg & 1) << 2)));
      c.voffA = (unsigned)(8 * w + i) * 1280 + 16 * (cp ^ ((4 * w + (i >> 1)) & 7));
      c.oB = rot * 64 * c.pitch; c.oA = rot * 128;
      const unsigned lB = c.ldsb + L1_B + w * 3072, lA = c.ldsb + L1_A + w * 1024;
      dma<0>(c.rx, c.voffA, c.oA, lA);
#pragma unroll
      for (int s = 0; s < 3; ++s) {
        dma<0>(c.rw, c.voffB, c.oB, lB + s * L1_BST);
        dma<128>(c.rw, c.voffB, c.oB, lB + s * L1_BST + 1024);
        dma<256>(c.rw, c.voffB, c.oB, lB + s * L1_BST + 2048);
        c.oB += 32 * c.pitch;
        dma<0>(c.rx, c.voffA, c.oA + ((s + 1) >> 1) * 128 + ((s + 1) & 1) * 81920, lA + (s + 1) * 8192);
      }
      c.oA += 256;
    }
    SB();
    tag = make_tag(ws);
    PINS(tag);
    unsigned vb[6], va0;
    {
      const int g = lane >> 4, q = (lane & 15) >> 2, p = lane & 3, r = lane & 15, nw = c.nw;
      const int swz = (q & 2) | ((g & 1) << 2);
#pragma unroll
      for (int pos = 0; pos < 6; ++pos) {
        const int cf = 64 * pos + 16 * nw + 4 * p;
        const int hb = (cslot & 1 ? 0 : 32) + 40 * nw;
        const int ck = pos < 4 ? (pos >> 1) * 192 + hb + 20 * (pos & 1) + 4 * p : (p >> 1) * 192 + hb + 20 * (p & 1) + 16;
        const int tau = pos < 4 ? (nw & 1) * 6 + pos : 4 + (nw & 1);
        const int cq = (nw >> 1) * 224 + 40 * p + 4 * tau;
        const int col = type == 0 ? cf : type == 1 ? ck : cq;
        const int line = col >> 6, seg = line >= 3;
        vb[pos] = c.ldsb + L1_B + seg * (12288 - 3072) + (g * 3 + line) * 1024 + q * 128 + 16 * (((col & 63) >> 3) ^ swz) + 8 * ((col >> 2) & 1);
      }
      va0 = c.ldsb + L1_A + (c.mw * 64 + r) * 128 + 16 * (g ^ ((r >> 1) & 7));
    }
    if (threadIdx.x < 16) ((LAS unsigned*)(lds + L2_FLAG))[threadIdx.x] = 0u;
    f32x4 acc[4][6];
#pragma unroll
    for (int m = 0; m < 4; ++m)
#pragma unroll
      for (int i = 0; i < 6; ++i) acc[m][i] = f32x4{0.f, 0.f, 0.f, 0.f};
    u32x2 sv[10];
    if (type == 1) { if (w < 4) p1_loop<true, true>(c, vb, va0, acc, sv, ws, rws, tag, lane); else p1_loop<true, false>(c, vb, va0, acc, sv, ws, rws, tag, lane); }
    else { if (w < 4) p1_loop<false, true>(c, vb, va0, acc, sv, ws, rws, tag, lane); else p1_loop<false, false>(c, vb, va0, acc, sv, ws, rws, tag, lane); }
    STAMP(2);
    STAMP(3);
    if (type != 1) wd_prefetch<10, 18>(srd(ws, WS_END), ldsb, cslot & 7, w, lane);
    if (type == 0) epi_ffn(lds, acc, rws, c, lane);
    else if (type == 1) epi_kv(lds, acc, rws, c, lane);
    else epi_q(lds, acc, rws, c, lane);
    STAMP(4);
    WAIT_V(0); WAIT_L0(); BAR();
    if (threadIdx.x == 0) set_flag(ws, OFF_F2 + (Mt * 16 + cslot) * 8, tag);
    STAMP(5);
    if (type == 1) { wd_prefetch<10, 18>(srd(ws, WS_END), ldsb, cslot & 7, w, lane); WAIT_V(0); BAR(); }
  }
  const int mt2 = Mt * 2 + (cslot >> 3), j = cslot & 7;
  if (w < 4) p2_ffn(lds, x, ws, rws, out, tag, Mt, mt2, j, w, lane);
  else p2_att(lds, ws, rws, tag, Mt, mt2, j, w, lane);
  STAMP(10);
}

extern "C" void kernel_launch(void* const* d_in, const int* in_sizes, int n_in, void* d_out, int out_size,
                              void* d_ws, size_t ws_size, hipStream_t stream) {
  (void)in_sizes; (void)n_in; (void)out_size; (void)ws_size;
  (void)hipFuncSetAttribute((const void*)fused, hipFuncAttributeMaxDynamicSharedMemorySize, LDS_SIZE);
  fused<<<256, 512, LDS_SIZE, stream>>>((const char*)d_in[0], (const char*)d_in[1], (const char*)d_in[2], (const char*)d_in[4],
                                        (const char*)d_in[5], (const char*)d_in[6], (char*)d_ws, (char*)d_out);
}
```

```cpp
#include <hip/hip_runtime.h>
#include <stdint.h>
#include <type_traits>

#define GAS __attribute__((address_space(1)))
#define LAS __attribute__((address_space(3)))
typedef __attribute__((ext_vector_type(2))) __bf16 bf16x2;
typedef __attribute__((ext_vector_type(8))) __bf16 bf16x8;
typedef __attribute__((ext_vector_type(4))) short s16x4;
typedef __attribute__((ext_vector_type(4))) float f32x4;
typedef __attribute__((ext_vector_type(2))) float f32x2;
typedef __attribute__((ext_vector_type(4))) unsigned u32x4;
typedef __attribute__((ext_vector_type(2))) unsigned u32x2;
typedef unsigned long long u64;
typedef __amdgpu_buffer_rsrc_t rsrc_t;

#define DEVI __device__ __forceinline__
#define WAIT_V_(n) asm volatile("s_waitcnt vmcnt(" #n ")" ::: "memory")
#define WAIT_V(n) WAIT_V_(n)
#define WAIT_L0() do { __builtin_amdgcn_s_waitcnt(0xC07F); asm volatile("" ::: "memory"); } while (0)
#define SB() __builtin_amdgcn_sched_barrier(0)
#define PIN(x) asm volatile("" : "+v"(x))
#define PINS(x) asm volatile("" : "+s"(x))
#define BAR() do { asm volatile("" ::: "memory"); __builtin_amdgcn_s_barrier(); asm volatile("" ::: "memory"); } while (0)
#define RLX_AGENT __ATOMIC_RELAXED, __HIP_MEMORY_SCOPE_AGENT
#define RLX_WG __ATOMIC_RELAXED, __HIP_MEMORY_SCOPE_WORKGROUP
#define SC1 16

#define STAMP(i)

constexpr int kD = 640, kF = 1728, kQKV = 1920;
constexpr unsigned OFF_E   = 0;
constexpr unsigned OFF_F1  = 256;
constexpr unsigned OFF_FH  = OFF_F1 + 2048;
constexpr unsigned OFF_FQ  = OFF_FH + 2048;
constexpr unsigned OFF_FK  = OFF_FQ + 2048;
constexpr unsigned OFF_FR  = OFF_FK + 512;
constexpr unsigned OFF_F3  = OFF_FR + 128;
constexpr unsigned OFF_SC  = 8192;
constexpr unsigned OFF_SR  = OFF_SC + 15 * 10 * 384 * 4;
constexpr unsigned OFF_KVP = OFF_SR + 16 * 10 * 256 * 4;
constexpr unsigned OFF_MP  = OFF_KVP + 16 * 16 * 3456;
constexpr unsigned OFF_WD  = OFF_MP + 8 * 20 * 5 * 1024;
constexpr unsigned OFF_Q   = OFF_WD + 8 * 54 * 5 * 1024;
constexpr unsigned OFF_H   = OFF_Q + 32 * 20 * 4096;
constexpr unsigned WS_END  = OFF_H + 32 * 54 * 4096;
constexpr unsigned OOB = 0x80000000u;
static_assert(OFF_F3 + 1024 <= OFF_SC && OFF_MP % 1024 == 0 && OFF_KVP % 128 == 0, "align");

constexpr int L1_B = 0, L1_BST = 24576;
constexpr int L1_A = 4 * L1_BST, L1_AST = 16384;
constexpr int L1_ST = L1_A + 3 * L1_AST;
constexpr int L1_CS = L1_ST + 1024;
constexpr int L1_EX = 72 * 1024;
constexpr int L1_GC = L1_A + L1_AST;
constexpr int L2_RG = 35840;
constexpr int L2_RED = L1_ST;
constexpr int L1_GR = L1_CS + 1536;
constexpr int L2_FLAG = L1_GR + 10240;
constexpr int LDS_SIZE = L2_FLAG + 64;
enum { LF_RED = 0, LF_ST = 1, LF_H = 2, LF_Q = 3, LF_M = 4, LF_K = 5, LF_PF = 8 };

DEVI f32x4 mfma(u32x4 a, u32x4 b, f32x4 c) {
  return __builtin_amdgcn_mfma_f32_16x16x32_bf16(__builtin_bit_cast(bf16x8, a), __builtin_bit_cast(bf16x8, b), c, 0, 0, 0);
}
DEVI void mfma_acc(f32x4& c, u32x4 a, u32x4 b) { asm volatile("v_mfma_f32_16x16x32_bf16 %0, %1, %2, %0" : "+v"(c) : "v"(a), "v"(b)); }
DEVI void mfma_new(f32x4& c, u32x4 a, u32x4 b) { asm volatile("v_mfma_f32_16x16x32_bf16 %0, %1, %2, 0" : "=&v"(c) : "v"(a), "v"(b)); }
DEVI unsigned pk2(f32x2 f) { return __builtin_bit_cast(unsigned, __builtin_convertvector(f, bf16x2)); }
DEVI unsigned pk2(float lo, float hi) { return pk2(f32x2{lo, hi}); }
DEVI f32x2 unpk(unsigned u) { return f32x2{__builtin_bit_cast(float, u << 16), __builtin_bit_cast(float, u & 0xffff0000u)}; }
DEVI f32x2 fma2(f32x2 a, f32x2 b, f32x2 c) { return __builtin_elementwise_fma(a, b, c); }
DEVI f32x2 bc2(float v) { return f32x2{v, v}; }
DEVI f32x2 lo2(f32x4 v) { return f32x2{v[0], v[1]}; }
DEVI f32x2 hi2(f32x4 v) { return f32x2{v[2], v[3]}; }
DEVI f32x2 phi2(f32x2 g, f32x2 rs) {
  const f32x2 t = fma2(g, rs, bc2(1e-6f)), l = g * (rs * 1.4426950408889634f);
  return f32x2{fmaxf(t[0], 1e-6f), fmaxf(t[1], 1e-6f)} +
         f32x2{__builtin_amdgcn_fmed3f(__builtin_amdgcn_exp2f(l[0]), 0.f, 1.f), __builtin_amdgcn_fmed3f(__builtin_amdgcn_exp2f(l[1]), 0.f, 1.f)};
}
DEVI u32x4 lds_b128(unsigned a) { return *(LAS u32x4*)(size_t)a; }
DEVI u32x4 lds_trfrag(unsigned a) {
  const u32x2 lo = __builtin_bit_cast(u32x2, __builtin_amdgcn_ds_read_tr16_b64_v4i16((LAS s16x4*)(size_t)a));
  const u32x2 hi = __builtin_bit_cast(u32x2, __builtin_amdgcn_ds_read_tr16_b64_v4i16((LAS s16x4*)(size_t)(a + 512)));
  return u32x4{lo[0], lo[1], hi[0], hi[1]};
}
DEVI u32x4 srd(const void* p, unsigned bytes) {
  const u64 a = (u64)p;
  return u32x4{(unsigned)a, (unsigned)(a >> 32) & 0xffffu, bytes, 0x00020000u};
}
DEVI void setm0(unsigned v) { asm volatile("s_mov_b32 m0, %0" :: "s"(v) : "memory"); }
template <int IMM>
DEVI void dma_n(u32x4 r, unsigned voff, unsigned soff) {
  asm volatile("buffer_load_dwordx4 %0, %1, %2 offen offset:%3 lds" :: "v"(voff), "s"(r), "s"(soff), "i"(IMM) : "memory");
}
template <int IMM>
DEVI void dma(u32x4 r, unsigned voff, unsigned soff, unsigned lds) {
  asm volatile("s_mov_b32 m0, %4\n\ts_nop 0\n\tbuffer_load_dwordx4 %0, %1, %2 offen offset:%3 lds"
               :: "v"(voff), "s"(r), "s"(soff), "i"(IMM), "s"(lds - IMM) : "memory");
}
DEVI void dma_sc1(u32x4 r, unsigned voff, unsigned soff, unsigned lds) {
  asm volatile("s_mov_b32 m0, %3\n\ts_nop 0\n\tbuffer_load_dwordx4 %0, %1, %2 offen sc1 lds"
               :: "v"(voff), "s"(r), "s"(soff), "s"(lds) : "memory");
}
DEVI u32x2 ld8_hidden(u32x4 r, unsigned voff, unsigned soff) {
  u32x2 v;
  asm volatile("buffer_load_dwordx2 %0, %1, %2, %3 offen sc1" : "=v"(v) : "v"(voff), "s"(r), "s"(soff) : "memory");
  return v;
}
template <int N0, int N1>
DEVI void wd_prefetch(u32x4 rws, unsigned ldsb, int j, int w, int lane) {
#pragma unroll
  for (int n = N0; n < N1; ++n) {
    const unsigned a = (unsigned)(w + 8 * n) * 1024;
    if (n < 17 || w < 4) dma_sc1(rws, lane * 16, OFF_WD + j * 276480 + a, ldsb + a);
  }
}
DEVI void wait_flags(char* ws, unsigned off, u64 tag) {
#pragma unroll 1
  for (unsigned spins = 0; spins < (1u << 20); ++spins) {
    const u64 v = __hip_atomic_load((GAS u64*)(ws + off), RLX_AGENT);
    if (__all(v == tag)) break;
    __builtin_amdgcn_s_sleep(1);
  }
  asm volatile("" ::: "memory");
}
DEVI u64 make_tag(const char* ws) {
  return *(const __attribute__((address_space(4))) u64*)(ws + OFF_E) * 0x9E3779B97F4A7C15ull + 0xD1B54A32D192ED03ull;
}
DEVI void set_flag(char* ws, unsigned off, u64 tag) { __hip_atomic_store((GAS u64*)(ws + off), tag, RLX_AGENT); }
DEVI void lds_set(LAS unsigned* p, int lane) { if (lane == 0) __hip_atomic_store(p, 1u, RLX_WG); }
DEVI void lds_wait(LAS unsigned* p, unsigned want) {
  while ((unsigned)__builtin_amdgcn_readfirstlane(__hip_atomic_load(p, RLX_WG)) < want) __builtin_amdgcn_s_sleep(1);
  asm volatile("" ::: "memory");
}
DEVI u32x4 ld16(rsrc_t r, unsigned voff, unsigned soff) { return __builtin_amdgcn_raw_buffer_load_b128(r, voff, soff, SC1); }
DEVI void st16(rsrc_t r, unsigned voff, unsigned soff, u32x4 v) { __builtin_amdgcn_raw_buffer_store_b128(v, r, voff + soff, 0, SC1); }
DEVI void st8(rsrc_t r, unsigned voff, unsigned soff, u32x2 v) { __builtin_amdgcn_raw_buffer_store_b64(v, r, voff, soff, SC1); }

#define WARM 0
#define PRO_WAIT_(n) n
#define PRO_WAIT 8
constexpr unsigned CODE_BYTES = 56 * 1024;
DEVI void warm_up(const char* x, const char* wqkv, const char* wo, const char* wg, const char* wu, const char* wd,
                  const char* ws, const char* pc0, unsigned junk, int bid, int w, int lane) {
  const int cid = bid * 6 + w;
  const char* p = cid < 320 ? x : cid < 620 ? wqkv : cid < 890 ? wg : cid < 1160 ? wu : cid < 1430 ? wd : wo;
  unsigned o = (cid - (cid < 320 ? 0 : cid < 620 ? 320 : cid < 890 ? 620 : cid < 1160 ? 890 : cid < 1430 ? 1160 : 1430)) * 8192;
  unsigned n = (WARM & 1) && cid < 1530 ? o + 8192 : 0, sh = 7;
  if (w == 6) { p = ws; o = 0; n = (WARM & 2) ? WS_END : 0; sh = 18; }
  if (w == 7) { p = pc0; o = (bid >> 3) * 8192; n = (WARM & 4) ? CODE_BYTES : 0; }
  asm volatile("s_mov_b32 m0, %3\n\ts_nop 0\n\tbuffer_load_dword %0, %1, %2 offen lds"
               :: "v"(lane << sh), "s"(srd(p, n)), "s"(o), "s"(junk) : "memory");
}

struct P1 {
  u32x4 rw, rx;
  unsigned ldsb, pitch;
  unsigned voffB, voffA;
  unsigned oB, oA;
  int w, mw, nw, bstar, bflag;
  int Mt, cslot, j;
};

template <int TYPE, bool LATE>
DEVI void p1_loop(P1& c, unsigned (&vb)[5], unsigned va0, unsigned foff, f32x4 (&acc)[4][6], char* ws, rsrc_t rws, u64 tag, int lane) {
  constexpr int NT = TYPE == 0 ? 6 : 5;
  u32x4 fa[4], fb[NT];
  unsigned va1 = va0 ^ 64;
  const unsigned stepB = 32 * c.pitch, limB = 640 * c.pitch;
  const unsigned lB0 = c.ldsb + L1_B + c.w * 3072, lA0 = c.ldsb + L1_A + c.w * 1024;
  unsigned lB = lB0 + 3 * L1_BST, lA = lA0 + 2 * L1_AST;
  int dA = L1_AST, dB = 2 * L1_BST;
  f32x4 st[5];
  auto adB = [&](int i) { return TYPE == 0 ? vb[(LATE ? i >= 4 : i >= 2) ? 1 : 0] + (i / 3) * 12288 + (i % 3) * 1024 : vb[i < 5 ? i : 4]; };
  if (TYPE == 0) { vb[1] = vb[0]; PIN(vb[1]); }
  auto upB = [&](int i0, int i1) {
    if (TYPE == 0) { vb[i0 ? 1 : 0] += dB; PIN(vb[i0 ? 1 : 0]); }
    else {
#pragma unroll
      for (int i = i0; i < i1; ++i) { vb[i] += dB; PIN(vb[i]); }
    }
  };
  auto rdA = [&](int i, int sub) { fa[i] = lds_b128((sub ? va1 : va0) + i * 2048); };
  auto rdB = [&](int i, int sub) { fb[i] = lds_trfrag(adB(i) + sub * L1_BST); };
  auto rdBh = [&](int i, int hh, int sub) {
    const u32x2 v = __builtin_bit_cast(u32x2, __builtin_amdgcn_ds_read_tr16_b64_v4i16((LAS s16x4*)(size_t)(adB(i) + sub * L1_BST + hh * 512)));
    fb[i][2 * hh] = v[0]; fb[i][2 * hh + 1] = v[1];
  };
#define PRI 3
#define JIT 2
#define NCL 0
#define NCE 1
  auto m0of = [&](int n, int sub) { setm0(n == 3 ? lA + sub * 8192 : lB + n * (1024 - 128)); };
  auto piece = [&](int n, int sub) {
    if (n == 0) dma_n<0>(c.rw, c.voffB, c.oB);
    if (n == 1) dma_n<128>(c.rw, c.voffB, c.oB);
    if (n == 2) dma_n<256>(c.rw, c.voffB, c.oB);
    if (n == 3) dma_n<0>(c.rx, c.voffA, c.oA + sub * 81920);
  };
  auto L = [&](int sub, int n0, int n1) {
    int n = n0;
    if (PRI == 1) __builtin_amdgcn_s_setprio(1);
    auto gap = [&](auto rd) { if (n < n1) m0of(n, sub); SB(); rd(); SB(); if (n < n1) piece(n, sub); ++n; };
    gap([&]() { rdA(0, sub); rdA(1, sub); });
    gap([&]() { rdA(2, sub); rdA(3, sub); });
    gap([&]() { rdB(LATE ? 4 : 0, sub); });
    gap([&]() { if (!LATE) rdB(1, sub); else if (NT == 6) rdB(NT - 1, sub); else asm volatile("s_nop 0"); });
    if (!LATE && !sub) { upB(2, 5); dB = -dB; PINS(dB); }
    if (!LATE) {
#pragma unroll
      for (int i = 2; i < JIT && i < NT; ++i) rdB(i, sub);
    }
    SB();
  };
  auto U = [&](int sub) {
    if (sub) { va1 += dA; PIN(va1); if (LATE) { upB(0, 4); upB(4, 5); dB = -dB; PINS(dB); } else upB(0, 2); }
    else { dA = lA == lA0 + L1_AST ? -2 * L1_AST : L1_AST; PINS(dA); va0 += dA; PIN(va0); }
    SB();
  };
  auto stats = [&](int sub) {
    const u32x4 one = {0x3f803f80u, 0x3f803f80u, 0x3f803f80u, 0x3f803f80u};
    const u32x4 sa = lds_b128((sub ? va1 : va0) + c.nw * 2048);
    u32x4 sb[3];
#pragma unroll
    for (int i = 0; i < 3; ++i) { unsigned a = adB(LATE ? i : 3 + i); PIN(a); sb[i] = lds_trfrag(a + sub * L1_BST); }
    SB();
    if (sub == 0) {
#pragma unroll
      for (int i = 0; i < 5; ++i) st[i] = f32x4{0.f, 0.f, 0.f, 0.f};
    }
    st[0] = mfma(one, sa, st[0]);
    st[1] = mfma(sa, sa, st[1]);
#pragma unroll
    for (int i = 0; i < 3; ++i) st[2 + i] = mfma(one, sb[i], st[2 + i]);
    if (sub == 1) {
      const int cl = lane & 15, g = lane >> 4;
      const float sq = (cl & 2) ? ((cl & 1) ? st[1][3] : st[1][2]) : ((cl & 1) ? st[1][1] : st[1][0]);
      st8(rws, (cl >> 2) == g ? (c.mw * 64 + c.nw * 16 + cl) * 8 : OOB, c.cslot < 10 ? OFF_SR + (c.Mt * 10 + c.cslot) * 1024 : WS_END,
          u32x2{__builtin_bit_cast(unsigned, st[0][0]), __builtin_bit_cast(unsigned, sq)});
      const float v = g == 0 ? st[2][0] : g == 1 ? st[3][0] : st[4][0];
      __builtin_amdgcn_raw_buffer_store_b32(__builtin_bit_cast(unsigned, v), rws, g < 3 ? ((c.nw * 6 + c.mw * 3) * 16 + lane) * 4 : OOB,
                                            c.Mt < 10 ? OFF_SC + (c.cslot * 10 + c.Mt) * 1536 : WS_END, SC1);
    }
    SB();
  };
#define SEG(i)
  auto C = [&](int sub, bool more, bool book, auto vmwait) {
    SEG(0);
    if (PRI == 1) __builtin_amdgcn_s_setprio(0);
    if (PRI == 3) __builtin_amdgcn_s_setprio(1);
    if (LATE) { vmwait(); SEG(1); WAIT_L0(); BAR(); SEG(2); }
    SB();
#pragma unroll
    for (int k = 0; k < 4 * NT; ++k) {
      const int i = k >> 2, m = k & 3, e = 4 * NT - k;
      acc[m][i] = TYPE == 1 ? mfma(fa[m], fb[i], acc[m][i]) : mfma(fb[i], fa[m], acc[m][i]);
      if (LATE) { if (more && i >= 1 && i < 5 && m < 2) rdBh(i - 1, m, sub ^ 1); }
      else if (k < 2 * (NT - JIT)) rdBh(JIT + (k >> 1), k & 1, sub);
      if (book) {
        if (LATE) {
          if (NCL == 2) { if (e == 6) m0of(0, 0); if (e == 5) piece(0, 0); }
          if (NCL >= 1) { if (e == 3) m0of(NCL - 1, 0); if (e == 2) piece(NCL - 1, 0); }
        } else if (NCE) { if (e == 7) m0of(3, sub); if (e == 6) piece(3, sub); }
      }
      const int q = !book ? -1 : LATE ? (m >= 2 ? 2 * i + m - 2 : -1) : (e <= 4 ? 7 - e : k - 8 < 3 ? k - 8 : -1);
      if (q == 0) { c.oB += stepB; lB += L1_BST; PINS(c.oB); PINS(lB); }
      if (q == 1) { c.oB = c.oB == limB ? 0u : c.oB; PINS(c.oB); }
      if (q == 2) { lB = lB == lB0 + 4 * L1_BST ? lB0 : lB; PINS(lB); }
      if (sub) {
        if (q == 3) { c.oA += 128; lA += L1_AST; PINS(c.oA); PINS(lA); }
        if (q == 4) { c.oA = c.oA == 1280u ? 0u : c.oA; PINS(c.oA); }
        if (q == 5) { lA = lA == lA0 + 3 * L1_AST ? lA0 : lA; PINS(lA); }
      }
      SB();
    }
    SEG(3);
    if (PRI == 3) __builtin_amdgcn_s_setprio(0);
    if (!LATE) { vmwait(); SEG(1); WAIT_L0(); BAR(); SEG(2); }
  };
  constexpr int N0 = LATE ? NCL : 0, N1 = LATE ? 4 : 4 - NCE;
  if (!LATE) { upB(2, 5); dB = -dB; }
  if (PRI == 2 && !LATE) __builtin_amdgcn_s_setprio(1);
  WAIT_V(PRO_WAIT); BAR();
  if (LATE) {
    rdB(0, 0); rdB(1, 0); rdB(2, 0); rdB(3, 0);
#pragma unroll
    for (int n = 0; n < N0; ++n) { m0of(n, 0); asm volatile("s_nop 0"); piece(n, 0); }
  }
#pragma unroll 1
  for (int b = 0; b < 8; ++b) {
    L(0, N0, N1);
    if (__builtin_expect(b == c.bstar, 0)) stats(0);
    U(0);
    C(0, true, true, []() { WAIT_V(8); });
    L(1, N0, N1);
    if (__builtin_expect(b == c.bstar, 0)) stats(1);
    if (LATE) { if (__builtin_expect(b == c.bflag, 0)) { if (lane == 0) set_flag(ws, OFF_F1 + (c.Mt * 16 + c.cslot) * 8, tag); } }
    U(1);
    C(1, true, true, []() { WAIT_V(8); });
  }
  const u32x4 rw4 = srd(ws, WS_END);
  u32x2 fl = ld8_hidden(rw4, foff, 0);
  L(0, N0, 3);
  U(0);
  C(0, true, false, []() { WAIT_V(8); });
  L(1, 0, 0);
  if (N0 == 0) WAIT_V(3); else if (N0 == 1) WAIT_V(2); else WAIT_V(1);
  PIN(fl);
  if (__builtin_expect(!__all(__builtin_bit_cast(u64, fl) == tag), 0)) wait_flags(ws, foff, tag);
  dma_sc1(rw4, lane * 16, OFF_SC + c.cslot * 15360 + c.w * 1024, c.ldsb + L1_GC + c.w * 1024);
  dma_sc1(rw4, lane * 16, OFF_SC + c.cslot * 15360 + c.w * 1024 + 8192, c.ldsb + L1_GC + c.w * 1024 + 8192);
  dma_sc1(rw4, lane * 16, OFF_SR + c.Mt * 10240 + c.w * 1024, c.ldsb + L1_GR + c.w * 1024);
  if (LATE && c.w < 2) dma_sc1(rw4, lane * 16, OFF_SR + c.Mt * 10240 + c.w * 1024 + 8192, c.ldsb + L1_GR + c.w * 1024 + 8192);
  U(1);
  C(1, true, false, []() {});
  L(0, 0, 0);
  wd_prefetch<0, 4>(rw4, c.ldsb, c.j, c.w, lane);
  C(0, true, false, []() { WAIT_V(4); });
  L(1, 0, 0);
  {
    __builtin_amdgcn_s_setprio(2);
    if (LATE || c.w < 6) {
      const LAS float* src = (const LAS float*)(size_t)(c.ldsb + L1_GC + (c.w * 64 + lane) * 4);
      float v[10];
#pragma unroll
      for (int p = 0; p < 10; ++p) v[p] = src[p * 384];
      *(LAS float*)(size_t)(c.ldsb + L1_CS + (c.w * 64 + lane) * 4) = (((v[0] + v[1]) + (v[2] + v[3])) + ((v[4] + v[5]) + (v[6] + v[7]))) + (v[8] + v[9]);
    } else {
      const LAS f32x2* src = (const LAS f32x2*)(size_t)(c.ldsb + L1_GR + ((c.w - 6) * 64 + lane) * 8);
      f32x2 v[10];
#pragma unroll
      for (int p = 0; p < 10; ++p) v[p] = src[p * 128];
      const f32x2 s = ((((v[0] + v[1]) + (v[2] + v[3])) + ((v[4] + v[5]) + (v[6] + v[7]))) + (v[8] + v[9])) * (1.0f / kD);
      LAS float* d = (LAS float*)(size_t)(c.ldsb + L1_ST + ((c.w - 6) * 64 + lane) * 4);
      d[0] = s[0]; d[128] = __builtin_amdgcn_rsqf(fmaxf(__builtin_fmaf(-s[0], s[0], s[1]), 0.f) + 1e-5f);
    }
    __builtin_amdgcn_s_setprio(0);
  }
  SB();
  wd_prefetch<4, 6>(rw4, c.ldsb, c.j, c.w, lane);
  SB();
  C(1, false, false, []() {});
}

struct PF2 {
  u32x4 rw4; rsrc_t rws; unsigned ldsb; int j, w, lane;
  u32x4 br[4][5];
  template <int N> DEVI void item() {
    if constexpr (N < 12) wd_prefetch<6 + N, 7 + N>(rw4, ldsb, j, w, lane);
    else if constexpr (N < 27) {
      constexpr int r = (N - 12) / 5, t = (N - 12) % 5;
      br[r][t] = ld16(rws, lane * 16, OFF_WD + (j * 54 + 28 + w + 8 * r) * 5120 + t * 1024);
    } else if constexpr (N == 27) {
      if (w < 2) {
#pragma unroll
        for (int t = 0; t < 5; ++t) br[3][t] = ld16(rws, lane * 16, OFF_WD + (j * 54 + 52 + w) * 5120 + t * 1024);
      }
    }
    SB();
  }
  template <int N0, int N1> DEVI void items() { if constexpr (N0 < N1) { item<N0>(); items<N0 + 1, N1>(); } }
};
template <int N, class F> DEVI void static_for(F&& f) {
  if constexpr (N > 0) { static_for<N - 1>(f); f(std::integral_constant<int, N - 1>{}); }
}

DEVI void epi_ffn(LAS char* lds, f32x4 (&acc)[4][6], rsrc_t rws, const P1& c, PF2& pf, int lane) {
  const int r = lane & 15, g = lane >> 4;
  f32x4 cs[6];
#pragma unroll
  for (int i = 0; i < 6; ++i) cs[i] = *(LAS f32x4*)(lds + L1_CS + ((c.nw * 6 + i) * 16 + 4 * g) * 4);
  float mean[4], rstd[4];
#pragma unroll
  for (int mi = 0; mi < 4; ++mi) {
    const LAS float* sp = (const LAS float*)(lds + L1_ST) + c.mw * 64 + mi * 16 + r;
    mean[mi] = sp[0]; rstd[mi] = sp[128];
  }
  const unsigned hb = OFF_H + ((c.Mt * 2 + c.mw) * 54 + 6 * c.cslot) * 4096;
  unsigned keep[2];
  static_for<4>([&](auto MI) {
    constexpr int mi = MI;
    const float rs = rstd[mi], c0 = -2.0f * 0.7978845608028654f * 1.4426950408889634f;
    const f32x2 nm = bc2(-mean[mi]), k1 = bc2(c0 * rs), k2 = bc2(c0 * 0.044715f * rs * rs * rs), iv = bc2(__builtin_amdgcn_rcpf(rs * rs));
    unsigned o[6];
    static_for<3>([&](auto I) {
      constexpr int i = I;
#pragma unroll
      for (int hh = 0; hh < 2; ++hh) {
        const f32x2 G = fma2(nm, hh ? hi2(cs[i]) : lo2(cs[i]), hh ? hi2(acc[mi][i]) : lo2(acc[mi][i]));
        const f32x2 U = fma2(nm, hh ? hi2(cs[3 + i]) : lo2(cs[3 + i]), hh ? hi2(acc[mi][3 + i]) : lo2(acc[mi][3 + i]));
        const f32x2 z = G * fma2(G * G, k2, k1);
        const f32x2 d = fma2(f32x2{__builtin_amdgcn_exp2f(z[0]), __builtin_amdgcn_exp2f(z[1])}, iv, iv);
        o[2 * i + hh] = pk2(G * U * f32x2{__builtin_amdgcn_rcpf(d[0]), __builtin_amdgcn_rcpf(d[1])});
        PIN(o[2 * i + hh]);
      }
      SB();
      pf.template item<mi * 3 + i>();
    });
    st16(rws, lane * 16 + mi * 1024, hb + c.nw * 4096, u32x4{o[0], o[1], o[2], o[3]});
    if (mi & 1) {
      const auto s0 = __builtin_amdgcn_permlane32_swap(keep[0], o[4], false, false);
      const auto s1 = __builtin_amdgcn_permlane32_swap(keep[1], o[5], false, false);
      st16(rws, (lane & 31) * 16 + (lane >> 5) * 1024 + (mi - 1) * 1024, hb + (4 + (c.nw >> 1)) * 4096 + (c.nw & 1) * 512,
           u32x4{s0[0], s1[0], s0[1], s1[1]});
    } else { keep[0] = o[4]; keep[1] = o[5]; }
    SB();
  });
}

DEVI void epi_q(LAS char* lds, f32x4 (&acc)[4][6], char* ws, rsrc_t rws, u64 tag, const P1& c, PF2& pf, int lane) {
  const int r = lane & 15, g = lane >> 4, qi = c.cslot - 13;
  f32x4 cs[5];
#pragma unroll
  for (int i = 0; i < 5; ++i) cs[i] = *(LAS f32x4*)(lds + L1_CS + ((c.nw * 6 + i) * 16 + 4 * g) * 4);
  pf.items<0, 8>();
  static_for<4>([&](auto MI) {
    constexpr int mi = MI;
    const LAS float* sp = (const LAS float*)(lds + L1_ST) + c.mw * 64 + mi * 16 + r;
    const f32x2 nm = bc2(-sp[0]), rs = bc2(sp[128]);
    static_for<5>([&](auto I) {
      constexpr int i = I;
      const f32x2 a = phi2(fma2(nm, lo2(cs[i]), lo2(acc[mi][i])), rs), b = phi2(fma2(nm, hi2(cs[i]), hi2(acc[mi][i])), rs);
      acc[mi][i] = f32x4{a[0], a[1], b[0], b[1]};
      PIN(acc[mi][i]);
      SB();
      pf.template item<8 + mi * 5 + i>();
    });
  });
  STAMP(11);
  LAS unsigned* lf = (LAS unsigned*)(lds + L2_FLAG);
  if (c.w == 0) { wait_flags(ws, OFF_FK + qi * 256 + (lane & 31) * 8, tag); lds_set(lf + LF_K, lane); }
  else lds_wait(lf + LF_K, 1u);
  STAMP(12);
  {
    const int q4 = (c.w & 1) * 64 + lane, pr = q4 / 10;
    f32x4 part[4];
#pragma unroll
    for (int p = 0; p < 4; ++p)
      part[p] = __builtin_bit_cast(f32x4, ld16(rws, q4 < 80 ? pr * 55296 + (q4 - pr * 10) * 16 : OOB, OFF_KVP + 3200 + (qi * 128 + (c.w >> 1) * 4 + p) * 3456));
    SB();
    if (q4 < 80) *(LAS f32x4*)(lds + L1_GR + (c.w >> 1) * 1280 + q4 * 16) = (part[0] + part[1]) + (part[2] + part[3]);
    WAIT_L0(); BAR();
  }
  STAMP(13);
  f32x4 z[5];
#pragma unroll
  for (int i = 0; i < 5; ++i) {
    const LAS char* p = lds + L1_GR + (c.nw * 80 + 20 * (2 * (g & 1) + (g >> 1)) + 4 * i) * 4;
    z[i] = (*(const LAS f32x4*)p + *(const LAS f32x4*)(p + 1280)) + (*(const LAS f32x4*)(p + 2560) + *(const LAS f32x4*)(p + 3840));
  }
  const unsigned qb = OFF_Q + ((c.Mt * 2 + c.mw) * 20 + (2 * qi + (c.nw >> 1)) * 5) * 4096;
  unsigned keep[2];
#pragma unroll
  for (int mi = 0; mi < 4; ++mi) {
    f32x2 d0 = lo2(acc[mi][0]) * lo2(z[0]), d1 = hi2(acc[mi][0]) * hi2(z[0]);
#pragma unroll
    for (int i = 1; i < 5; ++i) { d0 = fma2(lo2(acc[mi][i]), lo2(z[i]), d0); d1 = fma2(hi2(acc[mi][i]), hi2(z[i]), d1); }
    d0 += d1;
    const f32x2 inv = bc2(__builtin_amdgcn_rcpf(d0[0] + d0[1] + 1e-6f));
    unsigned o[10];
#pragma unroll
    for (int i = 0; i < 5; ++i) { o[2 * i] = pk2(lo2(acc[mi][i]) * inv); o[2 * i + 1] = pk2(hi2(acc[mi][i]) * inv); }
    st16(rws, lane * 16 + mi * 1024, qb + (c.nw & 1) * 12288, u32x4{o[0], o[1], o[2], o[3]});
    st16(rws, lane * 16 + mi * 1024, qb + (c.nw & 1) * 12288 + 4096, u32x4{o[4], o[5], o[6], o[7]});
    if (mi & 1) {
      const auto s0 = __builtin_amdgcn_permlane32_swap(keep[0], o[8], false, false);
      const auto s1 = __builtin_amdgcn_permlane32_swap(keep[1], o[9], false, false);
      st16(rws, (lane & 31) * 16 + (lane >> 5) * 1024 + (mi - 1) * 1024, qb + 8192 + (c.nw & 1) * 512, u32x4{s0[0], s1[0], s0[1], s1[1]});
    } else { keep[0] = o[8]; keep[1] = o[9]; }
  }
}

DEVI void epi_kv(LAS char* lds, f32x4 (&acc)[4][6], rsrc_t rws, const P1& c, int lane) {
  const int cl = lane & 15, g = lane >> 4;
  float cs[5];
#pragma unroll
  for (int i = 0; i < 5; ++i) cs[i] = *(LAS float*)(lds + L1_CS + ((c.nw * 6 + i) * 16 + cl) * 4);
  u32x4 fr[2][6];
#pragma unroll
  for (int mi = 0; mi < 4; ++mi) {
    const f32x4 mean = *(LAS f32x4*)(lds + L1_ST + (c.mw * 64 + mi * 16 + 4 * g) * 4);
    const f32x4 rstd = *(LAS f32x4*)(lds + L1_ST + 512 + (c.mw * 64 + mi * 16 + 4 * g) * 4);
#pragma unroll
    for (int i = 0; i < 5; ++i) {
#pragma unroll
      for (int hh = 0; hh < 2; ++hh) {
        const f32x2 rs = hh ? hi2(rstd) : lo2(rstd);
        const f32x2 t = fma2(hh ? hi2(mean) : lo2(mean), bc2(-cs[i]), hh ? hi2(acc[mi][i]) : lo2(acc[mi][i]));
        fr[mi >> 1][i][(mi & 1) * 2 + hh] = pk2((i == 2 || i == 3) ? t * rs : phi2(t, rs));
        if (i == 4) fr[mi >> 1][5][(mi & 1) * 2 + hh] = cl < 8 ? 0x3f803f80u : pk2(t * rs);
      }
    }
  }
  constexpr int KI[7] = {0, 0, 4, 1, 1, 4, 4}, VI[7] = {2, 5, 2, 3, 5, 3, 5};
  f32x4 p[7];
#pragma unroll
  for (int q = 0; q < 7; ++q) {
    p[q] = mfma(fr[0][VI[q]], fr[0][KI[q]], f32x4{0.f, 0.f, 0.f, 0.f});
    p[q] = mfma(fr[1][VI[q]], fr[1][KI[q]], p[q]);
  }
  LAS char* ex = lds + L1_EX + c.w * 3584;
  const int uh = cl < 4 ? 16 + cl : 32 + cl;
  auto put = [&](bool ok, int u, int e, f32x4 v) { *(LAS f32x4*)(ex + (ok ? (u * 20 + e) * 4 : 3360)) = v; };
  auto putz = [&](bool ok, int u, float v) { *(LAS float*)(ex + (ok ? 3200 + u * 4 : 3360)) = v; };
  put(true, cl, 4 * g, p[0]);
  put(true, 20 + cl, 4 * g, p[3]);
  put(g >= 2, (g & 1) * 20 + cl, 16, g == 2 ? p[1] : p[4]);
  put(cl < 8, uh, 4 * g, cl < 4 ? p[2] : p[5]);
  put(cl < 8 && g == 2 + (cl >> 2), uh, 16, p[6]);
  putz(g < 2, g * 20 + cl, g == 0 ? p[1][0] : p[4][0]);
  putz(g == 0 && cl < 8, uh, p[6][0]);
  WAIT_L0(); BAR();
#pragma unroll
  for (int q = 0; q < 2; ++q) {
    const int o = (c.mw * 128 + q * 64 + lane) * 16;
    const LAS char* src = lds + L1_EX + c.nw * 3584 + (o < 3360 ? o : 0);
    const f32x4 v = *(const LAS f32x4*)src + *(const LAS f32x4*)(src + 4 * 3584);
    st16(rws, o < 3360 ? o : OOB, OFF_KVP + (((c.cslot - 9) * 4 + c.nw) * 16 + c.Mt) * 3456, __builtin_bit_cast(u32x4, v));
  }
}

DEVI void retile(const char* wd, rsrc_t rws, unsigned ldsw, int id, int w, int lane) {
  const u32x4 rd = srd(wd, kF * 1280);
  const int jj = lane >> 3, g = lane >> 4;
  const unsigned vlo = ((jj >> 2) * 64 + (jj & 3)) * 1280 + (lane & 7) * 16, vhi = ((jj >> 2) * 8 + (jj & 3)) * 1280 + (lane & 7) * 16;
  const unsigned ra = ldsw + g * 1024 + ((lane & 15) >> 2) * 128 + (lane & 3) * 8;
  auto fetch = [&](int n) {
    const int it = id * 8 + w + n * 128, ks = it / 10, ln = it - ks * 10, cg = ks / 6, uu = ks - cg * 6;
#pragma unroll
    for (int q = 0; q < 4; ++q) {
      const int row = cg * 192 + (uu < 4 ? 16 * uu + 4 * q : 32 * uu + 16 * (q >> 1) + 4 * (q & 1));
      dma<0>(rd, uu < 4 ? vlo : vhi, it < 540 ? row * 1280 + ln * 128 : OOB, ldsw + (n & 3) * 4096 + q * 1024);
    }
  };
  auto put = [&](int n) {
    const int it = id * 8 + w + n * 128, ks = it / 10, ln = it - ks * 10;
#pragma unroll
    for (int q = 0; q < 4; ++q) {
      const int nt = ln * 4 + q, j = nt / 5, t = nt - j * 5;
      st16(rws, lane * 16, it < 540 ? OFF_WD + ((j * 54 + ks) * 5 + t) * 1024 : OOB, lds_trfrag(ra + (n & 3) * 4096 + q * 32));
    }
  };
#pragma unroll 1
  for (int n = 0; n < 4; ++n) fetch(n);
  WAIT_V(0);
  put(0);
  WAIT_L0();
  fetch(4);
#pragma unroll 1
  for (int n = 1; n < 5; ++n) {
    if (n == 4) WAIT_V(0);
    put(n);
  }
}

DEVI void fold_kv(LAS char* lds, char* ws, rsrc_t rws, u64 tag, const u32x4 (&wf)[2][10], int hp, int a, int lane) {
  const int cl = lane & 15, g = lane >> 4;
  LAS unsigned* lf = (LAS unsigned*)(lds + L2_FLAG);
  __builtin_amdgcn_s_setprio(3);
  if (a == 0) { wait_flags(ws, OFF_FK + (hp >> 2) * 128 + cl * 8, tag); lds_set(lf + LF_K, lane); }
  else lds_wait(lf + LF_K, 1u);
  STAMP(11);
  {
    const int o = (a * 64 + lane) * 16;
    f32x4 part[16];
#pragma unroll
    for (int p = 0; p < 16; ++p) part[p] = __builtin_bit_cast(f32x4, ld16(rws, o < 3200 ? o : OOB, OFF_KVP + (hp * 16 + p) * 3456));
    SB();
#pragma unroll
    for (int d = 1; d < 16; d <<= 1)
#pragma unroll
      for (int p = 0; p < 16; p += 2 * d) part[p] += part[p + d];
    *(LAS f32x4*)(lds + L2_RED + o) = part[0];
    WAIT_L0();
    if (lane == 0) __hip_atomic_fetch_add(lf + LF_RED, 1u, RLX_WG);
    lds_wait(lf + LF_RED, 4u);
  }
  STAMP(12);
  u32x4 kf[6];
#pragma unroll
  for (int f = 0; f < 6; ++f) {
    const int u = 20 * (f & 1) + (f < 4 ? (cl & 4) * 2 + 4 * (f >> 1) : 16) + (cl & 3);
    const bool mine = f >= 4 || (cl >> 3) == (f & 1);
    const int o = u * 80 + g * 32;
    const f32x4 lo = *(LAS f32x4*)(lds + L2_RED + (mine && g < 3 ? o : 3200)), hi = *(LAS f32x4*)(lds + L2_RED + (mine && g < 2 ? o + 16 : 3200));
    kf[f] = u32x4{pk2(lo2(lo)), pk2(hi2(lo)), pk2(lo2(hi)), pk2(hi2(hi))};
  }
  const f32x4 zero = {0.f, 0.f, 0.f, 0.f};
  const unsigned vo = (3 * ((hp >> 1) & 1) + (g & 1)) * 5120 + (((hp & 1) + (g & 2)) * 16 + cl) * 16;
  const unsigned vo2 = g == 0 ? 2 * 5120 + ((hp & 3) * 16 + cl) * 16 : OOB;
#pragma unroll
  for (int i = 0; i < 10; ++i) {
    const f32x4 d1 = mfma(kf[1], wf[1][i], mfma(kf[0], wf[0][i], zero)), d2 = mfma(kf[3], wf[1][i], mfma(kf[2], wf[0][i], zero));
    const f32x4 d3 = mfma(kf[4], wf[0][i], zero), d4 = mfma(kf[5], wf[1][i], zero);
    const unsigned so = OFF_MP + (((2 * a + i / 5) * 20 + (hp >> 2) * 5) * 5 + i % 5) * 1024;
    st16(rws, vo, so, u32x4{pk2(lo2(d1)), pk2(hi2(d1)), pk2(lo2(d2)), pk2(hi2(d2))});
    st16(rws, vo2, so, u32x4{pk2(lo2(d3)), pk2(hi2(d3)), pk2(lo2(d4)), pk2(hi2(d4))});
  }
  WAIT_V(0);
  unsigned last = 0;
  if (lane == 0) last = __hip_atomic_fetch_add(lf + LF_ST, 1u, RLX_WG);
  if (__builtin_amdgcn_readfirstlane(last) == 3u && lane < 8) set_flag(ws, OFF_F3 + lane * 128 + hp * 8, tag);
  STAMP(13);
}

DEVI void p2_ffn(LAS char* lds, char* ws, rsrc_t rws, u64 tag, const u32x4 (&br)[4][5], int Mt, int mt2, int w, int lane) {
  LAS unsigned* lf = (LAS unsigned*)(lds + L2_FLAG);
  const unsigned ab = OFF_H + mt2 * 54 * 4096;
  u32x4 fa[11][4], bl[2][5];
  auto ldA = [&](int i) {
    const unsigned so = i == 1 ? (w < 2 ? ab + (52 + w) * 4096 : OOB) : ab + (i == 0 ? 28 + w : i < 4 ? 20 + w + 8 * i : 7 * w + i - 4) * 4096;
#pragma unroll
    for (int m = 0; m < 4; ++m) fa[i][m] = ld16(rws, lane * 16, so + ((m + w) & 3) * 1024);
    SB();
  };
  LAS char* rg = lds + w * L2_RG + lane * 16;
  if (w == 0) { wait_flags(ws, OFF_FH + Mt * 128 + ((lane & 15) < 9 ? (lane & 15) * 8 : 0), tag); lds_set(lf + LF_H, lane); }
  else lds_wait(lf + LF_H, 1u);
  STAMP(6);
#pragma unroll
  for (int i = 0; i < 5; ++i) ldA(i);
  f32x4 acc[4][5];
#pragma unroll
  for (int i = 0; i < 11; ++i) {
    if (i >= 3 && i < 10) {
#pragma unroll
      for (int t = 0; t < 5; ++t) bl[(i + 1) & 1][t] = *(LAS u32x4*)(rg + ((i - 3) * 5 + t) * 1024);
    }
    SB();
    if (i != 1 || w < 2) {
#pragma unroll
      for (int m = 0; m < 4; ++m)
#pragma unroll
        for (int t = 0; t < 5; ++t) {
          if (i == 0) mfma_new(acc[m][t], br[0][t], fa[0][m]);
          else mfma_acc(acc[m][t], i == 1 ? br[3][t] : i < 4 ? br[i - 1][t] : bl[i & 1][t], fa[i][m]);
        }
    }
    SB();
    if (i < 2) { ldA(5 + 2 * i); ldA(6 + 2 * i); }
    else if (i < 4) ldA(i + 7);
  }
  asm volatile("s_nop 7" ::: "memory");
  SB();
  STAMP(7);
#pragma unroll
  for (int m = 0; m < 4; ++m)
#pragma unroll
    for (int t = 0; t < 5; ++t) *(LAS f32x4*)(rg + (m * 5 + t) * 1024) = acc[m][t];
  WAIT_L0();
  lds_set(lf + LF_PF + w, lane);
}

DEVI void p2_att(LAS char* lds, const char* x, char* ws, rsrc_t rws, char* out, u64 tag, const u32x4 (&br)[4][5],
                 int Mt, int mt2, int j, int w, int lane) {
  const int a = w - 4;
  LAS unsigned* lf = (LAS unsigned*)(lds + L2_FLAG);
  LAS char* rg = lds + a * L2_RG + lane * 16;
  f32x4 acc[4][5];
  __builtin_amdgcn_s_setprio(3);
  lds_wait(lf + LF_H, 1u);
  STAMP(6);
  {
    u32x4 fa[3][4];
#pragma unroll
    for (int i = 0; i < 3; ++i) {
#pragma unroll
      for (int m = 0; m < 4; ++m) fa[i][m] = ld16(rws, lane * 16, OFF_H + (mt2 * 54 + 28 + w + 8 * i) * 4096 + ((m + w) & 3) * 1024);
      SB();
    }
#pragma unroll
    for (int i = 0; i < 3; ++i)
#pragma unroll
      for (int m = 0; m < 4; ++m)
#pragma unroll
        for (int t = 0; t < 5; ++t) {
          if (i == 0) mfma_new(acc[m][t], br[0][t], fa[0][m]);
          else mfma_acc(acc[m][t], br[i][t], fa[i][m]);
        }
    SB();
  }
  if (w == 4) { wait_flags(ws, OFF_FQ + Mt * 128 + (lane & 1) * 8, tag); lds_set(lf + LF_Q, lane); }
  else lds_wait(lf + LF_Q, 1u);
  STAMP(9);
  u32x4 pq[5][4];
#pragma unroll
  for (int s = 0; s < 5; ++s) {
#pragma unroll
    for (int m = 0; m < 4; ++m) pq[s][m] = ld16(rws, lane * 16, OFF_Q + (mt2 * 20 + a * 5 + s) * 4096 + ((m + w) & 3) * 1024);
    SB();
  }
  int absorbed = 0;
  auto absorb = [&]() {
#pragma unroll
    for (int m = 0; m < 4; ++m) {
#pragma unroll
      for (int t = 0; t < 5; ++t) acc[m][t] += *(LAS f32x4*)(rg + (m * 5 + t) * 1024);
      SB();
    }
    absorbed = 1;
  };
  auto peek = [&](int i) { return __builtin_amdgcn_readfirstlane(__hip_atomic_load(lf + i, RLX_WG)); };
#pragma unroll 1
  for (unsigned spins = 0; spins < (1u << 20); ++spins) {
    if (w == 4) { if (__all(__hip_atomic_load((GAS u64*)(ws + OFF_F3 + (blockIdx.x & 7) * 128 + (lane & 15) * 8), RLX_AGENT) == tag)) break; }
    else if (peek(LF_M)) break;
    if (!absorbed && peek(LF_PF + a)) { asm volatile("" ::: "memory"); absorb(); }
    else __builtin_amdgcn_s_sleep(1);
  }
  asm volatile("" ::: "memory");
  if (w == 4) lds_set(lf + LF_M, lane);
  STAMP(7);
  u32x4 mf[5][5];
  const unsigned mo = OFF_MP + (j * 20 + a * 5) * 5120;
  auto ldM = [&](int s) {
#pragma unroll
    for (int t = 0; t < 5; ++t) mf[s][t] = ld16(rws, lane * 16, mo + s * 5120 + t * 1024);
    SB();
  };
#pragma unroll
  for (int s = 0; s < 4; ++s) ldM(s);
  unsigned orow;
  u32x2 xr[5];
#pragma unroll
  for (int s = 0; s < 5; ++s) {
#pragma unroll
    for (int t = 0; t < 5; ++t)
#pragma unroll
      for (int m = 0; m < 4; ++m) mfma_acc(acc[m][t], mf[s][t], pq[s][m]);
    SB();
    if (s == 0) {
      ldM(4);
      int l2 = lane;
      PIN(l2);
      orow = (mt2 * 64 + a * 16 + (l2 & 15)) * 1280 + j * 160 + (l2 >> 4) * 8;
#pragma unroll
      for (int t = 0; t < 5; ++t) xr[t] = *(const GAS u32x2*)(x + orow + t * 32);
      SB();
    }
  }
  asm volatile("s_nop 7" ::: "memory");
  SB();
  STAMP(14);
  if (!absorbed) { lds_wait(lf + LF_PF + a, 1u); absorb(); }
#pragma unroll
  for (int m = 1; m < 4; ++m)
#pragma unroll
    for (int t = 0; t < 5; ++t) *(LAS f32x4*)(rg + (m * 5 + t) * 1024) = acc[m][t];
  WAIT_L0(); BAR();
  STAMP(8);
#pragma unroll
  for (int t = 0; t < 5; ++t) {
    f32x4 s = acc[0][t];
#pragma unroll
    for (int q = 1; q < 4; ++q) s += *(LAS f32x4*)(lds + ((a - q) & 3) * L2_RG + (q * 5 + t) * 1024 + lane * 16);
    *(GAS u32x2*)(out + orow + t * 32) = u32x2{pk2(lo2(s) + unpk(xr[t][0])), pk2(hi2(s) + unpk(xr[t][1]))};
  }
}

__global__ __launch_bounds__(512, 2) void fused(const char* x, const char* wqkv, const char* wo, const char* wg,
                                                const char* wu, const char* wd, char* ws, char* out) {
  extern __shared__ __attribute__((aligned(16))) char smem[];
  const char* pc0;
  asm volatile("s_getpc_b64 %0" : "=s"(pc0));
  asm volatile("" : "+s"(x), "+s"(wqkv), "+s"(wo), "+s"(wg), "+s"(wu), "+s"(wd), "+s"(ws), "+s"(out));
  LAS char* lds = (LAS char*)smem;
  const unsigned ldsb = (unsigned)(size_t)lds;
  const int bid = blockIdx.x, xcd = bid & 7, idx = bid >> 3;
  const int Mt = (xcd & 1) * 8 + (idx & 7), grp = xcd >> 1, pos4 = idx >> 3, slot2 = grp * 4 + pos4;
  const int cslot = (int)(0xCF76BE54AD329810ull >> (4 * slot2)) & 15;
  const int w = __builtin_amdgcn_readfirstlane(threadIdx.x >> 6), lane = threadIdx.x & 63;
  STAMP(0);
  const rsrc_t rws = __builtin_amdgcn_make_buffer_rsrc(ws, 0, WS_END, 0x00020000);
  u64 tag;
  PF2 pf;
  pf.rw4 = srd(ws, WS_END); pf.rws = rws; pf.ldsb = ldsb; pf.j = slot2 & 7; pf.w = w; pf.lane = lane;
  if (cslot == 15) {
    tag = make_tag(ws);
    PINS(tag);
    if (threadIdx.x < 16) ((LAS unsigned*)(lds + L2_FLAG))[threadIdx.x] = 0u;
    if (WARM) warm_up(x, wqkv, wo, wg, wu, wd, ws, pc0, ldsb + L1_GR + 8192, bid, w, lane);
    retile(wd, rws, ldsb + w * 16384, Mt, w, lane);
    u32x4 wf[2][10];
    if (w >= 4) {
      const int a = w - 4;
      const u32x4 ro = srd(wo, kD * 1280);
      WAIT_L0();
#pragma unroll 1
      for (int q = 0; q < 15; ++q)
        dma<0>(ro, (lane >> 3) * 1280 + (lane & 7) * 16, (Mt * 40 + (q / 3) * 8) * 1280 + ((a * 320) & ~127) + (q % 3) * 128, ldsb + w * 16384 + q * 1024);
      WAIT_V(0);
#pragma unroll
      for (int hh = 0; hh < 2; ++hh) {
        unsigned ra[2];
#pragma unroll
        for (int h2 = 0; h2 < 2; ++h2) {
          int row = hh * 20 + (lane >> 4) * 8 + h2 * 4 + ((lane & 15) >> 2);
          row = row < 40 ? row : 39;
          ra[h2] = ldsb + w * 16384 + (row >> 3) * 3072 + (row & 7) * 128 + (lane & 3) * 8;
        }
#pragma unroll
        for (int i = 0; i < 10; ++i) {
          const int cb = (a & 1) * 64 + i * 32, o = (cb >> 7) * 1024 + (cb & 127);
          const u32x2 lo = __builtin_bit_cast(u32x2, __builtin_amdgcn_ds_read_tr16_b64_v4i16((LAS s16x4*)(size_t)(ra[0] + o)));
          const u32x2 hi = __builtin_bit_cast(u32x2, __builtin_amdgcn_ds_read_tr16_b64_v4i16((LAS s16x4*)(size_t)(ra[1] + o)));
          wf[hh][i] = u32x4{lo[0], lo[1], hi[0], hi[1]};
        }
      }
    }
    WAIT_V(0); WAIT_L0(); BAR();
    if (threadIdx.x == 0) { set_flag(ws, OFF_FR + Mt * 8, tag); set_flag(ws, OFF_F1 + (Mt * 16 + 15) * 8, tag); }
    STAMP(5);
    wait_flags(ws, OFF_FR + (lane & 15) * 8, tag);
    wd_prefetch<0, 18>(pf.rw4, ldsb, pf.j, w, lane);
    WAIT_V(0); BAR();
    if (w >= 4) {
      if (Mt == 0 && w == 7) {
#pragma unroll 1
        for (int q = 0; q < 4; ++q) wait_flags(ws, OFF_F1 + (q * 64 + lane) * 8, tag);
        if (lane == 0) set_flag(ws, OFF_E, tag);
      }
      fold_kv(lds, ws, rws, tag, wf, Mt, w - 4, lane);
    }
    pf.items<12, 28>();
  } else {
    const int type = cslot < 9 ? 0 : cslot < 13 ? 1 : 2;
    P1 c;
    c.w = w; c.mw = w >> 2; c.nw = w & 3; c.Mt = Mt; c.cslot = cslot; c.j = slot2 & 7;
    c.ldsb = ldsb;
    {
      const int kb = (Mt + cslot) % 10, rot = kb >= 5 ? 5 : 0;
      c.bstar = kb - rot;
      c.bflag = w == 0 ? c.bstar + 2 : -1;
      const int seg = w >> 2;
      c.pitch = type == 0 ? kF * 2 : kQKV * 2;
      const int startb = type == 0 ? cslot * 384
                       : ((type == 1 ? (640 * (1 + seg) + 160 * (cslot - 9)) * 2 : 320 * (2 * (cslot - 13) + seg)) & ~127);
      const char* wb = type == 0 ? (seg ? wu : wg) : wqkv;
      c.rw = srd(wb + startb, 640 * c.pitch - startb);
      c.rx = srd(x + (size_t)Mt * 128 * 1280, 128 * 1280);
      const int i = lane >> 3, cp = lane & 7, pg = w & 3;
      c.voffB = (unsigned)(8 * pg + i) * c.pitch + 16 * (cp ^ ((i & 2) | ((pg & 1) << 2)));
      c.voffA = (unsigned)(8 * w + i) * 1280 + 16 * (cp ^ ((4 * w + (i >> 1)) & 7));
      c.oB = rot * 64 * c.pitch; c.oA = rot * 128;
      const unsigned lB = c.ldsb + L1_B + w * 3072, lA = c.ldsb + L1_A + w * 1024;
      dma<0>(c.rx, c.voffA, c.oA, lA);
#pragma unroll
      for (int s = 0; s < 3; ++s) {
        dma<0>(c.rw, c.voffB, c.oB, lB + s * L1_BST);
        dma<128>(c.rw, c.voffB, c.oB, lB + s * L1_BST + 1024);
        dma<256>(c.rw, c.voffB, c.oB, lB + s * L1_BST + 2048);
        c.oB += 32 * c.pitch;
        dma<0>(c.rx, c.voffA, c.oA + ((s + 1) >> 1) * 128 + ((s + 1) & 1) * 81920, lA + (s + 1) * 8192);
      }
      c.oA += 256;
    }
    SB();
    if (WARM & 8) BAR();
    if (WARM) warm_up(x, wqkv, wo, wg, wu, wd, ws, pc0, ldsb + L1_GR + 8192, bid, w, lane);
    SB();
    tag = make_tag(ws);
    PINS(tag);
    unsigned vb[5], va0;
    {
      const int g = lane >> 4, q = (lane & 15) >> 2, p = lane & 3, r = lane & 15, nw = c.nw;
      const int swz = (q & 2) | ((g & 1) << 2);
#pragma unroll
      for (int pos = 0; pos < 5; ++pos) {
        const int cf = 16 * nw + 4 * p;
        const int hb = (cslot & 1 ? 0 : 32) + 40 * nw;
        const int ck = pos < 4 ? (pos >> 1) * 192 + hb + 20 * (pos & 1) + 4 * p : (p >> 1) * 192 + hb + 20 * (p & 1) + 16;
        const int cq = (nw >> 1) * 224 + 20 * (4 * (nw & 1) + 2 * (p & 1) + (p >> 1)) + 4 * pos;
        const int col = type == 0 ? cf : type == 1 ? ck : cq;
        const int line = col >> 6, seg = line >= 3;
        vb[pos] = c.ldsb + L1_B + seg * (12288 - 3072) + (g * 3 + line) * 1024 + q * 128 + 16 * (((col & 63) >> 3) ^ swz) + 8 * ((col >> 2) & 1);
      }
      va0 = c.ldsb + L1_A + (c.mw * 64 + r) * 128 + 16 * (g ^ ((r >> 1) & 7));
    }
    if (threadIdx.x < 16) ((LAS unsigned*)(lds + L2_FLAG))[threadIdx.x] = 0u;
    f32x4 acc[4][6];
#pragma unroll
    for (int m = 0; m < 4; ++m)
#pragma unroll
      for (int i = 0; i < 6; ++i) acc[m][i] = f32x4{0.f, 0.f, 0.f, 0.f};
    unsigned foff = lane >= 20 ? OFF_FR + (lane & 15) * 8 : OFF_F1 + (lane < 10 ? lane * 16 + cslot : Mt * 16 + lane - 10) * 8;
    PIN(foff);
#define P1_STAMP
#define P1_RUN(T) do { if (w < 4) p1_loop<T, true>(c, vb, va0, foff, acc, ws, rws, tag, lane); \
                       else p1_loop<T, false>(c, vb, va0, foff, acc, ws, rws, tag, lane); P1_STAMP; } while (0)
    unsigned fo;
    if (type == 0) { P1_RUN(0); epi_ffn(lds, acc, rws, c, pf, lane); fo = OFF_FH + Mt * 128 + cslot * 8; }
    else if (type == 1) { P1_RUN(1); epi_kv(lds, acc, rws, c, lane); fo = OFF_FK + (cslot - 9) * 128 + Mt * 8; }
    else { P1_RUN(2); epi_q(lds, acc, ws, rws, tag, c, pf, lane); fo = OFF_FQ + Mt * 128 + (cslot - 13) * 8; }
    STAMP(4);
    if (type == 0) { pf.items<12, 17>(); WAIT_V(5); } else WAIT_V(0);
    WAIT_L0(); BAR();
    if (threadIdx.x == 0) set_flag(ws, fo, tag);
    STAMP(5);
    if (type == 1) { pf.items<0, 17>(); WAIT_V(5); BAR(); }
    if (type != 2) pf.items<17, 28>();
  }
  const int mt2 = Mt * 2 + (slot2 >> 3);
  if (w < 4) {
    p2_ffn(lds, ws, rws, tag, pf.br, Mt, mt2, w, lane);
#pragma unroll
    for (int r = 0; r < 3; ++r)
#pragma unroll
      for (int t = 0; t < 5; ++t) asm volatile("" : "=v"(pf.br[r][t]));
  }
  int w2 = w;
  PINS(w2);
  if (w2 >= 4) p2_att(lds, x, ws, rws, out, tag, pf.br, Mt, mt2, slot2 & 7, w2, lane);
  STAMP(10);
}

extern "C" void kernel_launch(void* const* d_in, const int* in_sizes, int n_in, void* d_out, int out_size,
                              void* d_ws, size_t ws_size, hipStream_t stream) {
  (void)in_sizes; (void)n_in; (void)out_size; (void)ws_size;
  (void)hipFuncSetAttribute((const void*)fused, hipFuncAttributeMaxDynamicSharedMemorySize, LDS_SIZE);
  fused<<<256, 512, LDS_SIZE, stream>>>((const char*)d_in[0], (const char*)d_in[1], (const char*)d_in[2], (const char*)d_in[4],
                                        (const char*)d_in[5], (const char*)d_in[6], (char*)d_ws, (char*)d_out);
}
```

```cpp
#include <hip/hip_runtime.h>
#include <stdint.h>
#include <type_traits>

#define GAS __attribute__((address_space(1)))
#define LAS __attribute__((address_space(3)))
typedef __attribute__((ext_vector_type(2))) __bf16 bf16x2;
typedef __attribute__((ext_vector_type(8))) __bf16 bf16x8;
typedef __attribute__((ext_vector_type(4))) short s16x4;
typedef __attribute__((ext_vector_type(4))) float f32x4;
typedef __attribute__((ext_vector_type(2))) float f32x2;
typedef __attribute__((ext_vector_type(4))) unsigned u32x4;
typedef __attribute__((ext_vector_type(2))) unsigned u32x2;
typedef unsigned long long u64;
typedef __amdgpu_buffer_rsrc_t rsrc_t;

#define DEVI __device__ __forceinline__
#define WAIT_V_(n) asm volatile("s_waitcnt vmcnt(" #n ")" ::: "memory")
#define WAIT_V(n) WAIT_V_(n)
#define WAIT_L0() do { __builtin_amdgcn_s_waitcnt(0xC07F); asm volatile("" ::: "memory"); } while (0)
#define SB() __builtin_amdgcn_sched_barrier(0)
#define PIN(x) asm volatile("" : "+v"(x))
#define PINS(x) asm volatile("" : "+s"(x))
#define BAR() do { asm volatile("" ::: "memory"); __builtin_amdgcn_s_barrier(); asm volatile("" ::: "memory"); } while (0)
#define RLX_AGENT __ATOMIC_RELAXED, __HIP_MEMORY_SCOPE_AGENT
#define RLX_WG __ATOMIC_RELAXED, __HIP_MEMORY_SCOPE_WORKGROUP
#define SC1 16

#define STAMP(i)

constexpr int kD = 640, kF = 1728, kQKV = 1920;
constexpr unsigned OFF_E   = 0;
constexpr unsigned OFF_F1  = 256;
constexpr unsigned OFF_FR  = OFF_F1 + 2048;
constexpr unsigned OFF_FK  = 2560;
constexpr unsigned OFF_F3  = OFF_FK + 4096;
constexpr unsigned OFF_FH  = OFF_F3 + 1024;
constexpr unsigned OFF_SC  = 16384;
constexpr unsigned OFF_SR  = OFF_SC + 15 * 10 * 384 * 4;
constexpr unsigned OFF_KVP = OFF_SR + 16 * 10 * 256 * 4;
constexpr unsigned OFF_MP  = OFF_KVP + 16 * 16 * 3456;
constexpr unsigned OFF_WD  = OFF_MP + 8 * 20 * 5 * 1024;
constexpr unsigned OFF_Q   = OFF_WD + 8 * 54 * 5 * 1024;
constexpr unsigned OFF_H   = OFF_Q + 32 * 20 * 4096;
constexpr unsigned WS_END  = OFF_H + 32 * 54 * 4096;
constexpr unsigned OOB = 0x80000000u;
static_assert(OFF_FH + 8192 <= OFF_SC && OFF_MP % 1024 == 0 && OFF_KVP % 128 == 0, "align");

constexpr int L1_B = 0, L1_BST = 24576;
constexpr int L1_A = 4 * L1_BST, L1_AST = 16384;
constexpr int L1_GR = L1_A + 3 * L1_AST;
constexpr int L1_GC = L1_A + L1_AST;
constexpr int L1_ST = 158720;
constexpr int L1_CS = L1_ST + 1024;
constexpr int L1_EX = 72 * 1024;
constexpr int L2_PF = 0, L2_BASE = 60 * 1024, L2_PA = 80 * 1024;
constexpr int L2_RED = 150 * 1024;
constexpr int L2_FLAG = 162816;
constexpr int LDS_SIZE = L2_FLAG + 64;
enum { LF_STATUS = 0, LF_DMA = 1, LF_K = 2, LF_BC = 3, LF_X = 4, LF_PUB = 8 };
enum { S_H = 1, S_Q = 2, S_M = 4 };

DEVI f32x4 mfma(u32x4 a, u32x4 b, f32x4 c) {
  return __builtin_amdgcn_mfma_f32_16x16x32_bf16(__builtin_bit_cast(bf16x8, a), __builtin_bit_cast(bf16x8, b), c, 0, 0, 0);
}
DEVI void mfma_acc(f32x4& c, u32x4 a, u32x4 b) { asm volatile("v_mfma_f32_16x16x32_bf16 %0, %1, %2, %0" : "+v"(c) : "v"(a), "v"(b)); }
DEVI void mfma_new(f32x4& c, u32x4 a, u32x4 b) { asm volatile("v_mfma_f32_16x16x32_bf16 %0, %1, %2, 0" : "=&v"(c) : "v"(a), "v"(b)); }
DEVI unsigned pk2(f32x2 f) { return __builtin_bit_cast(unsigned, __builtin_convertvector(f, bf16x2)); }
DEVI unsigned pk2(float lo, float hi) { return pk2(f32x2{lo, hi}); }
DEVI f32x2 unpk(unsigned u) { return f32x2{__builtin_bit_cast(float, u << 16), __builtin_bit_cast(float, u & 0xffff0000u)}; }
DEVI f32x2 fma2(f32x2 a, f32x2 b, f32x2 c) { return __builtin_elementwise_fma(a, b, c); }
DEVI f32x2 bc2(float v) { return f32x2{v, v}; }
DEVI f32x2 lo2(f32x4 v) { return f32x2{v[0], v[1]}; }
DEVI f32x2 hi2(f32x4 v) { return f32x2{v[2], v[3]}; }
DEVI f32x2 phi2(f32x2 g, f32x2 rs) {
  const f32x2 t = fma2(g, rs, bc2(1e-6f)), l = g * (rs * 1.4426950408889634f);
  return f32x2{fmaxf(t[0], 1e-6f), fmaxf(t[1], 1e-6f)} +
         f32x2{__builtin_amdgcn_fmed3f(__builtin_amdgcn_exp2f(l[0]), 0.f, 1.f), __builtin_amdgcn_fmed3f(__builtin_amdgcn_exp2f(l[1]), 0.f, 1.f)};
}
DEVI u32x4 lds_b128(unsigned a) { return *(LAS u32x4*)(size_t)a; }
DEVI u32x4 lds_trfrag(unsigned a) {
  const u32x2 lo = __builtin_bit_cast(u32x2, __builtin_amdgcn_ds_read_tr16_b64_v4i16((LAS s16x4*)(size_t)a));
  const u32x2 hi = __builtin_bit_cast(u32x2, __builtin_amdgcn_ds_read_tr16_b64_v4i16((LAS s16x4*)(size_t)(a + 512)));
  return u32x4{lo[0], lo[1], hi[0], hi[1]};
}
DEVI u32x4 srd(const void* p, unsigned bytes) {
  const u64 a = (u64)p;
  return u32x4{(unsigned)a, (unsigned)(a >> 32) & 0xffffu, bytes, 0x00020000u};
}
DEVI void setm0(unsigned v) { asm volatile("s_mov_b32 m0, %0" :: "s"(v) : "memory"); }
template <int IMM>
DEVI void dma_n(u32x4 r, unsigned voff, unsigned soff) {
  asm volatile("buffer_load_dwordx4 %0, %1, %2 offen offset:%3 lds" :: "v"(voff), "s"(r), "s"(soff), "i"(IMM) : "memory");
}
template <int IMM>
DEVI void dma(u32x4 r, unsigned voff, unsigned soff, unsigned lds) {
  asm volatile("s_mov_b32 m0, %4\n\ts_nop 0\n\tbuffer_load_dwordx4 %0, %1, %2 offen offset:%3 lds"
               :: "v"(voff), "s"(r), "s"(soff), "i"(IMM), "s"(lds - IMM) : "memory");
}
DEVI void dma_sc1(u32x4 r, unsigned voff, unsigned soff, unsigned lds) {
  asm volatile("s_mov_b32 m0, %3\n\ts_nop 0\n\tbuffer_load_dwordx4 %0, %1, %2 offen sc1 lds"
               :: "v"(voff), "s"(r), "s"(soff), "s"(lds) : "memory");
}
DEVI u32x2 ld8_hidden(u32x4 r, unsigned voff, unsigned soff) {
  u32x2 v;
  asm volatile("buffer_load_dwordx2 %0, %1, %2, %3 offen sc1" : "=v"(v) : "v"(voff), "s"(r), "s"(soff) : "memory");
  return v;
}
template <int N0, int N1>
DEVI void wd_prefetch(u32x4 rws, unsigned ldsb, int j, int w, int lane) {
#pragma unroll
  for (int n = N0; n < N1; ++n) {
    const unsigned a = (unsigned)(w + 8 * n) * 1024;
    if (n < 18 || w < 6) dma_sc1(rws, lane * 16, OFF_WD + j * 276480 + a, ldsb + a);
  }
}
DEVI void wait_flags(char* ws, unsigned off, u64 tag) {
#pragma unroll 1
  for (unsigned spins = 0; spins < (1u << 20); ++spins) {
    const u64 v = __hip_atomic_load((GAS u64*)(ws + off), RLX_AGENT);
    if (__all(v == tag)) break;
    __builtin_amdgcn_s_sleep(1);
  }
  asm volatile("" ::: "memory");
}
DEVI u64 make_tag(const char* ws) {
  return *(const __attribute__((address_space(4))) u64*)(ws + OFF_E) * 0x9E3779B97F4A7C15ull + 0xD1B54A32D192ED03ull;
}
DEVI void set_flag(char* ws, unsigned off, u64 tag) { __hip_atomic_store((GAS u64*)(ws + off), tag, RLX_AGENT); }
DEVI unsigned lds_inc(LAS unsigned* p, int lane) {
  unsigned o = 0;
  if (lane == 0) o = __hip_atomic_fetch_add(p, 1u, RLX_WG);
  return __builtin_amdgcn_readfirstlane(o);
}
DEVI void lds_wait_bits(LAS unsigned* p, unsigned bits) {
  while ((~(unsigned)__builtin_amdgcn_readfirstlane(__hip_atomic_load(p, RLX_WG)) & bits) != 0u) __builtin_amdgcn_s_sleep(1);
  asm volatile("" ::: "memory");
}
DEVI void lds_wait(LAS unsigned* p, unsigned want) {
  while ((unsigned)__builtin_amdgcn_readfirstlane(__hip_atomic_load(p, RLX_WG)) < want) __builtin_amdgcn_s_sleep(1);
  asm volatile("" ::: "memory");
}
DEVI u32x4 ld16(rsrc_t r, unsigned voff, unsigned soff) { return __builtin_amdgcn_raw_buffer_load_b128(r, voff, soff, SC1); }
DEVI void st16(rsrc_t r, unsigned voff, unsigned soff, u32x4 v) { __builtin_amdgcn_raw_buffer_store_b128(v, r, voff + soff, 0, SC1); }
DEVI void st8(rsrc_t r, unsigned voff, unsigned soff, u32x2 v) { __builtin_amdgcn_raw_buffer_store_b64(v, r, voff, soff, SC1); }

#define WARM 0
#define PRO_WAIT_(n) n
#define PRO_WAIT 8
constexpr unsigned CODE_BYTES = 56 * 1024;
DEVI void warm_up(const char* x, const char* wqkv, const char* wo, const char* wg, const char* wu, const char* wd,
                  const char* ws, const char* pc0, unsigned junk, int bid, int w, int lane) {
  const int cid = bid * 6 + w;
  const char* p = cid < 320 ? x : cid < 620 ? wqkv : cid < 890 ? wg : cid < 1160 ? wu : cid < 1430 ? wd : wo;
  unsigned o = (cid - (cid < 320 ? 0 : cid < 620 ? 320 : cid < 890 ? 620 : cid < 1160 ? 890 : cid < 1430 ? 1160 : 1430)) * 8192;
  unsigned n = (WARM & 1) && cid < 1530 ? o + 8192 : 0, sh = 7;
  if (w == 6) { p = ws; o = 0; n = (WARM & 2) ? WS_END : 0; sh = 18; }
  if (w == 7) { p = pc0; o = (bid >> 3) * 8192; n = (WARM & 4) ? CODE_BYTES : 0; }
  asm volatile("s_mov_b32 m0, %3\n\ts_nop 0\n\tbuffer_load_dword %0, %1, %2 offen lds"
               :: "v"(lane << sh), "s"(srd(p, n)), "s"(o), "s"(junk) : "memory");
}

struct P1 {
  u32x4 rw, rx;
  unsigned ldsb, pitch;
  unsigned voffB, voffA;
  unsigned oB, oA;
  int w, mw, nw, bstar;
  int Mt, cslot, j;
};

#define ABL_RD 0
#define AINC 1
template <int TYPE, bool LATE>
DEVI void p1_loop(P1& c, unsigned (&vb)[5], unsigned va0, unsigned foff, f32x4 (&acc)[4][6], char* ws, rsrc_t rws, u64 tag, int lane) {
  constexpr int NT = TYPE == 0 ? 6 : 5;
  constexpr bool AC = !LATE || AINC;
  u32x4 fa[4], fb[NT];
  unsigned va1 = va0 ^ 64;
  const unsigned stepB = 32 * c.pitch, limB = 640 * c.pitch;
  const unsigned lB0 = c.ldsb + L1_B + c.w * 3072, lA0 = c.ldsb + L1_A + c.w * 1024;
  unsigned lB = lB0 + 3 * L1_BST, lA = lA0 + 2 * L1_AST;
  unsigned vB1 = c.voffB - 896, vB2 = c.voffB - 1792, vA1 = c.voffA + 81920;
  PIN(vB1); PIN(vB2); PIN(vA1);
  int dA = L1_AST, dB = 2 * L1_BST, rare = c.bstar, rare2 = 1;
  f32x4 st[5];
  auto adB = [&](int i) { return TYPE == 0 ? vb[!LATE && i >= 2 ? 1 : 0] + (i / 3) * 12288 + (i % 3) * 1024 : vb[i < 5 ? i : 4]; };
  if (TYPE == 0) { vb[1] = vb[0]; PIN(vb[1]); }
  auto upB = [&](int i0, int i1) {
    if (TYPE == 0) { vb[i0 ? 1 : 0] += dB; PIN(vb[i0 ? 1 : 0]); }
    else {
#pragma unroll
      for (int i = i0; i < i1; ++i) { vb[i] += dB; PIN(vb[i]); }
    }
  };
  auto rdA = [&](int i, int sub) { if (ABL_RD & 1) { PIN(fa[i]); return; } fa[i] = lds_b128((sub ? va1 : va0) + i * 2048); };
  auto rdB = [&](int i, int sub) { if (ABL_RD & 2) { PIN(fb[i]); return; } fb[i] = lds_trfrag(adB(i) + sub * L1_BST); };
  auto rdBh = [&](int i, int hh, int sub) {
    if (ABL_RD & 2) { PIN(fb[i]); return; }
    const u32x2 v = __builtin_bit_cast(u32x2, __builtin_amdgcn_ds_read_tr16_b64_v4i16((LAS s16x4*)(size_t)(adB(i) + sub * L1_BST + hh * 512)));
    fb[i][2 * hh] = v[0]; fb[i][2 * hh + 1] = v[1];
  };
  auto pieceA = [&](int sub) { dma_n<0>(c.rx, sub ? vA1 : c.voffA, c.oA); };
  auto L = [&](int sub, int mode) {
    if (mode) dma_n<0>(c.rw, c.voffB, c.oB);
    SB(); rdA(0, sub); rdA(1, sub); SB();
    if (mode) dma_n<1024>(c.rw, vB1, c.oB);
    SB(); rdA(2, sub); rdA(3, sub); SB();
    if (mode) dma_n<2048>(c.rw, vB2, c.oB);
    SB(); rdB(LATE ? 4 : 0, sub); SB();
    if (mode == 2) setm0(lA + sub * 8192);
    SB(); if (!LATE) rdB(1, sub); else if (NT == 6) rdB(NT - 1, sub); else if (mode == 2) asm volatile("s_nop 0"); SB();
    if (mode == 2) pieceA(sub);
    if (!LATE && !sub) upB(2, 5);
    SB();
  };
  auto U = [&](int sub) {
    if (sub) { va1 += dA; PIN(va1); upB(0, LATE ? 5 : 2); }
    else { va0 += dA; PIN(va0); }
    SB();
  };
  auto stats = [&](int sub) {
    const u32x4 one = {0x3f803f80u, 0x3f803f80u, 0x3f803f80u, 0x3f803f80u};
    const u32x4 sa = lds_b128((sub ? va1 : va0) + c.nw * 2048);
    u32x4 sb[3];
#pragma unroll
    for (int i = 0; i < 3; ++i) { unsigned a = adB(LATE ? i : 3 + i); PIN(a); sb[i] = lds_trfrag(a + sub * L1_BST); }
    SB();
    if (sub == 0) {
#pragma unroll
      for (int i = 0; i < 5; ++i) st[i] = f32x4{0.f, 0.f, 0.f, 0.f};
    }
    st[0] = mfma(one, sa, st[0]);
    st[1] = mfma(sa, sa, st[1]);
#pragma unroll
    for (int i = 0; i < 3; ++i) st[2 + i] = mfma(one, sb[i], st[2 + i]);
    if (sub == 1) {
      const int cl = lane & 15, g = lane >> 4;
      const float sq = (cl & 2) ? ((cl & 1) ? st[1][3] : st[1][2]) : ((cl & 1) ? st[1][1] : st[1][0]);
      st8(rws, (cl >> 2) == g ? (c.mw * 64 + c.nw * 16 + cl) * 8 : OOB, c.cslot < 10 ? OFF_SR + (c.Mt * 10 + c.cslot) * 1024 : WS_END,
          u32x2{__builtin_bit_cast(unsigned, st[0][0]), __builtin_bit_cast(unsigned, sq)});
      const float v = g == 0 ? st[2][0] : g == 1 ? st[3][0] : st[4][0];
      __builtin_amdgcn_raw_buffer_store_b32(__builtin_bit_cast(unsigned, v), rws, g < 3 ? ((c.nw * 6 + c.mw * 3) * 16 + lane) * 4 : OOB,
                                            c.Mt < 10 ? OFF_SC + (c.cslot * 10 + c.Mt) * 1536 : WS_END, SC1);
    }
    SB();
  };
#define SEG(i)
  auto C = [&](int sub, bool more, int book, auto vmwait) {
    SEG(0);
    if (LATE) { vmwait(); SEG(1); WAIT_L0(); BAR(); SEG(2); }
    __builtin_amdgcn_s_setprio(1);
    SB();
#pragma unroll
    for (int k = 0; k < 4 * NT; ++k) {
      const int i = k >> 2, m = k & 3;
      acc[m][i] = TYPE == 1 ? mfma(fa[m], fb[i], acc[m][i]) : mfma(fb[i], fa[m], acc[m][i]);
      if (LATE) { if (more && i >= 1 && i < 5 && m < 2) rdBh(i - 1, m, sub ^ 1); }
      else if (k < 2 * (NT - 2)) rdBh(2 + (k >> 1), k & 1, sub);
      const int q = LATE ? (k < 2 ? k : m >= 2 && i < 5 ? 2 * i + m : -1) : k - 2 * (NT - 2);
      constexpr int QA = LATE ? 8 : 0, QU = LATE ? 0 : 3;
      if (book == 1) {
        if (AC) {
          if (q == QA) setm0(lA + (LATE ? sub ^ 1 : sub) * 8192);
          if (q == QA + 1) pieceA(LATE ? sub ^ 1 : sub);
        }
        if (q == QU) { c.oB += stepB; lB += L1_BST; PINS(c.oB); PINS(lB); }
        if (q == QU + 1) { c.oB = c.oB == limB ? 0u : c.oB; PINS(c.oB); }
        if (q == QU + 2) { lB = lB == lB0 + 4 * L1_BST ? lB0 : lB; PINS(lB); }
        if (sub) {
          if (q == QU + 3) { c.oA += 128; lA += L1_AST; PINS(c.oA); PINS(lA); }
          if (q == QU + 4) { c.oA = c.oA == 1280u ? 0u : c.oA; PINS(c.oA); }
          if (q == QU + 5) { lA = lA == lA0 + 3 * L1_AST ? lA0 : lA; PINS(lA); }
          if (q == QU + 6) { dA = lA == lA0 + L1_AST ? -2 * L1_AST : L1_AST; PINS(dA); }
          if (q == QU + 7) { rare -= 1; PINS(rare); }
        }
        if (!sub && q == QU + 3) { rare2 = LATE ? (rare + 2) & ~2 : rare; PINS(rare2); }
        if (q == 11) setm0(lB);
      }
      if (book && q == 10 && (LATE ? sub : !sub)) { dB = -dB; PINS(dB); }
      SB();
    }
    __builtin_amdgcn_s_setprio(0);
    SEG(3);
    if (!LATE) { vmwait(); SEG(1); WAIT_L0(); BAR(); SEG(2); }
  };
  constexpr int LM = AC ? 1 : 2;
  if (!LATE) { upB(2, 5); dB = -dB; }
  WAIT_V(PRO_WAIT); BAR();
  if (ABL_RD) {
#pragma unroll
    for (int i = 0; i < 4; ++i) fa[i] = lds_b128(va0 + i * 2048);
#pragma unroll
    for (int i = 0; i < NT; ++i) fb[i] = lds_trfrag(c.ldsb + (lane & 63) * 8 + i * 1024);
  }
  if (LATE) {
    if (AC) { setm0(lA); SB(); }
    rdB(0, 0); rdB(1, 0); SB();
    if (AC) pieceA(0);
  }
  setm0(lB);
  SB();
  if (LATE) { rdB(2, 0); rdB(3, 0); } else asm volatile("s_nop 0");
  SB();
  const int rend = c.bstar - 8;
#pragma unroll 1
  do {
    L(0, LM);
    if (__builtin_expect(rare == 0, 0)) stats(0);
    U(0);
    C(0, true, 1, []() { WAIT_V(8); });
    L(1, LM);
    if (__builtin_expect(rare2 == 0, 0)) {
      PINS(rare);
      if (rare == 0) stats(1);
      else if (LATE && c.w == 0 && lane == 0) set_flag(ws, OFF_F1 + (c.Mt * 16 + c.cslot) * 8, tag);
    }
    U(1);
    C(1, true, 1, []() { WAIT_V(8); });
  } while (rare != rend);
  const u32x4 rw4 = srd(ws, WS_END);
  u32x2 fl = ld8_hidden(rw4, foff, 0);
  L(0, 1);
  U(0);
  C(0, true, 2, []() { WAIT_V(8); });
  L(1, 0);
  WAIT_V(3);
  PIN(fl);
  if (__builtin_expect(!__all(__builtin_bit_cast(u64, fl) == tag), 0)) wait_flags(ws, foff, tag);
  dma_sc1(rw4, lane * 16, OFF_SC + c.cslot * 15360 + c.w * 1024, c.ldsb + L1_GC + c.w * 1024);
  dma_sc1(rw4, lane * 16, OFF_SC + c.cslot * 15360 + c.w * 1024 + 8192, c.ldsb + L1_GC + c.w * 1024 + 8192);
  dma_sc1(rw4, lane * 16, OFF_SR + c.Mt * 10240 + c.w * 1024, c.ldsb + L1_GR + c.w * 1024);
  if (LATE && c.w < 2) dma_sc1(rw4, lane * 16, OFF_SR + c.Mt * 10240 + c.w * 1024 + 8192, c.ldsb + L1_GR + c.w * 1024 + 8192);
  U(1);
  C(1, true, 2, []() {});
  L(0, 0);
  wd_prefetch<0, 4>(rw4, c.ldsb, c.j, c.w, lane);
  C(0, true, 0, []() { WAIT_V(4); });
  L(1, 0);
  {
    __builtin_amdgcn_s_setprio(2);
    if (LATE || c.w < 6) {
      const LAS float* src = (const LAS float*)(size_t)(c.ldsb + L1_GC + (c.w * 64 + lane) * 4);
      float v[10];
#pragma unroll
      for (int p = 0; p < 10; ++p) v[p] = src[p * 384];
      *(LAS float*)(size_t)(c.ldsb + L1_CS + (c.w * 64 + lane) * 4) = (((v[0] + v[1]) + (v[2] + v[3])) + ((v[4] + v[5]) + (v[6] + v[7]))) + (v[8] + v[9]);
    } else {
      const LAS f32x2* src = (const LAS f32x2*)(size_t)(c.ldsb + L1_GR + ((c.w - 6) * 64 + lane) * 8);
      f32x2 v[10];
#pragma unroll
      for (int p = 0; p < 10; ++p) v[p] = src[p * 128];
      const f32x2 s = ((((v[0] + v[1]) + (v[2] + v[3])) + ((v[4] + v[5]) + (v[6] + v[7]))) + (v[8] + v[9])) * (1.0f / kD);
      LAS float* d = (LAS float*)(size_t)(c.ldsb + L1_ST + ((c.w - 6) * 64 + lane) * 4);
      d[0] = s[0]; d[128] = __builtin_amdgcn_rsqf(fmaxf(__builtin_fmaf(-s[0], s[0], s[1]), 0.f) + 1e-5f);
    }
    __builtin_amdgcn_s_setprio(0);
  }
  SB();
  wd_prefetch<4, 6>(rw4, c.ldsb, c.j, c.w, lane);
  SB();
  C(1, false, 0, []() {});
}

template <int N, class F> DEVI void static_for(F&& f) {
  if constexpr (N > 0) { static_for<N - 1>(f); f(std::integral_constant<int, N - 1>{}); }
}
DEVI u32x4 pair16(u32x2 a, u32x2 b) {
  const auto s0 = __builtin_amdgcn_permlane32_swap(a[0], b[0], false, false);
  const auto s1 = __builtin_amdgcn_permlane32_swap(a[1], b[1], false, false);
  return u32x4{s0[0], s1[0], s0[1], s1[1]};
}
struct Watch {
  LAS unsigned* lf; char* ws; u64 tag; int mt2, j, w, lane;
  unsigned status;
  DEVI bool poll(bool giveup) {
    int l2 = lane;
    PIN(l2);
    const unsigned f0 = l2 < 32 ? OFF_FH + mt2 * 256 + (j >> 2) * 128 + (l2 < 11 ? l2 : 0) * 8 : OFF_F3 + (blockIdx.x & 7) * 128 + (l2 & 15) * 8;
    const u64 no = __ballot(__hip_atomic_load((GAS u64*)(ws + f0), RLX_AGENT) != tag);
    unsigned s = ((no & 0x1ffull) ? 0u : S_H) | ((no & 0x600ull) ? 0u : S_Q) | ((no >> 32) ? 0u : S_M);
    if (giveup) s = 7u;
    if (s == status) return false;
    status = s;
    __hip_atomic_store(lf + LF_STATUS, s, RLX_WG);
    return true;
  }
  DEVI void need(unsigned bits) {
    if (w != 7) { lds_wait_bits(lf + LF_STATUS, bits); return; }
#pragma unroll 1
    for (unsigned spins = 0; (~status & bits) != 0u; ++spins)
      if (!poll(spins >= (1u << 20))) __builtin_amdgcn_s_sleep(1);
    asm volatile("" ::: "memory");
  }
};
DEVI void publish(Watch& wt, int cnt, unsigned flag) {
  if (wt.w == 7) wt.poll(false);
  WAIT_V(0);
  if (lds_inc(wt.lf + LF_PUB + cnt, wt.lane) == 3u && wt.lane < 2) set_flag(wt.ws, flag + wt.lane * 128, wt.tag);
}

struct PF2 {
  char* ws; rsrc_t rws; unsigned ldsb; int j, w, lane;
  u32x4 (&br)[3][5];
  template <int N> DEVI void item() {
    if constexpr (N < 13) wd_prefetch<6 + N, 7 + N>(srd(ws, WS_END), ldsb, j, w, lane);
    else br[(N - 13) / 5][(N - 13) % 5] = ld16(rws, lane * 16, OFF_WD + (j * 54 + 30 + w + 8 * ((N - 13) / 5)) * 5120 + ((N - 13) % 5) * 1024);
    SB();
  }
  template <int N0, int N1> DEVI void items() { if constexpr (N0 < N1) { item<N0>(); items<N0 + 1, N1>(); } }
};

DEVI void epi_ffn(LAS char* lds, f32x4 (&acc)[4][6], char* ws, rsrc_t rws, Watch& wt, const P1& c, PF2& pf, int lane) {
  const int r = lane & 15, g = lane >> 4;
  pf.items<0, 4>();
  float nm[4], k1[4], k2[4], iv[4];
#pragma unroll
  for (int mi = 0; mi < 4; ++mi) {
    const LAS float* sp = (const LAS float*)(lds + L1_ST) + c.mw * 64 + mi * 16 + r;
    const float rs = sp[128], c0 = -2.0f * 0.7978845608028654f * 1.4426950408889634f;
    nm[mi] = -sp[0]; k1[mi] = c0 * rs; k2[mi] = c0 * 0.044715f * rs * rs * rs; iv[mi] = __builtin_amdgcn_rcpf(rs * rs);
  }
  const unsigned hb = OFF_H + ((c.Mt * 2 + c.mw) * 54 + 2 * c.cslot + (c.nw >> 1)) * 4096 + (c.nw & 1) * 512 + (lane & 31) * 16 + (lane >> 5) * 1024;
  u32x4 v[6];
  static_for<3>([&](auto I) {
    constexpr int i = I;
    const f32x4 cg = *(LAS f32x4*)(lds + L1_CS + ((c.nw * 6 + i) * 16 + 4 * g) * 4);
    const f32x4 cu = *(LAS f32x4*)(lds + L1_CS + ((c.nw * 6 + 3 + i) * 16 + 4 * g) * 4);
    u32x2 o[4];
    static_for<4>([&](auto MI) {
      constexpr int mi = MI, n = 4 * i + mi;
      if constexpr (n < 10) pf.template items<4 + (12 * n) / 5, 4 + (12 * n + 12) / 5>();
#pragma unroll
      for (int hh = 0; hh < 2; ++hh) {
        const f32x2 G = fma2(bc2(nm[mi]), hh ? hi2(cg) : lo2(cg), hh ? hi2(acc[mi][i]) : lo2(acc[mi][i]));
        const f32x2 U = fma2(bc2(nm[mi]), hh ? hi2(cu) : lo2(cu), hh ? hi2(acc[mi][3 + i]) : lo2(acc[mi][3 + i]));
        const f32x2 z = G * fma2(G * G, bc2(k2[mi]), bc2(k1[mi]));
        const f32x2 d = fma2(f32x2{__builtin_amdgcn_exp2f(z[0]), __builtin_amdgcn_exp2f(z[1])}, bc2(iv[mi]), bc2(iv[mi]));
        o[mi][hh] = pk2(G * U * f32x2{__builtin_amdgcn_rcpf(d[0]), __builtin_amdgcn_rcpf(d[1])});
      }
      PIN(o[mi]);
      SB();
#define STN 10
#define STS 1
      if constexpr (n == STN || n == STN + STS) {
        constexpr int p = (n - STN) / STS;
        st16(rws, hb, p * 18 * 4096, v[2 * p]);
        st16(rws, hb, p * 18 * 4096 + 2048, v[2 * p + 1]);
        SB();
      }
    });
    v[2 * i] = pair16(o[0], o[1]); v[2 * i + 1] = pair16(o[2], o[3]);
    PIN(v[2 * i]); PIN(v[2 * i + 1]);
  });
  st16(rws, hb, 36 * 4096, v[4]);
  st16(rws, hb, 36 * 4096 + 2048, v[5]);
  STAMP(4);
  publish(wt, c.mw, OFF_FH + (c.Mt * 2 + c.mw) * 256 + c.cslot * 8);
  lds_inc((LAS unsigned*)(lds + L2_FLAG) + LF_DMA, lane);
}

DEVI void epi_q(LAS char* lds, f32x4 (&acc)[4][6], char* ws, rsrc_t rws, Watch& wt, const P1& c, PF2& pf, int lane) {
  const int r = lane & 15, g = lane >> 4, qi = c.cslot - 13;
  f32x4 cs[5];
#pragma unroll
  for (int i = 0; i < 5; ++i) cs[i] = *(LAS f32x4*)(lds + L1_CS + ((c.nw * 6 + i) * 16 + 4 * g) * 4);
  pf.items<0, 8>();
  static_for<4>([&](auto MI) {
    constexpr int mi = MI;
    const LAS float* sp = (const LAS float*)(lds + L1_ST) + c.mw * 64 + mi * 16 + r;
    const f32x2 nm = bc2(-sp[0]), rs = bc2(sp[128]);
    static_for<5>([&](auto I) {
      constexpr int i = I;
      const f32x2 a = phi2(fma2(nm, lo2(cs[i]), lo2(acc[mi][i])), rs), b = phi2(fma2(nm, hi2(cs[i]), hi2(acc[mi][i])), rs);
      acc[mi][i] = f32x4{a[0], a[1], b[0], b[1]};
      PIN(acc[mi][i]);
      SB();
      pf.template item<8 + mi * 5 + i>();
    });
  });
  STAMP(3);
  LAS unsigned* lf = (LAS unsigned*)(lds + L2_FLAG);
  if (c.w == 4) { wait_flags(ws, OFF_FK + (blockIdx.x & 7) * 512 + qi * 256 + (lane & 31) * 8, wt.tag); lds_inc(lf + LF_K, lane); }
  else lds_wait(lf + LF_K, 1u);
  STAMP(12);
  {
    const int q4 = (c.w & 1) * 64 + lane, pr = q4 / 10;
    f32x4 part[4];
#pragma unroll
    for (int p = 0; p < 4; ++p)
      part[p] = __builtin_bit_cast(f32x4, ld16(rws, q4 < 80 ? pr * 55296 + (q4 - pr * 10) * 16 : OOB, OFF_KVP + 3200 + (qi * 128 + (c.w >> 1) * 4 + p) * 3456));
    SB();
    if (q4 < 80) *(LAS f32x4*)(lds + L2_RED + (c.w >> 1) * 1280 + q4 * 16) = (part[0] + part[1]) + (part[2] + part[3]);
    WAIT_L0(); BAR();
  }
  STAMP(13);
  f32x4 z[5];
#pragma unroll
  for (int i = 0; i < 5; ++i) {
    const LAS char* p = lds + L2_RED + (c.nw * 80 + 20 * (2 * (g & 1) + (g >> 1)) + 4 * i) * 4;
    z[i] = (*(const LAS f32x4*)p + *(const LAS f32x4*)(p + 1280)) + (*(const LAS f32x4*)(p + 2560) + *(const LAS f32x4*)(p + 3840));
  }
  const unsigned qb = OFF_Q + ((c.Mt * 2 + c.mw) * 20 + (2 * qi + (c.nw >> 1)) * 5) * 4096;
  u32x2 keep;
#pragma unroll
  for (int mi = 0; mi < 4; ++mi) {
    f32x2 d0 = lo2(acc[mi][0]) * lo2(z[0]), d1 = hi2(acc[mi][0]) * hi2(z[0]);
#pragma unroll
    for (int i = 1; i < 5; ++i) { d0 = fma2(lo2(acc[mi][i]), lo2(z[i]), d0); d1 = fma2(hi2(acc[mi][i]), hi2(z[i]), d1); }
    d0 += d1;
    const f32x2 inv = bc2(__builtin_amdgcn_rcpf(d0[0] + d0[1] + 1e-6f));
    unsigned o[10];
#pragma unroll
    for (int i = 0; i < 5; ++i) { o[2 * i] = pk2(lo2(acc[mi][i]) * inv); o[2 * i + 1] = pk2(hi2(acc[mi][i]) * inv); }
    st16(rws, lane * 16 + mi * 1024, qb + (c.nw & 1) * 12288, u32x4{o[0], o[1], o[2], o[3]});
    st16(rws, lane * 16 + mi * 1024, qb + (c.nw & 1) * 12288 + 4096, u32x4{o[4], o[5], o[6], o[7]});
    if (mi & 1) st16(rws, (lane & 31) * 16 + (lane >> 5) * 1024 + (mi - 1) * 1024, qb + 8192 + (c.nw & 1) * 512, pair16(keep, u32x2{o[8], o[9]}));
    else keep = u32x2{o[8], o[9]};
  }
  STAMP(4);
  publish(wt, c.mw, OFF_FH + (c.Mt * 2 + c.mw) * 256 + (9 + qi) * 8);
  lds_inc(lf + LF_DMA, lane);
}

DEVI void epi_kv(LAS char* lds, f32x4 (&acc)[4][6], rsrc_t rws, const P1& c, int lane) {
  const int cl = lane & 15, g = lane >> 4;
  float cs[5];
#pragma unroll
  for (int i = 0; i < 5; ++i) cs[i] = *(LAS float*)(lds + L1_CS + ((c.nw * 6 + i) * 16 + cl) * 4);
  u32x4 fr[2][6];
#pragma unroll
  for (int mi = 0; mi < 4; ++mi) {
    const f32x4 mean = *(LAS f32x4*)(lds + L1_ST + (c.mw * 64 + mi * 16 + 4 * g) * 4);
    const f32x4 rstd = *(LAS f32x4*)(lds + L1_ST + 512 + (c.mw * 64 + mi * 16 + 4 * g) * 4);
#pragma unroll
    for (int i = 0; i < 5; ++i) {
#pragma unroll
      for (int hh = 0; hh < 2; ++hh) {
        const f32x2 rs = hh ? hi2(rstd) : lo2(rstd);
        const f32x2 t = fma2(hh ? hi2(mean) : lo2(mean), bc2(-cs[i]), hh ? hi2(acc[mi][i]) : lo2(acc[mi][i]));
        fr[mi >> 1][i][(mi & 1) * 2 + hh] = pk2((i == 2 || i == 3) ? t * rs : phi2(t, rs));
        if (i == 4) fr[mi >> 1][5][(mi & 1) * 2 + hh] = cl < 8 ? 0x3f803f80u : pk2(t * rs);
      }
    }
  }
  constexpr int KI[7] = {0, 0, 4, 1, 1, 4, 4}, VI[7] = {2, 5, 2, 3, 5, 3, 5};
  f32x4 p[7];
#pragma unroll
  for (int q = 0; q < 7; ++q) {
    p[q] = mfma(fr[0][VI[q]], fr[0][KI[q]], f32x4{0.f, 0.f, 0.f, 0.f});
    p[q] = mfma(fr[1][VI[q]], fr[1][KI[q]], p[q]);
  }
  LAS char* ex = lds + L1_EX + c.w * 3584;
  const int uh = cl < 4 ? 16 + cl : 32 + cl;
  auto put = [&](bool ok, int u, int e, f32x4 v) { *(LAS f32x4*)(ex + (ok ? (u * 20 + e) * 4 : 3360)) = v; };
  auto putz = [&](bool ok, int u, float v) { *(LAS float*)(ex + (ok ? 3200 + u * 4 : 3360)) = v; };
  put(true, cl, 4 * g, p[0]);
  put(true, 20 + cl, 4 * g, p[3]);
  put(g >= 2, (g & 1) * 20 + cl, 16, g == 2 ? p[1] : p[4]);
  put(cl < 8, uh, 4 * g, cl < 4 ? p[2] : p[5]);
  put(cl < 8 && g == 2 + (cl >> 2), uh, 16, p[6]);
  putz(g < 2, g * 20 + cl, g == 0 ? p[1][0] : p[4][0]);
  putz(g == 0 && cl < 8, uh, p[6][0]);
  WAIT_L0(); BAR();
#pragma unroll
  for (int q = 0; q < 2; ++q) {
    const int o = (c.mw * 128 + q * 64 + lane) * 16;
    const LAS char* src = lds + L1_EX + c.nw * 3584 + (o < 3360 ? o : 0);
    const f32x4 v = *(const LAS f32x4*)src + *(const LAS f32x4*)(src + 4 * 3584);
    st16(rws, o < 3360 ? o : OOB, OFF_KVP + (((c.cslot - 9) * 4 + c.nw) * 16 + c.Mt) * 3456, __builtin_bit_cast(u32x4, v));
  }
}

DEVI void retile(const char* wd, rsrc_t rws, unsigned ldsw, int id, int w, int lane) {
  const u32x4 rd = srd(wd, kF * 1280);
  const int jj = lane >> 3, g = lane >> 4;
  const unsigned vo = ((jj >> 2) * 8 + (jj & 3)) * 1280 + (lane & 7) * 16;
  const unsigned ra = ldsw + g * 1024 + ((lane & 15) >> 2) * 128 + (lane & 3) * 8;
  auto fetch = [&](int n) {
    const int it = id * 8 + w + n * 128, rb = it / 10, ln = it - rb * 10;
#pragma unroll
    for (int q = 0; q < 4; ++q)
      dma<0>(rd, vo, it < 540 ? (rb * 32 + 16 * (q >> 1) + 4 * (q & 1)) * 1280 + ln * 128 : OOB, ldsw + (n & 3) * 4096 + q * 1024);
  };
  auto put = [&](int n) {
    const int it = id * 8 + w + n * 128, rb = it / 10, ln = it - rb * 10, cg = rb / 6, uu = rb - cg * 6;
    const int ks = 18 * (uu >> 1) + 2 * cg + (uu & 1);
#pragma unroll
    for (int q = 0; q < 4; ++q) {
      const int nt = ln * 4 + q, j = nt / 5, t = nt - j * 5;
      st16(rws, lane * 16, it < 540 ? OFF_WD + ((j * 54 + ks) * 5 + t) * 1024 : OOB, lds_trfrag(ra + (n & 3) * 4096 + q * 32));
    }
  };
#pragma unroll 1
  for (int n = 0; n < 4; ++n) fetch(n);
  WAIT_V(0);
  put(0);
  WAIT_L0();
  fetch(4);
#pragma unroll 1
  for (int n = 1; n < 5; ++n) {
    if (n == 4) WAIT_V(0);
    put(n);
  }
}

DEVI void fold_kv(LAS char* lds, char* ws, rsrc_t rws, Watch& wt, const u32x4 (&wf)[2][5], int hp, int w, int lane) {
  const u64 tag = wt.tag;
  const int cl = lane & 15, g = lane >> 4;
  const int o = ((w & 3) * 64 + lane) * 16;
  unsigned po = o < 3200 ? o : OOB, ko[6][2];
#pragma unroll
  for (int f = 0; f < 6; ++f) {
    const int u = 20 * (f & 1) + (f < 4 ? (cl & 4) * 2 + 4 * (f >> 1) : 16) + (cl & 3);
    const bool mine = f >= 4 || (cl >> 3) == (f & 1);
    ko[f][0] = (unsigned)(size_t)lds + L2_RED + (mine && g < 3 ? u * 80 + g * 32 : 3360);
    ko[f][1] = (unsigned)(size_t)lds + L2_RED + (mine && g < 2 ? u * 80 + g * 32 + 16 : 3360);
    PIN(ko[f][0]); PIN(ko[f][1]);
  }
  unsigned vo = OFF_MP + (w * 20 + (hp >> 2) * 5) * 5120 + (3 * ((hp >> 1) & 1) + (g & 1)) * 5120 + (((hp & 1) + (g & 2)) * 16 + cl) * 16;
  unsigned vo2 = OFF_MP + (w * 20 + (hp >> 2) * 5 + 2) * 5120 + g * 1024 + ((hp & 3) * 16 + cl) * 16;
  PIN(po); PIN(vo); PIN(vo2);
  WAIT_V(0);
  if (w == 0) wait_flags(ws, OFF_FK + (blockIdx.x & 7) * 512 + (hp >> 2) * 128 + cl * 8, tag);
  BAR();
  STAMP(2);
  {
    f32x4 part[8];
#pragma unroll
    for (int p = 0; p < 8; ++p) part[p] = __builtin_bit_cast(f32x4, ld16(rws, po, OFF_KVP + (hp * 16 + (w >> 2) * 8 + p) * 3456));
    SB();
    *(LAS f32x4*)(lds + L2_RED + (w >> 2) * 4096 + o) = ((part[0] + part[1]) + (part[2] + part[3])) + ((part[4] + part[5]) + (part[6] + part[7]));
    WAIT_L0(); BAR();
  }
  STAMP(12);
  u32x4 kf[6];
#pragma unroll
  for (int f = 0; f < 6; ++f) {
    const f32x4 lo = *(LAS f32x4*)(size_t)ko[f][0] + *(LAS f32x4*)(size_t)(ko[f][0] + 4096);
    const f32x4 hi = *(LAS f32x4*)(size_t)ko[f][1] + *(LAS f32x4*)(size_t)(ko[f][1] + 4096);
    kf[f] = u32x4{pk2(lo2(lo)), pk2(hi2(lo)), pk2(lo2(hi)), pk2(hi2(hi))};
  }
  const f32x4 zero = {0.f, 0.f, 0.f, 0.f};
  u32x4 r1;
#pragma unroll
  for (int i = 0; i < 5; ++i) {
    const f32x4 d1 = mfma(kf[1], wf[1][i], mfma(kf[0], wf[0][i], zero)), d2 = mfma(kf[3], wf[1][i], mfma(kf[2], wf[0][i], zero));
    const f32x4 d3 = mfma(kf[4], wf[0][i], zero), d4 = mfma(kf[5], wf[1][i], zero);
    st16(rws, vo, i * 1024, u32x4{pk2(lo2(d1)), pk2(hi2(d1)), pk2(lo2(d2)), pk2(hi2(d2))});
    const u32x4 t = {pk2(lo2(d3)), pk2(hi2(d3)), pk2(lo2(d4)), pk2(hi2(d4))};
    if (i == 0 || i == 4) r1 = t; else r1 = g == i ? t : r1;
    if (i == 3) st16(rws, vo2, 0, r1);
    if (i == 4) st16(rws, g == 0 ? vo2 + 4096 : OOB, 0, r1);
  }
  STAMP(3);
  if (w == 7) wt.poll(false);
  WAIT_V(0);
  if (lds_inc(wt.lf + LF_PUB, lane) == 7u && lane < 8) set_flag(ws, OFF_F3 + lane * 128 + hp * 8, tag);
  STAMP(13);
}

DEVI void phase2(LAS char* lds, const char* x, char* ws, rsrc_t rws, char* out, Watch& wt, const u32x4 (&br)[3][5],
                 int mt2, int j, int w, int lane) {
  const int a = w & 3;
  LAS unsigned* lf = wt.lf;
  const unsigned obase = (mt2 * 64 + a * 16) * 1280 + j * 160;
  f32x4 acc[4][5];
  if (w >= 4) __builtin_amdgcn_s_setprio(2);
  {
    const rsrc_t rx = __builtin_amdgcn_make_buffer_rsrc((void*)x, 0, 2048 * 1280, 0x00020000);
    u32x2 xr[5];
#pragma unroll
    for (int t = 0; t < 5; ++t)
      xr[t] = __builtin_amdgcn_raw_buffer_load_b64(rx, (lane & 15) * 1280 + (lane >> 4) * 8 + t * 32, w >= 4 ? obase : OOB, 0);
    SB();
    wt.need(S_H);
    lds_wait(lf + LF_DMA, 8u);
    STAMP(6);
    const unsigned ab = OFF_H + (mt2 * 54 + w) * 4096;
    u32x4 fa[7][4], fb[4][5];
    auto ldA = [&](int n) {
      const unsigned so = n == 6 && w >= 6 ? OOB : ab + (n < 3 ? 30 + 8 * n : 8 * n - 24) * 4096;
#pragma unroll
      for (int m = 0; m < 4; ++m) fa[n][m] = ld16(rws, lane * 16, so + ((m + w) & 3) * 1024);
      SB();
    };
    unsigned lb = (unsigned)(size_t)lds + w * 5120 + lane * 16;
    auto rdB = [&](int n) {
      if (n == 2) { lb += 81920; PIN(lb); }
#pragma unroll
      for (int t = 0; t < 5; ++t) fb[n][t] = lds_b128(lb + (n & 1) * 40960 + t * 1024);
      SB();
    };
#pragma unroll
    for (int n = 0; n < 6; ++n) ldA(n);
#pragma unroll
    for (int t = 0; t < 5; ++t) {
      const f32x2 x0 = unpk(xr[t][0]), x1 = unpk(xr[t][1]);
      acc[0][t] = f32x4{x0[0], x0[1], x1[0], x1[1]};
      PIN(acc[0][t]);
    }
    SB();
#pragma unroll
    for (int n = 0; n < 7; ++n) {
      if (n >= 2 && n < 6) rdB(n - 2);
      if (n != 6 || w < 6) {
#pragma unroll
        for (int m = 0; m < 4; ++m)
#pragma unroll
          for (int t = 0; t < 5; ++t) {
            if (n == 0 && m > 0) mfma_new(acc[m][t], br[0][t], fa[0][m]);
            else mfma_acc(acc[m][t], n < 3 ? br[n][t] : fb[n - 3][t], fa[n][m]);
          }
      }
      SB();
      if (n == 0) { STAMP(9); ldA(6); }
      if (n == 5) { WAIT_L0(); lds_inc(lf + LF_BC, lane); }
    }
  }
  STAMP(11);
  if (w < 4) __builtin_amdgcn_s_setprio(3);
  if (w >= 4) {
    wt.need(S_Q);
    u32x4 pq[5][4], mf[5][5];
#pragma unroll
    for (int s = 0; s < 5; ++s) {
#pragma unroll
      for (int m = 0; m < 4; ++m) pq[s][m] = ld16(rws, lane * 16, OFF_Q + (mt2 * 20 + a * 5 + s) * 4096 + ((m + w) & 3) * 1024);
      SB();
    }
    wt.need(S_M);
    STAMP(7);
    const unsigned mo = OFF_MP + (j * 20 + a * 5) * 5120;
    auto ldM = [&](int s) {
#pragma unroll
      for (int t = 0; t < 5; ++t) mf[s][t] = ld16(rws, lane * 16, mo + s * 5120 + t * 1024);
      SB();
    };
#pragma unroll
    for (int s = 0; s < 4; ++s) ldM(s);
    __builtin_amdgcn_s_setprio(0);
#pragma unroll
    for (int s = 0; s < 5; ++s) {
#pragma unroll
      for (int t = 0; t < 5; ++t)
#pragma unroll
        for (int m = 0; m < 4; ++m) mfma_acc(acc[m][t], mf[s][t], pq[s][m]);
      SB();
      if (s == 0) ldM(4);
    }
  }
  asm volatile("s_nop 7" ::: "memory");
  SB();
  STAMP(14);
  LAS char* ex = lds + (w >= 4 ? L2_PA : L2_PF) + lane * 16;
  lds_wait(lf + LF_BC, 8u);
#pragma unroll
  for (int m = 1; m < 4; ++m)
#pragma unroll
    for (int t = 0; t < 5; ++t) *(LAS f32x4*)(ex + (a * 15 + (m - 1) * 5 + t) * 1024) = acc[m][t];
  WAIT_L0();
  if (w < 4) { lds_inc(lf + LF_X, lane); lds_wait(lf + LF_X, 4u); }
  else BAR();
  STAMP(8);
#pragma unroll
  for (int t = 0; t < 5; ++t)
#pragma unroll
    for (int q = 1; q < 4; ++q) acc[0][t] += *(LAS f32x4*)(ex + (((a - q) & 3) * 15 + (q - 1) * 5 + t) * 1024);
  LAS char* base = lds + L2_BASE + a * 5120 + lane * 16;
  if (w < 4) {
#pragma unroll
    for (int t = 0; t < 5; ++t) *(LAS f32x4*)(base + t * 1024) = acc[0][t];
    WAIT_L0();
  } else {
    const rsrc_t ro = __builtin_amdgcn_make_buffer_rsrc(out, 0, 2048 * 1280, 0x00020000);
#pragma unroll
    for (int t = 0; t < 5; ++t) {
      const f32x4 s = acc[0][t] + *(LAS f32x4*)(base + t * 1024);
      __builtin_amdgcn_raw_buffer_store_b64(u32x2{pk2(lo2(s)), pk2(hi2(s))}, ro, (lane & 15) * 1280 + (lane >> 4) * 8 + t * 32, obase, 0);
    }
  }
}

__global__ __launch_bounds__(512, 2) void fused(const char* x, const char* wqkv, const char* wo, const char* wg,
                                                const char* wu, const char* wd, char* ws, char* out) {
  extern __shared__ __attribute__((aligned(16))) char smem[];
  const char* pc0;
  asm volatile("s_getpc_b64 %0" : "=s"(pc0));
  asm volatile("" : "+s"(x), "+s"(wqkv), "+s"(wo), "+s"(wg), "+s"(wu), "+s"(wd), "+s"(ws), "+s"(out));
  LAS char* lds = (LAS char*)smem;
  const unsigned ldsb = (unsigned)(size_t)lds;
  const int bid = blockIdx.x, xcd = bid & 7, idx = bid >> 3;
  const int Mt = (xcd & 1) * 8 + (idx & 7), grp = xcd >> 1, pos4 = idx >> 3, slot2 = grp * 4 + pos4;
  const int cslot = (int)(0xCF76BE54AD329810ull >> (4 * slot2)) & 15;
  const int w = __builtin_amdgcn_readfirstlane(threadIdx.x >> 6), lane = threadIdx.x & 63;
  STAMP(0);
  const rsrc_t rws = __builtin_amdgcn_make_buffer_rsrc(ws, 0, WS_END, 0x00020000);
  u64 tag;
  Watch wt{(LAS unsigned*)(lds + L2_FLAG), ws, 0ull, Mt * 2 + (slot2 >> 3), slot2 & 7, w, lane, 0u};
  u32x4 br[3][5];
  PF2 pf{ws, rws, ldsb, slot2 & 7, w, lane, br};
  if (cslot == 15) {
    tag = make_tag(ws);
    PINS(tag);
    wt.tag = tag;
    if (threadIdx.x < 16) ((LAS unsigned*)(lds + L2_FLAG))[threadIdx.x] = 0u;
    if (WARM) warm_up(x, wqkv, wo, wg, wu, wd, ws, pc0, ldsb + L1_GR + 8192, bid, w, lane);
    retile(wd, rws, ldsb + w * 16384, Mt, w, lane);
    WAIT_V(0); WAIT_L0(); BAR();
    if (threadIdx.x == 0) { set_flag(ws, OFF_FR + Mt * 8, tag); set_flag(ws, OFF_F1 + (Mt * 16 + 15) * 8, tag); }
    STAMP(5);
    u32x4 wf[2][5];
    {
      const u32x4 ro = srd(wo, kD * 1280);
#pragma unroll 1
      for (int q = 0; q < 10; ++q)
        dma<0>(ro, (lane >> 3) * 1280 + (lane & 7) * 16, (Mt * 40 + (q >> 1) * 8) * 1280 + ((w * 160) & ~127) + (q & 1) * 128, ldsb + w * 16384 + q * 1024);
      WAIT_V(0);
#pragma unroll
      for (int hh = 0; hh < 2; ++hh) {
        unsigned ra[2];
#pragma unroll
        for (int h2 = 0; h2 < 2; ++h2) {
          int row = hh * 20 + (lane >> 4) * 8 + h2 * 4 + ((lane & 15) >> 2);
          row = row < 40 ? row : 39;
          ra[h2] = ldsb + w * 16384 + (row >> 3) * 2048 + (row & 7) * 128 + (lane & 3) * 8;
        }
#pragma unroll
        for (int i = 0; i < 5; ++i) {
          const int cb = (w & 3) * 32 + i * 32, o = (cb >> 7) * 1024 + (cb & 127);
          const u32x2 lo = __builtin_bit_cast(u32x2, __builtin_amdgcn_ds_read_tr16_b64_v4i16((LAS s16x4*)(size_t)(ra[0] + o)));
          const u32x2 hi = __builtin_bit_cast(u32x2, __builtin_amdgcn_ds_read_tr16_b64_v4i16((LAS s16x4*)(size_t)(ra[1] + o)));
          wf[hh][i] = u32x4{lo[0], lo[1], hi[0], hi[1]};
        }
      }
    }
    wait_flags(ws, OFF_FR + (lane & 15) * 8, tag);
    WAIT_L0(); BAR();
    wd_prefetch<0, 6>(srd(ws, WS_END), ldsb, slot2 & 7, w, lane);
    pf.items<0, 28>();
    if (Mt == 0 && w == 7) {
#pragma unroll 1
      for (int q = 0; q < 4; ++q) wait_flags(ws, OFF_F1 + (q * 64 + lane) * 8, tag);
      if (lane == 0) set_flag(ws, OFF_E, tag);
    }
    fold_kv(lds, ws, rws, wt, wf, Mt, w, lane);
    lds_inc((LAS unsigned*)(lds + L2_FLAG) + LF_DMA, lane);
  } else {
    const int type = cslot < 9 ? 0 : cslot < 13 ? 1 : 2;
    P1 c;
    c.w = w; c.mw = w >> 2; c.nw = w & 3; c.Mt = Mt; c.cslot = cslot; c.j = slot2 & 7;
    c.ldsb = ldsb;
    {
#define ROTM 1
      const int kb = (Mt + cslot) % 10, rot = ROTM == 0 ? (kb >= 5 ? 5 : 0) : ROTM == 1 ? (kb > 7 ? 7 : kb) : (kb / 3) * 3 - (kb == 9 ? 3 : 0);
      c.bstar = kb - rot;
      const int seg = w >> 2;
      c.pitch = type == 0 ? kF * 2 : kQKV * 2;
      const int startb = type == 0 ? cslot * 384
                       : ((type == 1 ? (640 * (1 + seg) + 160 * (cslot - 9)) * 2 : 320 * (2 * (cslot - 13) + seg)) & ~127);
      const char* wb = type == 0 ? (seg ? wu : wg) : wqkv;
      c.rw = srd(wb + startb - 2048, 640 * c.pitch - startb + 2048);
      c.rx = srd(x + (size_t)Mt * 128 * 1280, 128 * 1280);
      const int i = lane >> 3, cp = lane & 7, pg = w & 3;
      c.voffB = (unsigned)(8 * pg + i) * c.pitch + 16 * (cp ^ ((i & 2) | ((pg & 1) << 2))) + 2048;
      c.voffA = (unsigned)(8 * w + i) * 1280 + 16 * (cp ^ ((4 * w + (i >> 1)) & 7));
      c.oB = rot * 64 * c.pitch; c.oA = rot * 128;
      const unsigned lB = c.ldsb + L1_B + w * 3072, lA = c.ldsb + L1_A + w * 1024;
      dma<0>(c.rx, c.voffA, c.oA, lA);
#pragma unroll
      for (int s = 0; s < 3; ++s) {
        dma<0>(c.rw, c.voffB, c.oB, lB + s * L1_BST);
        dma<128>(c.rw, c.voffB, c.oB, lB + s * L1_BST + 1024);
        dma<256>(c.rw, c.voffB, c.oB, lB + s * L1_BST + 2048);
        c.oB += 32 * c.pitch;
        dma<0>(c.rx, c.voffA, c.oA + ((s + 1) >> 1) * 128 + ((s + 1) & 1) * 81920, lA + (s + 1) * 8192);
      }
      c.oA += 256;
    }
    SB();
    if (WARM & 8) BAR();
    if (WARM) warm_up(x, wqkv, wo, wg, wu, wd, ws, pc0, ldsb + L1_GR + 8192, bid, w, lane);
    SB();
    tag = make_tag(ws);
    PINS(tag);
    wt.tag = tag;
    unsigned vb[5], va0;
    {
      const int g = lane >> 4, q = (lane & 15) >> 2, p = lane & 3, r = lane & 15, nw = c.nw;
      const int swz = (q & 2) | ((g & 1) << 2);
#pragma unroll
      for (int pos = 0; pos < 5; ++pos) {
        const int cf = 16 * nw + 4 * p;
        const int hb = (cslot & 1 ? 0 : 32) + 40 * nw;
        const int ck = pos < 4 ? (pos >> 1) * 192 + hb + 20 * (pos & 1) + 4 * p : (p >> 1) * 192 + hb + 20 * (p & 1) + 16;
        const int cq = (nw >> 1) * 224 + 20 * (4 * (nw & 1) + 2 * (p & 1) + (p >> 1)) + 4 * pos;
        const int col = type == 0 ? cf : type == 1 ? ck : cq;
        const int line = col >> 6, seg = line >= 3;
        vb[pos] = c.ldsb + L1_B + seg * (12288 - 3072) + (g * 3 + line) * 1024 + q * 128 + 16 * (((col & 63) >> 3) ^ swz) + 8 * ((col >> 2) & 1);
      }
      va0 = c.ldsb + L1_A + (c.mw * 64 + r) * 128 + 16 * (g ^ ((r >> 1) & 7));
    }
    if (threadIdx.x < 16) ((LAS unsigned*)(lds + L2_FLAG))[threadIdx.x] = 0u;
    f32x4 acc[4][6];
#pragma unroll
    for (int m = 0; m < 4; ++m)
#pragma unroll
      for (int i = 0; i < 6; ++i) acc[m][i] = f32x4{0.f, 0.f, 0.f, 0.f};
    unsigned foff = lane >= 20 ? OFF_FR + (lane & 15) * 8 : OFF_F1 + (lane < 10 ? lane * 16 + cslot : Mt * 16 + lane - 10) * 8;
    PIN(foff);
#define P1_STAMP
#define P1_RUN(T) do { if (w < 4) p1_loop<T, true>(c, vb, va0, foff, acc, ws, rws, tag, lane); \
                       else p1_loop<T, false>(c, vb, va0, foff, acc, ws, rws, tag, lane); P1_STAMP; } while (0)
    if (type == 0) { P1_RUN(0); epi_ffn(lds, acc, ws, rws, wt, c, pf, lane); STAMP(5); }
    else if (type == 2) { P1_RUN(2); epi_q(lds, acc, ws, rws, wt, c, pf, lane); STAMP(5); }
    else {
      P1_RUN(1);
      epi_kv(lds, acc, rws, c, lane);
      STAMP(4);
      WAIT_V(0); WAIT_L0(); BAR();
      if (threadIdx.x < 8) set_flag(ws, OFF_FK + threadIdx.x * 512 + (cslot - 9) * 128 + Mt * 8, tag);
      STAMP(5);
      pf.items<0, 28>();
      WAIT_V(15);
      lds_inc((LAS unsigned*)(lds + L2_FLAG) + LF_DMA, lane);
    }
  }
  phase2(lds, x, ws, rws, out, wt, br, Mt * 2 + (slot2 >> 3), slot2 & 7, w, lane);
  STAMP(10);
}

extern "C" void kernel_launch(void* const* d_in, const int* in_sizes, int n_in, void* d_out, int out_size,
                              void* d_ws, size_t ws_size, hipStream_t stream) {
  (void)in_sizes; (void)n_in; (void)out_size; (void)ws_size;
  (void)hipFuncSetAttribute((const void*)fused, hipFuncAttributeMaxDynamicSharedMemorySize, LDS_SIZE);
  fused<<<256, 512, LDS_SIZE, stream>>>((const char*)d_in[0], (const char*)d_in[1], (const char*)d_in[2], (const char*)d_in[4],
                                        (const char*)d_in[5], (const char*)d_in[6], (char*)d_ws, (char*)d_out);
}
```

```cpp
#define DIAG 0
#include <hip/hip_runtime.h>
#include <stdint.h>
#include <type_traits>

#define GAS __attribute__((address_space(1)))
#define LAS __attribute__((address_space(3)))
typedef __attribute__((ext_vector_type(2))) __bf16 bf16x2;
typedef __attribute__((ext_vector_type(8))) __bf16 bf16x8;
typedef __attribute__((ext_vector_type(4))) short s16x4;
typedef __attribute__((ext_vector_type(4))) float f32x4;
typedef __attribute__((ext_vector_type(2))) float f32x2;
typedef __attribute__((ext_vector_type(4))) unsigned u32x4;
typedef __attribute__((ext_vector_type(2))) unsigned u32x2;
typedef unsigned long long u64;
typedef __amdgpu_buffer_rsrc_t rsrc_t;

#define DEVI __device__ __forceinline__
#define WAIT_V_(n) asm volatile("s_waitcnt vmcnt(" #n ")" ::: "memory")
#define WAIT_V(n) WAIT_V_(n)
#define WAIT_L0() do { __builtin_amdgcn_s_waitcnt(0xC07F); asm volatile("" ::: "memory"); } while (0)
#define SB() __builtin_amdgcn_sched_barrier(0)
#define PIN(x) asm volatile("" : "+v"(x))
#define PINS(x) asm volatile("" : "+s"(x))
#define BAR() do { asm volatile("" ::: "memory"); __builtin_amdgcn_s_barrier(); asm volatile("" ::: "memory"); } while (0)
#define RLX_AGENT __ATOMIC_RELAXED, __HIP_MEMORY_SCOPE_AGENT
#define RLX_WG __ATOMIC_RELAXED, __HIP_MEMORY_SCOPE_WORKGROUP
#define SC1 16

#define STAMPS
#define OFF_STAMP (128u << 20)
#define STAMP_LDS(i) (*(LAS unsigned*)(size_t)(162880 + (threadIdx.x >> 6) * 64 + (i) * 4))
#define STAMP(i) do { STAMP_LDS(i) = (unsigned)__builtin_amdgcn_s_memrealtime(); } while (0)
#define STAMP_FLUSH() do { WAIT_L0(); if ((threadIdx.x & 63) < 16) *(GAS unsigned long long*)(ws + OFF_STAMP + ((blockIdx.x * 8 + (threadIdx.x >> 6)) * 16 + (threadIdx.x & 63)) * 8) = STAMP_LDS(threadIdx.x & 63); } while (0)

constexpr int kD = 640, kF = 1728, kQKV = 1920;
constexpr unsigned OFF_E   = 0;
constexpr unsigned OFF_F1  = 256;
constexpr unsigned OFF_FR  = OFF_F1 + 2048;
constexpr unsigned OFF_FK  = 2560;
constexpr unsigned OFF_F3  = OFF_FK + 4096;
constexpr unsigned OFF_FH  = OFF_F3 + 1024;
constexpr unsigned OFF_SC  = 16384;
constexpr unsigned OFF_SR  = OFF_SC + 15 * 10 * 384 * 4;
constexpr unsigned OFF_KVP = OFF_SR + 16 * 10 * 256 * 4;
constexpr unsigned OFF_MP  = OFF_KVP + 16 * 16 * 3456;
constexpr unsigned OFF_WD  = OFF_MP + 8 * 20 * 5 * 1024;
constexpr unsigned OFF_Q   = OFF_WD + 8 * 54 * 5 * 1024;
constexpr unsigned OFF_H   = OFF_Q + 32 * 20 * 4096;
constexpr unsigned WS_END  = OFF_H + 32 * 54 * 4096;
constexpr unsigned OOB = 0x80000000u;
static_assert(OFF_FH + 8192 <= OFF_SC && OFF_MP % 1024 == 0 && OFF_KVP % 128 == 0, "align");

constexpr int L1_B = 0, L1_BST = 24576;
constexpr int L1_A = 4 * L1_BST, L1_AST = 16384;
constexpr int L1_GR = L1_A + 3 * L1_AST;
constexpr int L1_GC = L1_A + L1_AST;
constexpr int L1_ST = 158720;
constexpr int L1_CS = L1_ST + 1024;
constexpr int L1_EX = 72 * 1024;
constexpr int L2_PF = 0, L2_BASE = 60 * 1024, L2_PA = 80 * 1024;
constexpr int L2_RED = 150 * 1024;
constexpr int L2_FLAG = 162816;
constexpr int LDS_SIZE = L2_FLAG + 64 + 512;
enum { LF_STATUS = 0, LF_DMA = 1, LF_K = 2, LF_BC = 3, LF_X = 4, LF_PUB = 8 };
enum { S_H = 1, S_Q = 2, S_M = 4 };

DEVI f32x4 mfma(u32x4 a, u32x4 b, f32x4 c) {
  return __builtin_amdgcn_mfma_f32_16x16x32_bf16(__builtin_bit_cast(bf16x8, a), __builtin_bit_cast(bf16x8, b), c, 0, 0, 0);
}
DEVI void mfma_acc(f32x4& c, u32x4 a, u32x4 b) { asm volatile("v_mfma_f32_16x16x32_bf16 %0, %1, %2, %0" : "+v"(c) : "v"(a), "v"(b)); }
DEVI void mfma_new(f32x4& c, u32x4 a, u32x4 b) { asm volatile("v_mfma_f32_16x16x32_bf16 %0, %1, %2, 0" : "=&v"(c) : "v"(a), "v"(b)); }
DEVI unsigned pk2(f32x2 f) { return __builtin_bit_cast(unsigned, __builtin_convertvector(f, bf16x2)); }
DEVI unsigned pk2(float lo, float hi) { return pk2(f32x2{lo, hi}); }
DEVI f32x2 unpk(unsigned u) { return f32x2{__builtin_bit_cast(float, u << 16), __builtin_bit_cast(float, u & 0xffff0000u)}; }
DEVI f32x2 fma2(f32x2 a, f32x2 b, f32x2 c) { return __builtin_elementwise_fma(a, b, c); }
DEVI f32x2 bc2(float v) { return f32x2{v, v}; }
DEVI f32x2 lo2(f32x4 v) { return f32x2{v[0], v[1]}; }
DEVI f32x2 hi2(f32x4 v) { return f32x2{v[2], v[3]}; }
DEVI f32x2 phi2(f32x2 g, f32x2 rs) {
  const f32x2 t = fma2(g, rs, bc2(1e-6f)), l = g * (rs * 1.4426950408889634f);
  return f32x2{fmaxf(t[0], 1e-6f), fmaxf(t[1], 1e-6f)} +
         f32x2{__builtin_amdgcn_fmed3f(__builtin_amdgcn_exp2f(l[0]), 0.f, 1.f), __builtin_amdgcn_fmed3f(__builtin_amdgcn_exp2f(l[1]), 0.f, 1.f)};
}
DEVI u32x4 lds_b128(unsigned a) { return *(LAS u32x4*)(size_t)a; }
DEVI u32x4 lds_trfrag(unsigned a) {
  const u32x2 lo = __builtin_bit_cast(u32x2, __builtin_amdgcn_ds_read_tr16_b64_v4i16((LAS s16x4*)(size_t)a));
  const u32x2 hi = __builtin_bit_cast(u32x2, __builtin_amdgcn_ds_read_tr16_b64_v4i16((LAS s16x4*)(size_t)(a + 512)));
  return u32x4{lo[0], lo[1], hi[0], hi[1]};
}
DEVI u32x4 srd(const void* p, unsigned bytes) {
  const u64 a = (u64)p;
  return u32x4{(unsigned)a, (unsigned)(a >> 32) & 0xffffu, bytes, 0x00020000u};
}
DEVI void setm0(unsigned v) { asm volatile("s_mov_b32 m0, %0" :: "s"(v) : "memory"); }
template <int IMM>
DEVI void dma_n(u32x4 r, unsigned voff, unsigned soff) {
  asm volatile("buffer_load_dwordx4 %0, %1, %2 offen offset:%3 lds" :: "v"(voff), "s"(r), "s"(soff), "i"(IMM) : "memory");
}
template <int IMM>
DEVI void dma(u32x4 r, unsigned voff, unsigned soff, unsigned lds) {
  asm volatile("s_mov_b32 m0, %4\n\ts_nop 0\n\tbuffer_load_dwordx4 %0, %1, %2 offen offset:%3 lds"
               :: "v"(voff), "s"(r), "s"(soff), "i"(IMM), "s"(lds - IMM) : "memory");
}
DEVI void dma_sc1(u32x4 r, unsigned voff, unsigned soff, unsigned lds) {
  asm volatile("s_mov_b32 m0, %3\n\ts_nop 0\n\tbuffer_load_dwordx4 %0, %1, %2 offen sc1 lds"
               :: "v"(voff), "s"(r), "s"(soff), "s"(lds) : "memory");
}
DEVI u32x2 ld8_hidden(u32x4 r, unsigned voff, unsigned soff) {
  u32x2 v;
  asm volatile("buffer_load_dwordx2 %0, %1, %2, %3 offen sc1" : "=v"(v) : "v"(voff), "s"(r), "s"(soff) : "memory");
  return v;
}
template <int N0, int N1>
DEVI void wd_prefetch(u32x4 rws, unsigned ldsb, int j, int w, int lane) {
#pragma unroll
  for (int n = N0; n < N1; ++n) {
    const unsigned a = (unsigned)(w + 8 * n) * 1024;
    if (n < 18 || w < 6) dma_sc1(rws, lane * 16, OFF_WD + j * 276480 + a, ldsb + a);
  }
}
DEVI void wait_flags(char* ws, unsigned off, u64 tag) {
#pragma unroll 1
  for (unsigned spins = 0; spins < (1u << 20); ++spins) {
    const u64 v = __hip_atomic_load((GAS u64*)(ws + off), RLX_AGENT);
    if (__all(v == tag)) break;
    __builtin_amdgcn_s_sleep(1);
  }
  asm volatile("" ::: "memory");
}
DEVI u64 make_tag(const char* ws) {
  return *(const __attribute__((address_space(4))) u64*)(ws + OFF_E) * 0x9E3779B97F4A7C15ull + 0xD1B54A32D192ED03ull;
}
DEVI void set_flag(char* ws, unsigned off, u64 tag) { __hip_atomic_store((GAS u64*)(ws + off), tag, RLX_AGENT); }
DEVI unsigned lds_inc(LAS unsigned* p, int lane) {
  unsigned o = 0;
  if (lane == 0) o = __hip_atomic_fetch_add(p, 1u, RLX_WG);
  return __builtin_amdgcn_readfirstlane(o);
}
DEVI void lds_wait_bits(LAS unsigned* p, unsigned bits) {
  while ((~(unsigned)__builtin_amdgcn_readfirstlane(__hip_atomic_load(p, RLX_WG)) & bits) != 0u) __builtin_amdgcn_s_sleep(1);
  asm volatile("" ::: "memory");
}
DEVI void lds_wait(LAS unsigned* p, unsigned want) {
  while ((unsigned)__builtin_amdgcn_readfirstlane(__hip_atomic_load(p, RLX_WG)) < want) __builtin_amdgcn_s_sleep(1);
  asm volatile("" ::: "memory");
}
DEVI u32x4 ld16(rsrc_t r, unsigned voff, unsigned soff) { return __builtin_amdgcn_raw_buffer_load_b128(r, voff, soff, SC1); }
DEVI void st16(rsrc_t r, unsigned voff, unsigned soff, u32x4 v) { __builtin_amdgcn_raw_buffer_store_b128(v, r, voff + soff, 0, SC1); }
DEVI void st8(rsrc_t r, unsigned voff, unsigned soff, u32x2 v) { __builtin_amdgcn_raw_buffer_store_b64(v, r, voff, soff, SC1); }

#define WARM 0
#define PRO_WAIT_(n) n
#define PRO_WAIT 8
constexpr unsigned CODE_BYTES = 56 * 1024;
DEVI void warm_up(const char* x, const char* wqkv, const char* wo, const char* wg, const char* wu, const char* wd,
                  const char* ws, const char* pc0, unsigned junk, int bid, int w, int lane) {
  const int cid = bid * 6 + w;
  const char* p = cid < 320 ? x : cid < 620 ? wqkv : cid < 890 ? wg : cid < 1160 ? wu : cid < 1430 ? wd : wo;
  unsigned o = (cid - (cid < 320 ? 0 : cid < 620 ? 320 : cid < 890 ? 620 : cid < 1160 ? 890 : cid < 1430 ? 1160 : 1430)) * 8192;
  unsigned n = (WARM & 1) && cid < 1530 ? o + 8192 : 0, sh = 7;
  if (w == 6) { p = ws; o = 0; n = (WARM & 2) ? WS_END : 0; sh = 18; }
  if (w == 7) { p = pc0; o = (bid >> 3) * 8192; n = (WARM & 4) ? CODE_BYTES : 0; }
  asm volatile("s_mov_b32 m0, %3\n\ts_nop 0\n\tbuffer_load_dword %0, %1, %2 offen lds"
               :: "v"(lane << sh), "s"(srd(p, n)), "s"(o), "s"(junk) : "memory");
}

struct P1 {
  u32x4 rw, rx;
  unsigned ldsb, pitch;
  unsigned voffB, voffA;
  unsigned oB, oA;
  int w, mw, nw, bstar;
  int Mt, cslot, j;
};

#define ABL_RD 0
#define AINC 1
template <int TYPE, bool LATE>
DEVI void p1_loop(P1& c, unsigned (&vb)[5], unsigned va0, unsigned foff, f32x4 (&acc)[4][6], char* ws, rsrc_t rws, u64 tag, int lane) {
  constexpr int NT = TYPE == 0 ? 6 : 5;
  constexpr bool AC = !LATE || AINC;
  u32x4 fa[4], fb[NT];
  unsigned va1 = va0 ^ 64;
  const unsigned stepB = 32 * c.pitch, limB = 640 * c.pitch;
  const unsigned lB0 = c.ldsb + L1_B + c.w * 3072, lA0 = c.ldsb + L1_A + c.w * 1024;
  unsigned lB = lB0 + 3 * L1_BST, lA = lA0 + 2 * L1_AST;
  unsigned vB1 = c.voffB - 896, vB2 = c.voffB - 1792, vA1 = c.voffA + 81920;
  PIN(vB1); PIN(vB2); PIN(vA1);
  int dA = L1_AST, dB = 2 * L1_BST, rare = c.bstar, rare2 = 1;
  f32x4 st[5];
  auto adB = [&](int i) { return TYPE == 0 ? vb[!LATE && i >= 2 ? 1 : 0] + (i / 3) * 12288 + (i % 3) * 1024 : vb[i < 5 ? i : 4]; };
  if (TYPE == 0) { vb[1] = vb[0]; PIN(vb[1]); }
  auto upB = [&](int i0, int i1) {
    if (TYPE == 0) { vb[i0 ? 1 : 0] += dB; PIN(vb[i0 ? 1 : 0]); }
    else {
#pragma unroll
      for (int i = i0; i < i1; ++i) { vb[i] += dB; PIN(vb[i]); }
    }
  };
  auto rdA = [&](int i, int sub) { if (ABL_RD & 1) { PIN(fa[i]); return; } fa[i] = lds_b128((sub ? va1 : va0) + i * 2048); };
  auto rdB = [&](int i, int sub) { if (ABL_RD & 2) { PIN(fb[i]); return; } fb[i] = lds_trfrag(adB(i) + sub * L1_BST); };
  auto rdBh = [&](int i, int hh, int sub) {
    if (ABL_RD & 2) { PIN(fb[i]); return; }
    const u32x2 v = __builtin_bit_cast(u32x2, __builtin_amdgcn_ds_read_tr16_b64_v4i16((LAS s16x4*)(size_t)(adB(i) + sub * L1_BST + hh * 512)));
    fb[i][2 * hh] = v[0]; fb[i][2 * hh + 1] = v[1];
  };
  auto pieceA = [&](int sub) { dma_n<0>(c.rx, sub ? vA1 : c.voffA, c.oA); };
  auto L = [&](int sub, int mode) {
    if (mode) dma_n<0>(c.rw, c.voffB, c.oB);
    SB(); rdA(0, sub); rdA(1, sub); SB();
    if (mode) dma_n<1024>(c.rw, vB1, c.oB);
    SB(); rdA(2, sub); rdA(3, sub); SB();
    if (mode) dma_n<2048>(c.rw, vB2, c.oB);
    SB(); rdB(LATE ? 4 : 0, sub); SB();
    if (mode == 2) setm0(lA + sub * 8192);
    SB(); if (!LATE) rdB(1, sub); else if (NT == 6) rdB(NT - 1, sub); else if (mode == 2) asm volatile("s_nop 0"); SB();
    if (mode == 2) pieceA(sub);
    if (!LATE && !sub) upB(2, 5);
    SB();
  };
  auto U = [&](int sub) {
    if (sub) { va1 += dA; PIN(va1); upB(0, LATE ? 5 : 2); }
    else { va0 += dA; PIN(va0); }
    SB();
  };
  auto stats = [&](int sub) {
    const u32x4 one = {0x3f803f80u, 0x3f803f80u, 0x3f803f80u, 0x3f803f80u};
    const u32x4 sa = lds_b128((sub ? va1 : va0) + c.nw * 2048);
    u32x4 sb[3];
#pragma unroll
    for (int i = 0; i < 3; ++i) { unsigned a = adB(LATE ? i : 3 + i); PIN(a); sb[i] = lds_trfrag(a + sub * L1_BST); }
    SB();
    if (sub == 0) {
#pragma unroll
      for (int i = 0; i < 5; ++i) st[i] = f32x4{0.f, 0.f, 0.f, 0.f};
    }
    st[0] = mfma(one, sa, st[0]);
    st[1] = mfma(sa, sa, st[1]);
#pragma unroll
    for (int i = 0; i < 3; ++i) st[2 + i] = mfma(one, sb[i], st[2 + i]);
    if (sub == 1) {
      const int cl = lane & 15, g = lane >> 4;
      const float sq = (cl & 2) ? ((cl & 1) ? st[1][3] : st[1][2]) : ((cl & 1) ? st[1][1] : st[1][0]);
      st8(rws, (cl >> 2) == g ? (c.mw * 64 + c.nw * 16 + cl) * 8 : OOB, c.cslot < 10 ? OFF_SR + (c.Mt * 10 + c.cslot) * 1024 : WS_END,
          u32x2{__builtin_bit_cast(unsigned, st[0][0]), __builtin_bit_cast(unsigned, sq)});
      const float v = g == 0 ? st[2][0] : g == 1 ? st[3][0] : st[4][0];
      __builtin_amdgcn_raw_buffer_store_b32(__builtin_bit_cast(unsigned, v), rws, g < 3 ? ((c.nw * 6 + c.mw * 3) * 16 + lane) * 4 : OOB,
                                            c.Mt < 10 ? OFF_SC + (c.cslot * 10 + c.Mt) * 1536 : WS_END, SC1);
    }
    SB();
  };
  unsigned tl = 0; int tcnt = 0;
#define SEG(i)
  auto C = [&](int sub, bool more, int book, auto vmwait) {
    SEG(0);
    if (LATE) { vmwait(); SEG(1); WAIT_L0(); BAR(); SEG(2); }
    __builtin_amdgcn_s_setprio(1);
    SB();
#pragma unroll
    for (int k = 0; k < 4 * NT; ++k) {
      const int i = k >> 2, m = k & 3;
      acc[m][i] = TYPE == 1 ? mfma(fa[m], fb[i], acc[m][i]) : mfma(fb[i], fa[m], acc[m][i]);
      if (LATE) { if (more && i >= 1 && i < 5 && m < 2) rdBh(i - 1, m, sub ^ 1); }
      else if (k < 2 * (NT - 2)) rdBh(2 + (k >> 1), k & 1, sub);
      const int q = LATE ? (k < 2 ? k : m >= 2 && i < 5 ? 2 * i + m : -1) : k - 2 * (NT - 2);
      constexpr int QA = LATE ? 8 : 0, QU = LATE ? 0 : 3;
      if (book == 1) {
        if (AC) {
          if (q == QA) setm0(lA + (LATE ? sub ^ 1 : sub) * 8192);
          if (q == QA + 1) pieceA(LATE ? sub ^ 1 : sub);
        }
        if (q == QU) { c.oB += stepB; lB += L1_BST; PINS(c.oB); PINS(lB); }
        if (q == QU + 1) { c.oB = c.oB == limB ? 0u : c.oB; PINS(c.oB); }
        if (q == QU + 2) { lB = lB == lB0 + 4 * L1_BST ? lB0 : lB; PINS(lB); }
        if (sub) {
          if (q == QU + 3) { c.oA += 128; lA += L1_AST; PINS(c.oA); PINS(lA); }
          if (q == QU + 4) { c.oA = c.oA == 1280u ? 0u : c.oA; PINS(c.oA); }
          if (q == QU + 5) { lA = lA == lA0 + 3 * L1_AST ? lA0 : lA; PINS(lA); }
          if (q == QU + 6) { dA = lA == lA0 + L1_AST ? -2 * L1_AST : L1_AST; PINS(dA); }
          if (q == QU + 7) { rare -= 1; PINS(rare); }
        }
        if (!sub && q == QU + 3) { rare2 = LATE ? (rare + 2) & ~2 : rare; PINS(rare2); }
        if (q == 11) setm0(lB);
      }
      if (book && q == 10 && (LATE ? sub : !sub)) { dB = -dB; PINS(dB); }
      SB();
    }
    __builtin_amdgcn_s_setprio(0);
    SEG(3);
    if (!LATE) { vmwait(); SEG(1); WAIT_L0(); BAR(); SEG(2); }
  };
  constexpr int LM = AC ? 1 : 2;
  if (!LATE) { upB(2, 5); dB = -dB; }
  WAIT_V(PRO_WAIT); BAR();
  if (ABL_RD) {
#pragma unroll
    for (int i = 0; i < 4; ++i) fa[i] = lds_b128(va0 + i * 2048);
#pragma unroll
    for (int i = 0; i < NT; ++i) fb[i] = lds_trfrag(c.ldsb + (lane & 63) * 8 + i * 1024);
  }
  if (LATE) {
    if (AC) { setm0(lA); SB(); }
    rdB(0, 0); rdB(1, 0); SB();
    if (AC) pieceA(0);
  }
  setm0(lB);
  SB();
  if (LATE) { rdB(2, 0); rdB(3, 0); } else asm volatile("s_nop 0");
  SB();
  const int rend = c.bstar - 8;
#pragma unroll 1
  do {
    L(0, LM);
    if (__builtin_expect(rare == 0, 0)) stats(0);
    U(0);
    C(0, true, 1, []() { WAIT_V(8); });
    L(1, LM);
    if (__builtin_expect(rare2 == 0, 0)) {
      PINS(rare);
      if (rare == 0) stats(1);
      else if (LATE && c.w == 0 && lane == 0) set_flag(ws, OFF_F1 + (c.Mt * 16 + c.cslot) * 8, tag);
    }
    U(1);
    C(1, true, 1, []() { WAIT_V(8); });
  } while (rare != rend);
  const u32x4 rw4 = srd(ws, WS_END);
  u32x2 fl = ld8_hidden(rw4, foff, 0);
  L(0, 1);
  U(0);
  C(0, true, 2, []() { WAIT_V(8); });
  L(1, 0);
  WAIT_V(3);
  PIN(fl);
  if (__builtin_expect(!__all(__builtin_bit_cast(u64, fl) == tag), 0)) wait_flags(ws, foff, tag);
  dma_sc1(rw4, lane * 16, OFF_SC + c.cslot * 15360 + c.w * 1024, c.ldsb + L1_GC + c.w * 1024);
  dma_sc1(rw4, lane * 16, OFF_SC + c.cslot * 15360 + c.w * 1024 + 8192, c.ldsb + L1_GC + c.w * 1024 + 8192);
  dma_sc1(rw4, lane * 16, OFF_SR + c.Mt * 10240 + c.w * 1024, c.ldsb + L1_GR + c.w * 1024);
  if (LATE && c.w < 2) dma_sc1(rw4, lane * 16, OFF_SR + c.Mt * 10240 + c.w * 1024 + 8192, c.ldsb + L1_GR + c.w * 1024 + 8192);
  U(1);
  C(1, true, 2, []() {});
  L(0, 0);
  wd_prefetch<0, 4>(rw4, c.ldsb, c.j, c.w, lane);
  C(0, true, 0, []() { WAIT_V(4); });
  L(1, 0);
  {
    __builtin_amdgcn_s_setprio(2);
    if (LATE || c.w < 6) {
      const LAS float* src = (const LAS float*)(size_t)(c.ldsb + L1_GC + (c.w * 64 + lane) * 4);
      float v[10];
#pragma unroll
      for (int p = 0; p < 10; ++p) v[p] = src[p * 384];
      *(LAS float*)(size_t)(c.ldsb + L1_CS + (c.w * 64 + lane) * 4) = (((v[0] + v[1]) + (v[2] + v[3])) + ((v[4] + v[5]) + (v[6] + v[7]))) + (v[8] + v[9]);
    } else {
      const LAS f32x2* src = (const LAS f32x2*)(size_t)(c.ldsb + L1_GR + ((c.w - 6) * 64 + lane) * 8);
      f32x2 v[10];
#pragma unroll
      for (int p = 0; p < 10; ++p) v[p] = src[p * 128];
      const f32x2 s = ((((v[0] + v[1]) + (v[2] + v[3])) + ((v[4] + v[5]) + (v[6] + v[7]))) + (v[8] + v[9])) * (1.0f / kD);
      LAS float* d = (LAS float*)(size_t)(c.ldsb + L1_ST + ((c.w - 6) * 64 + lane) * 4);
      d[0] = s[0]; d[128] = __builtin_amdgcn_rsqf(fmaxf(__builtin_fmaf(-s[0], s[0], s[1]), 0.f) + 1e-5f);
    }
    __builtin_amdgcn_s_setprio(0);
  }
  SB();
  wd_prefetch<4, 6>(rw4, c.ldsb, c.j, c.w, lane);
  SB();
  C(1, false, 0, []() {});
}

template <int N, class F> DEVI void static_for(F&& f) {
  if constexpr (N > 0) { static_for<N - 1>(f); f(std::integral_constant<int, N - 1>{}); }
}
DEVI u32x4 pair16(u32x2 a, u32x2 b) {
  const auto s0 = __builtin_amdgcn_permlane32_swap(a[0], b[0], false, false);
  const auto s1 = __builtin_amdgcn_permlane32_swap(a[1], b[1], false, false);
  return u32x4{s0[0], s1[0], s0[1], s1[1]};
}
struct Watch {
  LAS unsigned* lf; char* ws; u64 tag; int mt2, j, w, lane;
  unsigned status;
  DEVI bool poll(bool giveup) {
    int l2 = lane;
    PIN(l2);
    const unsigned f0 = l2 < 32 ? OFF_FH + mt2 * 256 + (j >> 2) * 128 + (l2 < 11 ? l2 : 0) * 8 : OFF_F3 + (blockIdx.x & 7) * 128 + (l2 & 15) * 8;
    const u64 no = __ballot(__hip_atomic_load((GAS u64*)(ws + f0), RLX_AGENT) != tag);
    unsigned s = ((no & 0x1ffull) ? 0u : S_H) | ((no & 0x600ull) ? 0u : S_Q) | ((no >> 32) ? 0u : S_M);
    if (giveup) s = 7u;
    if (s == status) return false;
    status = s;
    __hip_atomic_store(lf + LF_STATUS, s, RLX_WG);
    return true;
  }
  DEVI void need(unsigned bits) {
    if (w != 7) { lds_wait_bits(lf + LF_STATUS, bits); return; }
#pragma unroll 1
    for (unsigned spins = 0; (~status & bits) != 0u; ++spins)
      if (!poll(spins >= (1u << 20))) __builtin_amdgcn_s_sleep(1);
    asm volatile("" ::: "memory");
  }
};
DEVI void publish(Watch& wt, int cnt, unsigned flag) {
  if (wt.w == 7) wt.poll(false);
  WAIT_V(0);
  if (lds_inc(wt.lf + LF_PUB + cnt, wt.lane) == 3u && wt.lane < 2) set_flag(wt.ws, flag + wt.lane * 128, wt.tag);
}

struct PF2 {
  char* ws; rsrc_t rws; unsigned ldsb; int j, w, lane;
  u32x4 (&br)[3][5];
  template <int N> DEVI void item() {
    if constexpr (N < 13) wd_prefetch<6 + N, 7 + N>(srd(ws, WS_END), ldsb, j, w, lane);
    else br[(N - 13) / 5][(N - 13) % 5] = ld16(rws, lane * 16, OFF_WD + (j * 54 + 30 + w + 8 * ((N - 13) / 5)) * 5120 + ((N - 13) % 5) * 1024);
    SB();
  }
  template <int N0, int N1> DEVI void items() { if constexpr (N0 < N1) { item<N0>(); items<N0 + 1, N1>(); } }
};

DEVI void epi_ffn(LAS char* lds, f32x4 (&acc)[4][6], char* ws, rsrc_t rws, Watch& wt, const P1& c, PF2& pf, int lane) {
  const int r = lane & 15, g = lane >> 4;
  pf.items<0, 4>();
  float nm[4], k1[4], k2[4], iv[4];
#pragma unroll
  for (int mi = 0; mi < 4; ++mi) {
    const LAS float* sp = (const LAS float*)(lds + L1_ST) + c.mw * 64 + mi * 16 + r;
    const float rs = sp[128], c0 = -2.0f * 0.7978845608028654f * 1.4426950408889634f;
    nm[mi] = -sp[0]; k1[mi] = c0 * rs; k2[mi] = c0 * 0.044715f * rs * rs * rs; iv[mi] = __builtin_amdgcn_rcpf(rs * rs);
  }
  const unsigned hb = OFF_H + ((c.Mt * 2 + c.mw) * 54 + 2 * c.cslot + (c.nw >> 1)) * 4096 + (c.nw & 1) * 512 + (lane & 31) * 16 + (lane >> 5) * 1024;
  u32x4 v[6];
  static_for<3>([&](auto I) {
    constexpr int i = I;
    const f32x4 cg = *(LAS f32x4*)(lds + L1_CS + ((c.nw * 6 + i) * 16 + 4 * g) * 4);
    const f32x4 cu = *(LAS f32x4*)(lds + L1_CS + ((c.nw * 6 + 3 + i) * 16 + 4 * g) * 4);
    u32x2 o[4];
    static_for<4>([&](auto MI) {
      constexpr int mi = MI, n = 4 * i + mi;
      if constexpr (n < 10) pf.template items<4 + (12 * n) / 5, 4 + (12 * n + 12) / 5>();
#pragma unroll
      for (int hh = 0; hh < 2; ++hh) {
        const f32x2 G = fma2(bc2(nm[mi]), hh ? hi2(cg) : lo2(cg), hh ? hi2(acc[mi][i]) : lo2(acc[mi][i]));
        const f32x2 U = fma2(bc2(nm[mi]), hh ? hi2(cu) : lo2(cu), hh ? hi2(acc[mi][3 + i]) : lo2(acc[mi][3 + i]));
        const f32x2 z = G * fma2(G * G, bc2(k2[mi]), bc2(k1[mi]));
        const f32x2 d = fma2(f32x2{__builtin_amdgcn_exp2f(z[0]), __builtin_amdgcn_exp2f(z[1])}, bc2(iv[mi]), bc2(iv[mi]));
        o[mi][hh] = pk2(G * U * f32x2{__builtin_amdgcn_rcpf(d[0]), __builtin_amdgcn_rcpf(d[1])});
      }
      PIN(o[mi]);
      SB();
#define STN 10
#define STS 1
      if constexpr (n == STN || n == STN + STS) {
        constexpr int p = (n - STN) / STS;
        st16(rws, hb, p * 18 * 4096, v[2 * p]);
        st16(rws, hb, p * 18 * 4096 + 2048, v[2 * p + 1]);
        SB();
      }
    });
    v[2 * i] = pair16(o[0], o[1]); v[2 * i + 1] = pair16(o[2], o[3]);
    PIN(v[2 * i]); PIN(v[2 * i + 1]);
  });
  st16(rws, hb, 36 * 4096, v[4]);
  st16(rws, hb, 36 * 4096 + 2048, v[5]);
  STAMP(4);
  publish(wt, c.mw, OFF_FH + (c.Mt * 2 + c.mw) * 256 + c.cslot * 8);
  lds_inc((LAS unsigned*)(lds + L2_FLAG) + LF_DMA, lane);
}

DEVI void epi_q(LAS char* lds, f32x4 (&acc)[4][6], char* ws, rsrc_t rws, Watch& wt, const P1& c, PF2& pf, int lane) {
  const int r = lane & 15, g = lane >> 4, qi = c.cslot - 13;
  f32x4 cs[5];
#pragma unroll
  for (int i = 0; i < 5; ++i) cs[i] = *(LAS f32x4*)(lds + L1_CS + ((c.nw * 6 + i) * 16 + 4 * g) * 4);
  pf.items<0, 8>();
  static_for<4>([&](auto MI) {
    constexpr int mi = MI;
    const LAS float* sp = (const LAS float*)(lds + L1_ST) + c.mw * 64 + mi * 16 + r;
    const f32x2 nm = bc2(-sp[0]), rs = bc2(sp[128]);
    static_for<5>([&](auto I) {
      constexpr int i = I;
      const f32x2 a = phi2(fma2(nm, lo2(cs[i]), lo2(acc[mi][i])), rs), b = phi2(fma2(nm, hi2(cs[i]), hi2(acc[mi][i])), rs);
      acc[mi][i] = f32x4{a[0], a[1], b[0], b[1]};
      PIN(acc[mi][i]);
      SB();
      pf.template item<8 + mi * 5 + i>();
    });
  });
  STAMP(3);
  LAS unsigned* lf = (LAS unsigned*)(lds + L2_FLAG);
  if (c.w == 4) { wait_flags(ws, OFF_FK + (blockIdx.x & 7) * 512 + qi * 256 + (lane & 31) * 8, wt.tag); lds_inc(lf + LF_K, lane); }
  else lds_wait(lf + LF_K, 1u);
  STAMP(12);
  {
    const int q4 = (c.w & 1) * 64 + lane, pr = q4 / 10;
    f32x4 part[4];
#pragma unroll
    for (int p = 0; p < 4; ++p)
      part[p] = __builtin_bit_cast(f32x4, ld16(rws, q4 < 80 ? pr * 55296 + (q4 - pr * 10) * 16 : OOB, OFF_KVP + 3200 + (qi * 128 + (c.w >> 1) * 4 + p) * 3456));
    SB();
    if (q4 < 80) *(LAS f32x4*)(lds + L2_RED + (c.w >> 1) * 1280 + q4 * 16) = (part[0] + part[1]) + (part[2] + part[3]);
    WAIT_L0(); BAR();
  }
  STAMP(13);
  f32x4 z[5];
#pragma unroll
  for (int i = 0; i < 5; ++i) {
    const LAS char* p = lds + L2_RED + (c.nw * 80 + 20 * (2 * (g & 1) + (g >> 1)) + 4 * i) * 4;
    z[i] = (*(const LAS f32x4*)p + *(const LAS f32x4*)(p + 1280)) + (*(const LAS f32x4*)(p + 2560) + *(const LAS f32x4*)(p + 3840));
  }
  const unsigned qb = OFF_Q + ((c.Mt * 2 + c.mw) * 20 + (2 * qi + (c.nw >> 1)) * 5) * 4096;
  u32x2 keep;
#pragma unroll
  for (int mi = 0; mi < 4; ++mi) {
    f32x2 d0 = lo2(acc[mi][0]) * lo2(z[0]), d1 = hi2(acc[mi][0]) * hi2(z[0]);
#pragma unroll
    for (int i = 1; i < 5; ++i) { d0 = fma2(lo2(acc[mi][i]), lo2(z[i]), d0); d1 = fma2(hi2(acc[mi][i]), hi2(z[i]), d1); }
    d0 += d1;
    const f32x2 inv = bc2(__builtin_amdgcn_rcpf(d0[0] + d0[1] + 1e-6f));
    unsigned o[10];
#pragma unroll
    for (int i = 0; i < 5; ++i) { o[2 * i] = pk2(lo2(acc[mi][i]) * inv); o[2 * i + 1] = pk2(hi2(acc[mi][i]) * inv); }
    st16(rws, lane * 16 + mi * 1024, qb + (c.nw & 1) * 12288, u32x4{o[0], o[1], o[2], o[3]});
    st16(rws, lane * 16 + mi * 1024, qb + (c.nw & 1) * 12288 + 4096, u32x4{o[4], o[5], o[6], o[7]});
    if (mi & 1) st16(rws, (lane & 31) * 16 + (lane >> 5) * 1024 + (mi - 1) * 1024, qb + 8192 + (c.nw & 1) * 512, pair16(keep, u32x2{o[8], o[9]}));
    else keep = u32x2{o[8], o[9]};
  }
  STAMP(4);
  publish(wt, c.mw, OFF_FH + (c.Mt * 2 + c.mw) * 256 + (9 + qi) * 8);
  lds_inc(lf + LF_DMA, lane);
}

DEVI void epi_kv(LAS char* lds, f32x4 (&acc)[4][6], rsrc_t rws, const P1& c, int lane) {
  const int cl = lane & 15, g = lane >> 4;
  float cs[5];
#pragma unroll
  for (int i = 0; i < 5; ++i) cs[i] = *(LAS float*)(lds + L1_CS + ((c.nw * 6 + i) * 16 + cl) * 4);
  u32x4 fr[2][6];
#pragma unroll
  for (int mi = 0; mi < 4; ++mi) {
    const f32x4 mean = *(LAS f32x4*)(lds + L1_ST + (c.mw * 64 + mi * 16 + 4 * g) * 4);
    const f32x4 rstd = *(LAS f32x4*)(lds + L1_ST + 512 + (c.mw * 64 + mi * 16 + 4 * g) * 4);
#pragma unroll
    for (int i = 0; i < 5; ++i) {
#pragma unroll
      for (int hh = 0; hh < 2; ++hh) {
        const f32x2 rs = hh ? hi2(rstd) : lo2(rstd);
        const f32x2 t = fma2(hh ? hi2(mean) : lo2(mean), bc2(-cs[i]), hh ? hi2(acc[mi][i]) : lo2(acc[mi][i]));
        fr[mi >> 1][i][(mi & 1) * 2 + hh] = pk2((i == 2 || i == 3) ? t * rs : phi2(t, rs));
        if (i == 4) fr[mi >> 1][5][(mi & 1) * 2 + hh] = cl < 8 ? 0x3f803f80u : pk2(t * rs);
      }
    }
  }
  constexpr int KI[7] = {0, 0, 4, 1, 1, 4, 4}, VI[7] = {2, 5, 2, 3, 5, 3, 5};
  f32x4 p[7];
#pragma unroll
  for (int q = 0; q < 7; ++q) {
    p[q] = mfma(fr[0][VI[q]], fr[0][KI[q]], f32x4{0.f, 0.f, 0.f, 0.f});
    p[q] = mfma(fr[1][VI[q]], fr[1][KI[q]], p[q]);
  }
  LAS char* ex = lds + L1_EX + c.w * 3584;
  const int uh = cl < 4 ? 16 + cl : 32 + cl;
  auto put = [&](bool ok, int u, int e, f32x4 v) { *(LAS f32x4*)(ex + (ok ? (u * 20 + e) * 4 : 3360)) = v; };
  auto putz = [&](bool ok, int u, float v) { *(LAS float*)(ex + (ok ? 3200 + u * 4 : 3360)) = v; };
  put(true, cl, 4 * g, p[0]);
  put(true, 20 + cl, 4 * g, p[3]);
  put(g >= 2, (g & 1) * 20 + cl, 16, g == 2 ? p[1] : p[4]);
  put(cl < 8, uh, 4 * g, cl < 4 ? p[2] : p[5]);
  put(cl < 8 && g == 2 + (cl >> 2), uh, 16, p[6]);
  putz(g < 2, g * 20 + cl, g == 0 ? p[1][0] : p[4][0]);
  putz(g == 0 && cl < 8, uh, p[6][0]);
  WAIT_L0(); BAR();
#pragma unroll
  for (int q = 0; q < 2; ++q) {
    const int o = (c.mw * 128 + q * 64 + lane) * 16;
    const LAS char* src = lds + L1_EX + c.nw * 3584 + (o < 3360 ? o : 0);
    const f32x4 v = *(const LAS f32x4*)src + *(const LAS f32x4*)(src + 4 * 3584);
    st16(rws, o < 3360 ? o : OOB, OFF_KVP + (((c.cslot - 9) * 4 + c.nw) * 16 + c.Mt) * 3456, __builtin_bit_cast(u32x4, v));
  }
}

DEVI void retile(const char* wd, rsrc_t rws, unsigned ldsw, int id, int w, int lane) {
  const u32x4 rd = srd(wd, kF * 1280);
  const int jj = lane >> 3, g = lane >> 4;
  const unsigned vo = ((jj >> 2) * 8 + (jj & 3)) * 1280 + (lane & 7) * 16;
  const unsigned ra = ldsw + g * 1024 + ((lane & 15) >> 2) * 128 + (lane & 3) * 8;
  auto fetch = [&](int n) {
    const int it = id * 8 + w + n * 128, rb = it / 10, ln = it - rb * 10;
#pragma unroll
    for (int q = 0; q < 4; ++q)
      dma<0>(rd, vo, it < 540 ? (rb * 32 + 16 * (q >> 1) + 4 * (q & 1)) * 1280 + ln * 128 : OOB, ldsw + (n & 3) * 4096 + q * 1024);
  };
  auto put = [&](int n) {
    const int it = id * 8 + w + n * 128, rb = it / 10, ln = it - rb * 10, cg = rb / 6, uu = rb - cg * 6;
    const int ks = 18 * (uu >> 1) + 2 * cg + (uu & 1);
#pragma unroll
    for (int q = 0; q < 4; ++q) {
      const int nt = ln * 4 + q, j = nt / 5, t = nt - j * 5;
      st16(rws, lane * 16, it < 540 ? OFF_WD + ((j * 54 + ks) * 5 + t) * 1024 : OOB, lds_trfrag(ra + (n & 3) * 4096 + q * 32));
    }
  };
#pragma unroll 1
  for (int n = 0; n < 4; ++n) fetch(n);
  WAIT_V(0);
  put(0);
  WAIT_L0();
  fetch(4);
#pragma unroll 1
  for (int n = 1; n < 5; ++n) {
    if (n == 4) WAIT_V(0);
    put(n);
  }
}

DEVI void fold_kv(LAS char* lds, char* ws, rsrc_t rws, Watch& wt, const u32x4 (&wf)[2][5], int hp, int w, int lane) {
  const u64 tag = wt.tag;
  const int cl = lane & 15, g = lane >> 4;
  const int o = ((w & 3) * 64 + lane) * 16;
  unsigned po = o < 3200 ? o : OOB, ko[6][2];
#pragma unroll
  for (int f = 0; f < 6; ++f) {
    const int u = 20 * (f & 1) + (f < 4 ? (cl & 4) * 2 + 4 * (f >> 1) : 16) + (cl & 3);
    const bool mine = f >= 4 || (cl >> 3) == (f & 1);
    ko[f][0] = (unsigned)(size_t)lds + L2_RED + (mine && g < 3 ? u * 80 + g * 32 : 3360);
    ko[f][1] = (unsigned)(size_t)lds + L2_RED + (mine && g < 2 ? u * 80 + g * 32 + 16 : 3360);
    PIN(ko[f][0]); PIN(ko[f][1]);
  }
  unsigned vo = OFF_MP + (w * 20 + (hp >> 2) * 5) * 5120 + (3 * ((hp >> 1) & 1) + (g & 1)) * 5120 + (((hp & 1) + (g & 2)) * 16 + cl) * 16;
  unsigned vo2 = OFF_MP + (w * 20 + (hp >> 2) * 5 + 2) * 5120 + g * 1024 + ((hp & 3) * 16 + cl) * 16;
  PIN(po); PIN(vo); PIN(vo2);
  WAIT_V(0);
  if (w == 0) wait_flags(ws, OFF_FK + (blockIdx.x & 7) * 512 + (hp >> 2) * 128 + cl * 8, tag);
  BAR();
  STAMP(2);
  {
    f32x4 part[8];
#pragma unroll
    for (int p = 0; p < 8; ++p) part[p] = __builtin_bit_cast(f32x4, ld16(rws, po, OFF_KVP + (hp * 16 + (w >> 2) * 8 + p) * 3456));
    SB();
    *(LAS f32x4*)(lds + L2_RED + (w >> 2) * 4096 + o) = ((part[0] + part[1]) + (part[2] + part[3])) + ((part[4] + part[5]) + (part[6] + part[7]));
    WAIT_L0(); BAR();
  }
  STAMP(12);
  u32x4 kf[6];
#pragma unroll
  for (int f = 0; f < 6; ++f) {
    const f32x4 lo = *(LAS f32x4*)(size_t)ko[f][0] + *(LAS f32x4*)(size_t)(ko[f][0] + 4096);
    const f32x4 hi = *(LAS f32x4*)(size_t)ko[f][1] + *(LAS f32x4*)(size_t)(ko[f][1] + 4096);
    kf[f] = u32x4{pk2(lo2(lo)), pk2(hi2(lo)), pk2(lo2(hi)), pk2(hi2(hi))};
  }
  const f32x4 zero = {0.f, 0.f, 0.f, 0.f};
  u32x4 r1;
#pragma unroll
  for (int i = 0; i < 5; ++i) {
    const f32x4 d1 = mfma(kf[1], wf[1][i], mfma(kf[0], wf[0][i], zero)), d2 = mfma(kf[3], wf[1][i], mfma(kf[2], wf[0][i], zero));
    const f32x4 d3 = mfma(kf[4], wf[0][i], zero), d4 = mfma(kf[5], wf[1][i], zero);
    st16(rws, vo, i * 1024, u32x4{pk2(lo2(d1)), pk2(hi2(d1)), pk2(lo2(d2)), pk2(hi2(d2))});
    const u32x4 t = {pk2(lo2(d3)), pk2(hi2(d3)), pk2(lo2(d4)), pk2(hi2(d4))};
    if (i == 0 || i == 4) r1 = t; else r1 = g == i ? t : r1;
    if (i == 3) st16(rws, vo2, 0, r1);
    if (i == 4) st16(rws, g == 0 ? vo2 + 4096 : OOB, 0, r1);
  }
  STAMP(3);
  if (w == 7) wt.poll(false);
  WAIT_V(0);
  if (lds_inc(wt.lf + LF_PUB, lane) == 7u && lane < 8) set_flag(ws, OFF_F3 + lane * 128 + hp * 8, tag);
  STAMP(13);
}

DEVI void phase2(LAS char* lds, const char* x, char* ws, rsrc_t rws, char* out, Watch& wt, const u32x4 (&br)[3][5],
                 int mt2, int j, int w, int lane) {
  const int a = w & 3;
  LAS unsigned* lf = wt.lf;
  const unsigned obase = (mt2 * 64 + a * 16) * 1280 + j * 160;
  f32x4 acc[4][5];
  if (w >= 4) __builtin_amdgcn_s_setprio(2);
  {
    const rsrc_t rx = __builtin_amdgcn_make_buffer_rsrc((void*)x, 0, 2048 * 1280, 0x00020000);
    u32x2 xr[5];
#pragma unroll
    for (int t = 0; t < 5; ++t)
      xr[t] = __builtin_amdgcn_raw_buffer_load_b64(rx, (lane & 15) * 1280 + (lane >> 4) * 8 + t * 32, w >= 4 ? obase : OOB, 0);
    SB();
    wt.need(S_H);
    lds_wait(lf + LF_DMA, 8u);
    STAMP(6);
    const unsigned ab = OFF_H + (mt2 * 54 + w) * 4096;
    u32x4 fa[7][4], fb[4][5];
    auto ldA = [&](int n) {
      const unsigned so = n == 6 && w >= 6 ? OOB : ab + (n < 3 ? 30 + 8 * n : 8 * n - 24) * 4096;
#pragma unroll
      for (int m = 0; m < 4; ++m) fa[n][m] = ld16(rws, lane * 16, so + ((m + w) & 3) * 1024);
      SB();
    };
    unsigned lb = (unsigned)(size_t)lds + w * 5120 + lane * 16;
    auto rdB = [&](int n) {
      if (n == 2) { lb += 81920; PIN(lb); }
#pragma unroll
      for (int t = 0; t < 5; ++t) fb[n][t] = lds_b128(lb + (n & 1) * 40960 + t * 1024);
      SB();
    };
#pragma unroll
    for (int n = 0; n < 6; ++n) ldA(n);
#pragma unroll
    for (int t = 0; t < 5; ++t) {
      const f32x2 x0 = unpk(xr[t][0]), x1 = unpk(xr[t][1]);
      acc[0][t] = f32x4{x0[0], x0[1], x1[0], x1[1]};
      PIN(acc[0][t]);
    }
    SB();
#pragma unroll
    for (int n = 0; n < 7; ++n) {
      if (n >= 2 && n < 6) rdB(n - 2);
      if (n != 6 || w < 6) {
#pragma unroll
        for (int m = 0; m < 4; ++m)
#pragma unroll
          for (int t = 0; t < 5; ++t) {
            if (n == 0 && m > 0) mfma_new(acc[m][t], br[0][t], fa[0][m]);
            else mfma_acc(acc[m][t], n < 3 ? br[n][t] : fb[n - 3][t], fa[n][m]);
          }
      }
      SB();
      if (n == 0) { STAMP(9); ldA(6); }
      if (n == 5) { WAIT_L0(); lds_inc(lf + LF_BC, lane); }
    }
  }
  STAMP(11);
  if (w < 4) __builtin_amdgcn_s_setprio(3);
  if (w >= 4) {
    wt.need(S_Q);
    u32x4 pq[5][4], mf[5][5];
#pragma unroll
    for (int s = 0; s < 5; ++s) {
#pragma unroll
      for (int m = 0; m < 4; ++m) pq[s][m] = ld16(rws, lane * 16, OFF_Q + (mt2 * 20 + a * 5 + s) * 4096 + ((m + w) & 3) * 1024);
      SB();
    }
    wt.need(S_M);
    STAMP(7);
    const unsigned mo = OFF_MP + (j * 20 + a * 5) * 5120;
    auto ldM = [&](int s) {
#pragma unroll
      for (int t = 0; t < 5; ++t) mf[s][t] = ld16(rws, lane * 16, mo + s * 5120 + t * 1024);
      SB();
    };
#pragma unroll
    for (int s = 0; s < 4; ++s) ldM(s);
    __builtin_amdgcn_s_setprio(0);
#pragma unroll
    for (int s = 0; s < 5; ++s) {
#pragma unroll
      for (int t = 0; t < 5; ++t)
#pragma unroll
        for (int m = 0; m < 4; ++m) mfma_acc(acc[m][t], mf[s][t], pq[s][m]);
      SB();
      if (s == 0) ldM(4);
    }
  }
  asm volatile("s_nop 7" ::: "memory");
  SB();
  STAMP(14);
  LAS char* ex = lds + (w >= 4 ? L2_PA : L2_PF) + lane * 16;
  lds_wait(lf + LF_BC, 8u);
#pragma unroll
  for (int m = 1; m < 4; ++m)
#pragma unroll
    for (int t = 0; t < 5; ++t) *(LAS f32x4*)(ex + (a * 15 + (m - 1) * 5 + t) * 1024) = acc[m][t];
  WAIT_L0();
  if (w < 4) { lds_inc(lf + LF_X, lane); lds_wait(lf + LF_X, 4u); }
  else BAR();
  STAMP(8);
#pragma unroll
  for (int t = 0; t < 5; ++t)
#pragma unroll
    for (int q = 1; q < 4; ++q) acc[0][t] += *(LAS f32x4*)(ex + (((a - q) & 3) * 15 + (q - 1) * 5 + t) * 1024);
  LAS char* base = lds + L2_BASE + a * 5120 + lane * 16;
  if (w < 4) {
#pragma unroll
    for (int t = 0; t < 5; ++t) *(LAS f32x4*)(base + t * 1024) = acc[0][t];
    WAIT_L0();
  } else {
    const rsrc_t ro = __builtin_amdgcn_make_buffer_rsrc(out, 0, 2048 * 1280, 0x00020000);
#pragma unroll
    for (int t = 0; t < 5; ++t) {
      const f32x4 s = acc[0][t] + *(LAS f32x4*)(base + t * 1024);
      __builtin_amdgcn_raw_buffer_store_b64(u32x2{pk2(lo2(s)), pk2(hi2(s))}, ro, (lane & 15) * 1280 + (lane >> 4) * 8 + t * 32, obase, 0);
    }
  }
}

__global__ __launch_bounds__(512, 2) void fused(const char* x, const char* wqkv, const char* wo, const char* wg,
                                                const char* wu, const char* wd, char* ws, char* out) {
  extern __shared__ __attribute__((aligned(16))) char smem[];
  unsigned long long tstart_;
  asm volatile("s_memrealtime %0" : "=s"(tstart_));
  const char* pc0;
  asm volatile("s_getpc_b64 %0" : "=s"(pc0));
  asm volatile("" : "+s"(x), "+s"(wqkv), "+s"(wo), "+s"(wg), "+s"(wu), "+s"(wd), "+s"(ws), "+s"(out));
  LAS char* lds = (LAS char*)smem;
  const unsigned ldsb = (unsigned)(size_t)lds;
  const int bid = blockIdx.x, xcd = bid & 7, idx = bid >> 3;
  const int Mt = (xcd & 1) * 8 + (idx & 7), grp = xcd >> 1, pos4 = idx >> 3, slot2 = grp * 4 + pos4;
  const int cslot = (int)(0xCF76BE54AD329810ull >> (4 * slot2)) & 15;
  const int w = __builtin_amdgcn_readfirstlane(threadIdx.x >> 6), lane = threadIdx.x & 63;
  STAMP(0);
  const rsrc_t rws = __builtin_amdgcn_make_buffer_rsrc(ws, 0, WS_END, 0x00020000);
  u64 tag;
  Watch wt{(LAS unsigned*)(lds + L2_FLAG), ws, 0ull, Mt * 2 + (slot2 >> 3), slot2 & 7, w, lane, 0u};
  u32x4 br[3][5];
  PF2 pf{ws, rws, ldsb, slot2 & 7, w, lane, br};
  if (cslot == 15) {
    tag = make_tag(ws);
    PINS(tag);
    wt.tag = tag;
    if (threadIdx.x < 16) ((LAS unsigned*)(lds + L2_FLAG))[threadIdx.x] = 0u;
    if (WARM) warm_up(x, wqkv, wo, wg, wu, wd, ws, pc0, ldsb + L1_GR + 8192, bid, w, lane);
    retile(wd, rws, ldsb + w * 16384, Mt, w, lane);
    WAIT_V(0); WAIT_L0(); BAR();
    if (threadIdx.x == 0) { set_flag(ws, OFF_FR + Mt * 8, tag); set_flag(ws, OFF_F1 + (Mt * 16 + 15) * 8, tag); }
    STAMP(5);
    u32x4 wf[2][5];
    {
      const u32x4 ro = srd(wo, kD * 1280);
#pragma unroll 1
      for (int q = 0; q < 10; ++q)
        dma<0>(ro, (lane >> 3) * 1280 + (lane & 7) * 16, (Mt * 40 + (q >> 1) * 8) * 1280 + ((w * 160) & ~127) + (q & 1) * 128, ldsb + w * 16384 + q * 1024);
      WAIT_V(0);
#pragma unroll
      for (int hh = 0; hh < 2; ++hh) {
        unsigned ra[2];
#pragma unroll
        for (int h2 = 0; h2 < 2; ++h2) {
          int row = hh * 20 + (lane >> 4) * 8 + h2 * 4 + ((lane & 15) >> 2);
          row = row < 40 ? row : 39;
          ra[h2] = ldsb + w * 16384 + (row >> 3) * 2048 + (row & 7) * 128 + (lane & 3) * 8;
        }
#pragma unroll
        for (int i = 0; i < 5; ++i) {
          const int cb = (w & 3) * 32 + i * 32, o = (cb >> 7) * 1024 + (cb & 127);
          const u32x2 lo = __builtin_bit_cast(u32x2, __builtin_amdgcn_ds_read_tr16_b64_v4i16((LAS s16x4*)(size_t)(ra[0] + o)));
          const u32x2 hi = __builtin_bit_cast(u32x2, __builtin_amdgcn_ds_read_tr16_b64_v4i16((LAS s16x4*)(size_t)(ra[1] + o)));
          wf[hh][i] = u32x4{lo[0], lo[1], hi[0], hi[1]};
        }
      }
    }
    wait_flags(ws, OFF_FR + (lane & 15) * 8, tag);
    WAIT_L0(); BAR();
    wd_prefetch<0, 6>(srd(ws, WS_END), ldsb, slot2 & 7, w, lane);
    pf.items<0, 28>();
    if (Mt == 0 && w == 7) {
#pragma unroll 1
      for (int q = 0; q < 4; ++q) wait_flags(ws, OFF_F1 + (q * 64 + lane) * 8, tag);
      if (lane == 0) set_flag(ws, OFF_E, tag);
    }
    fold_kv(lds, ws, rws, wt, wf, Mt, w, lane);
    lds_inc((LAS unsigned*)(lds + L2_FLAG) + LF_DMA, lane);
  } else {
    const int type = cslot < 9 ? 0 : cslot < 13 ? 1 : 2;
    P1 c;
    c.w = w; c.mw = w >> 2; c.nw = w & 3; c.Mt = Mt; c.cslot = cslot; c.j = slot2 & 7;
    c.ldsb = ldsb;
    {
#define ROTM 1
      const int kb = (Mt + cslot) % 10, rot = ROTM == 0 ? (kb >= 5 ? 5 : 0) : ROTM == 1 ? (kb > 7 ? 7 : kb) : (kb / 3) * 3 - (kb == 9 ? 3 : 0);
      c.bstar = kb - rot;
      const int seg = w >> 2;
      c.pitch = type == 0 ? kF * 2 : kQKV * 2;
      const int startb = type == 0 ? cslot * 384
                       : ((type == 1 ? (640 * (1 + seg) + 160 * (cslot - 9)) * 2 : 320 * (2 * (cslot - 13) + seg)) & ~127);
      const char* wb = type == 0 ? (seg ? wu : wg) : wqkv;
      c.rw = srd(wb + startb - 2048, 640 * c.pitch - startb + 2048);
      c.rx = srd(x + (size_t)Mt * 128 * 1280, 128 * 1280);
      const int i = lane >> 3, cp = lane & 7, pg = w & 3;
      c.voffB = (unsigned)(8 * pg + i) * c.pitch + 16 * (cp ^ ((i & 2) | ((pg & 1) << 2))) + 2048;
      c.voffA = (unsigned)(8 * w + i) * 1280 + 16 * (cp ^ ((4 * w + (i >> 1)) & 7));
      c.oB = rot * 64 * c.pitch; c.oA = rot * 128;
      const unsigned lB = c.ldsb + L1_B + w * 3072, lA = c.ldsb + L1_A + w * 1024;
      dma<0>(c.rx, c.voffA, c.oA, lA);
#pragma unroll
      for (int s = 0; s < 3; ++s) {
        dma<0>(c.rw, c.voffB, c.oB, lB + s * L1_BST);
        dma<128>(c.rw, c.voffB, c.oB, lB + s * L1_BST + 1024);
        dma<256>(c.rw, c.voffB, c.oB, lB + s * L1_BST + 2048);
        c.oB += 32 * c.pitch;
        dma<0>(c.rx, c.voffA, c.oA + ((s + 1) >> 1) * 128 + ((s + 1) & 1) * 81920, lA + (s + 1) * 8192);
      }
      c.oA += 256;
    }
    SB();
    if (WARM & 8) BAR();
    if (WARM) warm_up(x, wqkv, wo, wg, wu, wd, ws, pc0, ldsb + L1_GR + 8192, bid, w, lane);
    SB();
    tag = make_tag(ws);
    PINS(tag);
    wt.tag = tag;
    unsigned vb[5], va0;
    {
      const int g = lane >> 4, q = (lane & 15) >> 2, p = lane & 3, r = lane & 15, nw = c.nw;
      const int swz = (q & 2) | ((g & 1) << 2);
#pragma unroll
      for (int pos = 0; pos < 5; ++pos) {
        const int cf = 16 * nw + 4 * p;
        const int hb = (cslot & 1 ? 0 : 32) + 40 * nw;
        const int ck = pos < 4 ? (pos >> 1) * 192 + hb + 20 * (pos & 1) + 4 * p : (p >> 1) * 192 + hb + 20 * (p & 1) + 16;
        const int cq = (nw >> 1) * 224 + 20 * (4 * (nw & 1) + 2 * (p & 1) + (p >> 1)) + 4 * pos;
        const int col = type == 0 ? cf : type == 1 ? ck : cq;
        const int line = col >> 6, seg = line >= 3;
        vb[pos] = c.ldsb + L1_B + seg * (12288 - 3072) + (g * 3 + line) * 1024 + q * 128 + 16 * (((col & 63) >> 3) ^ swz) + 8 * ((col >> 2) & 1);
      }
      va0 = c.ldsb + L1_A + (c.mw * 64 + r) * 128 + 16 * (g ^ ((r >> 1) & 7));
    }
    if (threadIdx.x < 16) ((LAS unsigned*)(lds + L2_FLAG))[threadIdx.x] = 0u;
    f32x4 acc[4][6];
#pragma unroll
    for (int m = 0; m < 4; ++m)
#pragma unroll
      for (int i = 0; i < 6; ++i) acc[m][i] = f32x4{0.f, 0.f, 0.f, 0.f};
    unsigned foff = lane >= 20 ? OFF_FR + (lane & 15) * 8 : OFF_F1 + (lane < 10 ? lane * 16 + cslot : Mt * 16 + lane - 10) * 8;
    PIN(foff);
    WAIT_V(PRO_WAIT);
    unsigned long long t1_ = __builtin_amdgcn_s_memrealtime(), c1_ = __builtin_amdgcn_s_memtime();
    __builtin_amdgcn_s_waitcnt(0xC07F);
#define P1_STAMP do { STAMP(2); STAMP_LDS(1) = (unsigned)t1_; STAMP_LDS(15) = (unsigned)(__builtin_amdgcn_s_memtime() - c1_); STAMP(3); } while (0)
#define P1_RUN(T) do { if (w < 4) p1_loop<T, true>(c, vb, va0, foff, acc, ws, rws, tag, lane); \
                       else p1_loop<T, false>(c, vb, va0, foff, acc, ws, rws, tag, lane); P1_STAMP; } while (0)
    if (type == 0) { P1_RUN(0); epi_ffn(lds, acc, ws, rws, wt, c, pf, lane); STAMP(5); }
    else if (type == 2) { P1_RUN(2); epi_q(lds, acc, ws, rws, wt, c, pf, lane); STAMP(5); }
    else {
      P1_RUN(1);
      epi_kv(lds, acc, rws, c, lane);
      STAMP(4);
      WAIT_V(0); WAIT_L0(); BAR();
      if (threadIdx.x < 8) set_flag(ws, OFF_FK + threadIdx.x * 512 + (cslot - 9) * 128 + Mt * 8, tag);
      STAMP(5);
      pf.items<0, 28>();
      WAIT_V(15);
      lds_inc((LAS unsigned*)(lds + L2_FLAG) + LF_DMA, lane);
    }
  }
  phase2(lds, x, ws, rws, out, wt, br, Mt * 2 + (slot2 >> 3), slot2 & 7, w, lane);
  WAIT_V(0);
  STAMP(10);
  WAIT_L0();
  if (blockIdx.x == 0 && w == 4 && DIAG >= 0) {
    const unsigned d = 4u * (__builtin_amdgcn_readfirstlane(STAMP_LDS(DIAG < 0 ? 0 : DIAG)) - (unsigned)tstart_);
    const unsigned t1 = (unsigned)__builtin_amdgcn_s_memrealtime();
    while ((unsigned)__builtin_amdgcn_s_memrealtime() - t1 < (d < 20000u ? d : 20000u)) __builtin_amdgcn_s_sleep(1);
  }
}

extern "C" void kernel_launch(void* const* d_in, const int* in_sizes, int n_in, void* d_out, int out_size,
                              void* d_ws, size_t ws_size, hipStream_t stream) {
  (void)in_sizes; (void)n_in; (void)out_size; (void)ws_size;
  (void)hipFuncSetAttribute((const void*)fused, hipFuncAttributeMaxDynamicSharedMemorySize, LDS_SIZE);
  fused<<<256, 512, LDS_SIZE, stream>>>((const char*)d_in[0], (const char*)d_in[1], (const char*)d_in[2], (const char*)d_in[4],
                                        (const char*)d_in[5], (const char*)d_in[6], (char*)d_ws, (char*)d_out);
}
```

```cpp
#include <hip/hip_runtime.h>
#include <stdint.h>
#include <type_traits>

#define GAS __attribute__((address_space(1)))
#define LAS __attribute__((address_space(3)))
typedef __attribute__((ext_vector_type(2))) __bf16 bf16x2;
typedef __attribute__((ext_vector_type(8))) __bf16 bf16x8;
typedef __attribute__((ext_vector_type(4))) short s16x4;
typedef __attribute__((ext_vector_type(4))) float f32x4;
typedef __attribute__((ext_vector_type(2))) float f32x2;
typedef __attribute__((ext_vector_type(4))) unsigned u32x4;
typedef __attribute__((ext_vector_type(2))) unsigned u32x2;
typedef unsigned long long u64;
typedef __amdgpu_buffer_rsrc_t rsrc_t;

#define DEVI __device__ __forceinline__
#define WAIT_V_(n) asm volatile("s_waitcnt vmcnt(" #n ")" ::: "memory")
#define WAIT_V(n) WAIT_V_(n)
#define WAIT_L0() do { __builtin_amdgcn_s_waitcnt(0xC07F); asm volatile("" ::: "memory"); } while (0)
#define SB() __builtin_amdgcn_sched_barrier(0)
#define PIN(x) asm volatile("" : "+v"(x))
#define PINS(x) asm volatile("" : "+s"(x))
#define BAR() do { asm volatile("" ::: "memory"); __builtin_amdgcn_s_barrier(); asm volatile("" ::: "memory"); } while (0)
#define RLX_AGENT __ATOMIC_RELAXED, __HIP_MEMORY_SCOPE_AGENT
#define RLX_WG __ATOMIC_RELAXED, __HIP_MEMORY_SCOPE_WORKGROUP
#define SC1 16

#define STAMP(i)

constexpr int kD = 640, kF = 1728, kQKV = 1920;
constexpr unsigned OFF_E   = 0;
constexpr unsigned OFF_F1  = 256;
constexpr unsigned OFF_FR  = OFF_F1 + 2048;
constexpr unsigned OFF_FK  = 2560;
constexpr unsigned OFF_F3  = OFF_FK + 4096;
constexpr unsigned OFF_FH  = OFF_F3 + 1024;
constexpr unsigned OFF_FZ  = 16384;
constexpr unsigned OFF_ZP  = OFF_FZ + 4096;
constexpr unsigned OFF_SC  = OFF_ZP + 4 * 32 * 640;
constexpr unsigned OFF_SR  = OFF_SC + 15 * 10 * 384 * 4;
constexpr unsigned OFF_KVP = OFF_SR + 16 * 10 * 256 * 4;
constexpr unsigned OFF_MP  = OFF_KVP + 16 * 16 * 3456;
constexpr unsigned OFF_WD  = OFF_MP + 8 * 20 * 5 * 1024;
constexpr unsigned OFF_Q   = OFF_WD + 8 * 54 * 5 * 1024;
constexpr unsigned OFF_H   = OFF_Q + 32 * 20 * 4096;
constexpr unsigned WS_END  = OFF_H + 32 * 54 * 4096;
constexpr unsigned OOB = 0x80000000u;
static_assert(OFF_FH + 8192 <= OFF_FZ && OFF_MP % 1024 == 0 && OFF_KVP % 128 == 0, "align");

constexpr int L1_B = 0, L1_BST = 24576;
constexpr int L1_A = 4 * L1_BST, L1_AST = 16384;
constexpr int L1_GR = L1_A + 3 * L1_AST;
constexpr int L1_GC = L1_A + L1_AST;
constexpr int L1_ST = 158720;
constexpr int L1_CS = L1_ST + 1024;
constexpr int L1_EX = 72 * 1024;
constexpr int L2_BASE = 120 * 1024;
constexpr int L2_RED = 150 * 1024;
constexpr int L2_FLAG = 162816;
constexpr int LDS_SIZE = L2_FLAG + 64;
enum { LF_STATUS = 0, LF_DMA = 1, LF_K = 2, LF_X = 3, LF_BC = 4, LF_PUB = 8 };
enum { S_H = 1, S_Q = 2, S_M = 4 };

DEVI f32x4 mfma(u32x4 a, u32x4 b, f32x4 c) {
  return __builtin_amdgcn_mfma_f32_16x16x32_bf16(__builtin_bit_cast(bf16x8, a), __builtin_bit_cast(bf16x8, b), c, 0, 0, 0);
}
DEVI void mfma_acc(f32x4& c, u32x4 a, u32x4 b) { asm volatile("v_mfma_f32_16x16x32_bf16 %0, %1, %2, %0" : "+v"(c) : "v"(a), "v"(b)); }
DEVI void mfma_new(f32x4& c, u32x4 a, u32x4 b) { asm volatile("v_mfma_f32_16x16x32_bf16 %0, %1, %2, 0" : "=&v"(c) : "v"(a), "v"(b)); }
DEVI unsigned pk2(f32x2 f) { return __builtin_bit_cast(unsigned, __builtin_convertvector(f, bf16x2)); }
DEVI unsigned pk2(float lo, float hi) { return pk2(f32x2{lo, hi}); }
DEVI f32x2 unpk(unsigned u) { return f32x2{__builtin_bit_cast(float, u << 16), __builtin_bit_cast(float, u & 0xffff0000u)}; }
DEVI f32x2 fma2(f32x2 a, f32x2 b, f32x2 c) { return __builtin_elementwise_fma(a, b, c); }
DEVI f32x2 bc2(float v) { return f32x2{v, v}; }
DEVI f32x2 lo2(f32x4 v) { return f32x2{v[0], v[1]}; }
DEVI f32x2 hi2(f32x4 v) { return f32x2{v[2], v[3]}; }
DEVI f32x2 phi2(f32x2 g, f32x2 rs) {
  const f32x2 t = fma2(g, rs, bc2(1e-6f)), l = g * (rs * 1.4426950408889634f);
  return f32x2{fmaxf(t[0], 1e-6f), fmaxf(t[1], 1e-6f)} +
         f32x2{__builtin_amdgcn_fmed3f(__builtin_amdgcn_exp2f(l[0]), 0.f, 1.f), __builtin_amdgcn_fmed3f(__builtin_amdgcn_exp2f(l[1]), 0.f, 1.f)};
}
DEVI u32x4 lds_b128(unsigned a) { return *(LAS u32x4*)(size_t)a; }
DEVI u32x4 lds_trfrag(unsigned a) {
  const u32x2 lo = __builtin_bit_cast(u32x2, __builtin_amdgcn_ds_read_tr16_b64_v4i16((LAS s16x4*)(size_t)a));
  const u32x2 hi = __builtin_bit_cast(u32x2, __builtin_amdgcn_ds_read_tr16_b64_v4i16((LAS s16x4*)(size_t)(a + 512)));
  return u32x4{lo[0], lo[1], hi[0], hi[1]};
}
DEVI u32x4 srd(const void* p, unsigned bytes) {
  const u64 a = (u64)p;
  return u32x4{(unsigned)a, (unsigned)(a >> 32) & 0xffffu, bytes, 0x00020000u};
}
DEVI void setm0(unsigned v) { asm volatile("s_mov_b32 m0, %0" :: "s"(v) : "memory"); }
template <int IMM>
DEVI void dma_n(u32x4 r, unsigned voff, unsigned soff) {
  asm volatile("buffer_load_dwordx4 %0, %1, %2 offen offset:%3 lds" :: "v"(voff), "s"(r), "s"(soff), "i"(IMM) : "memory");
}
template <int IMM>
DEVI void dma(u32x4 r, unsigned voff, unsigned soff, unsigned lds) {
  asm volatile("s_mov_b32 m0, %4\n\ts_nop 0\n\tbuffer_load_dwordx4 %0, %1, %2 offen offset:%3 lds"
               :: "v"(voff), "s"(r), "s"(soff), "i"(IMM), "s"(lds - IMM) : "memory");
}
DEVI void dma_sc1(u32x4 r, unsigned voff, unsigned soff, unsigned lds) {
  asm volatile("s_mov_b32 m0, %3\n\ts_nop 0\n\tbuffer_load_dwordx4 %0, %1, %2 offen sc1 lds"
               :: "v"(voff), "s"(r), "s"(soff), "s"(lds) : "memory");
}
DEVI u32x2 ld8_hidden(u32x4 r, unsigned voff, unsigned soff) {
  u32x2 v;
  asm volatile("buffer_load_dwordx2 %0, %1, %2, %3 offen sc1" : "=v"(v) : "v"(voff), "s"(r), "s"(soff) : "memory");
  return v;
}
template <int N0, int N1>
DEVI void wd_prefetch(u32x4 rws, unsigned ldsb, int j, int w, int lane) {
#pragma unroll
  for (int n = N0; n < N1; ++n) {
    const unsigned a = (unsigned)(w + 8 * n) * 1024;
    if (n < 18 || w < 6) dma_sc1(rws, lane * 16, OFF_WD + j * 276480 + a, ldsb + a);
  }
}
DEVI void wait_flags(char* ws, unsigned off, u64 tag) {
#pragma unroll 1
  for (unsigned spins = 0; spins < (1u << 20); ++spins) {
    const u64 v = __hip_atomic_load((GAS u64*)(ws + off), RLX_AGENT);
    if (__all(v == tag)) break;
    __builtin_amdgcn_s_sleep(1);
  }
  asm volatile("" ::: "memory");
}
DEVI u64 make_tag(const char* ws) {
  return *(const __attribute__((address_space(4))) u64*)(ws + OFF_E) * 0x9E3779B97F4A7C15ull + 0xD1B54A32D192ED03ull;
}
DEVI void set_flag(char* ws, unsigned off, u64 tag) { __hip_atomic_store((GAS u64*)(ws + off), tag, RLX_AGENT); }
DEVI unsigned lds_inc(LAS unsigned* p, int lane) {
  unsigned o = 0;
  if (lane == 0) o = __hip_atomic_fetch_add(p, 1u, RLX_WG);
  return __builtin_amdgcn_readfirstlane(o);
}
DEVI void lds_wait_bits(LAS unsigned* p, unsigned bits) {
  while ((~(unsigned)__builtin_amdgcn_readfirstlane(__hip_atomic_load(p, RLX_WG)) & bits) != 0u) __builtin_amdgcn_s_sleep(1);
  asm volatile("" ::: "memory");
}
DEVI void lds_wait(LAS unsigned* p, unsigned want) {
  while ((unsigned)__builtin_amdgcn_readfirstlane(__hip_atomic_load(p, RLX_WG)) < want) __builtin_amdgcn_s_sleep(1);
  asm volatile("" ::: "memory");
}
DEVI u32x4 ld16(rsrc_t r, unsigned voff, unsigned soff) { return __builtin_amdgcn_raw_buffer_load_b128(r, voff, soff, SC1); }
DEVI void st16(rsrc_t r, unsigned voff, unsigned soff, u32x4 v) { __builtin_amdgcn_raw_buffer_store_b128(v, r, voff + soff, 0, SC1); }
DEVI void st8(rsrc_t r, unsigned voff, unsigned soff, u32x2 v) { __builtin_amdgcn_raw_buffer_store_b64(v, r, voff, soff, SC1); }

#define WARM 4
#define PRO_WAIT_(n) n
#define PRO_WAIT 9
constexpr unsigned CODE_BYTES = 60 * 1024;
DEVI void warm_up(const char* x, const char* wqkv, const char* wo, const char* wg, const char* wu, const char* wd,
                  const char* ws, const char* pc0, unsigned junk, int bid, int w, int lane) {
  const int cid = bid * 6 + w;
  const char* p = cid < 320 ? x : cid < 620 ? wqkv : cid < 890 ? wg : cid < 1160 ? wu : cid < 1430 ? wd : wo;
  unsigned o = (cid - (cid < 320 ? 0 : cid < 620 ? 320 : cid < 890 ? 620 : cid < 1160 ? 890 : cid < 1430 ? 1160 : 1430)) * 8192;
  unsigned n = (WARM & 1) && cid < 1530 ? o + 8192 : 0, sh = 7;
  if (w == 6) { p = ws; o = 0; n = (WARM & 2) ? WS_END : 0; sh = 18; }
  if (w == 7) { p = pc0; o = (bid >> 3) * 8192; n = (WARM & 4) ? CODE_BYTES : 0; }
  asm volatile("s_mov_b32 m0, %3\n\ts_nop 0\n\tbuffer_load_dword %0, %1, %2 offen lds"
               :: "v"(lane << sh), "s"(srd(p, n)), "s"(o), "s"(junk) : "memory");
}

struct P1 {
  u32x4 rw, rx;
  unsigned ldsb, pitch;
  unsigned voffB, voffA;
  unsigned oB, oA;
  int w, mw, nw, bstar;
  int Mt, cslot, j;
};

#define ABL_RD 0
#define AINC 1
template <int TYPE, bool LATE>
DEVI void p1_loop(P1& c, unsigned (&vb)[5], unsigned va0, unsigned foff, f32x4 (&acc)[4][6], char* ws, rsrc_t rws, u64 tag, int lane) {
  constexpr int NT = TYPE == 0 ? 6 : 5;
  constexpr bool AC = !LATE || AINC;
  u32x4 fa[4], fb[NT];
  unsigned va1 = va0 ^ 64;
  const unsigned stepB = 32 * c.pitch, limB = 640 * c.pitch;
  const unsigned lB0 = c.ldsb + L1_B + c.w * 3072, lA0 = c.ldsb + L1_A + c.w * 1024;
  unsigned lB = lB0 + 3 * L1_BST, lA = lA0 + 2 * L1_AST;
  unsigned vB1 = c.voffB - 896, vB2 = c.voffB - 1792, vA1 = c.voffA + 81920;
  PIN(vB1); PIN(vB2); PIN(vA1);
  int dA = L1_AST, dB = 2 * L1_BST, rare = c.bstar, rare2 = 1;
  f32x4 st[5];
  auto adB = [&](int i) { return TYPE == 0 ? vb[!LATE && i >= 2 ? 1 : 0] + (i / 3) * 12288 + (i % 3) * 1024 : vb[i < 5 ? i : 4]; };
  if (TYPE == 0) { vb[1] = vb[0]; PIN(vb[1]); }
  auto upB = [&](int i0, int i1) {
    if (TYPE == 0) { vb[i0 ? 1 : 0] += dB; PIN(vb[i0 ? 1 : 0]); }
    else {
#pragma unroll
      for (int i = i0; i < i1; ++i) { vb[i] += dB; PIN(vb[i]); }
    }
  };
  auto rdA = [&](int i, int sub) { if (ABL_RD & 1) { PIN(fa[i]); return; } fa[i] = lds_b128((sub ? va1 : va0) + i * 2048); };
  auto rdB = [&](int i, int sub) { if (ABL_RD & 2) { PIN(fb[i]); return; } fb[i] = lds_trfrag(adB(i) + sub * L1_BST); };
  auto rdBh = [&](int i, int hh, int sub) {
    if (ABL_RD & 2) { PIN(fb[i]); return; }
    const u32x2 v = __builtin_bit_cast(u32x2, __builtin_amdgcn_ds_read_tr16_b64_v4i16((LAS s16x4*)(size_t)(adB(i) + sub * L1_BST + hh * 512)));
    fb[i][2 * hh] = v[0]; fb[i][2 * hh + 1] = v[1];
  };
  auto pieceA = [&](int sub) { dma_n<0>(c.rx, sub ? vA1 : c.voffA, c.oA); };
  auto L = [&](int sub, int mode) {
    if (mode) dma_n<0>(c.rw, c.voffB, c.oB);
    SB(); rdA(0, sub); rdA(1, sub); SB();
    if (mode) dma_n<1024>(c.rw, vB1, c.oB);
    SB(); rdA(2, sub); rdA(3, sub); SB();
    if (mode) dma_n<2048>(c.rw, vB2, c.oB);
    SB(); rdB(LATE ? 4 : 0, sub); SB();
    if (mode == 2) setm0(lA + sub * 8192);
    SB(); if (!LATE) rdB(1, sub); else if (NT == 6) rdB(NT - 1, sub); else if (mode == 2) asm volatile("s_nop 0"); SB();
    if (mode == 2) pieceA(sub);
    if (!LATE && !sub) upB(2, 5);
    SB();
  };
  auto U = [&](int sub) {
    if (sub) { va1 += dA; PIN(va1); upB(0, LATE ? 5 : 2); }
    else { va0 += dA; PIN(va0); }
    SB();
  };
  auto stats = [&](int sub) {
    const u32x4 one = {0x3f803f80u, 0x3f803f80u, 0x3f803f80u, 0x3f803f80u};
    const u32x4 sa = lds_b128((sub ? va1 : va0) + c.nw * 2048);
    u32x4 sb[3];
#pragma unroll
    for (int i = 0; i < 3; ++i) { unsigned a = adB(LATE ? i : 3 + i); PIN(a); sb[i] = lds_trfrag(a + sub * L1_BST); }
    SB();
    if (sub == 0) {
#pragma unroll
      for (int i = 0; i < 5; ++i) st[i] = f32x4{0.f, 0.f, 0.f, 0.f};
    }
    st[0] = mfma(one, sa, st[0]);
    st[1] = mfma(sa, sa, st[1]);
#pragma unroll
    for (int i = 0; i < 3; ++i) st[2 + i] = mfma(one, sb[i], st[2 + i]);
    if (sub == 1) {
      const int cl = lane & 15, g = lane >> 4;
      const float sq = (cl & 2) ? ((cl & 1) ? st[1][3] : st[1][2]) : ((cl & 1) ? st[1][1] : st[1][0]);
      st8(rws, (cl >> 2) == g ? (c.mw * 64 + c.nw * 16 + cl) * 8 : OOB, c.cslot < 10 ? OFF_SR + (c.Mt * 10 + c.cslot) * 1024 : WS_END,
          u32x2{__builtin_bit_cast(unsigned, st[0][0]), __builtin_bit_cast(unsigned, sq)});
      const float v = g == 0 ? st[2][0] : g == 1 ? st[3][0] : st[4][0];
      __builtin_amdgcn_raw_buffer_store_b32(__builtin_bit_cast(unsigned, v), rws, g < 3 ? ((c.nw * 6 + c.mw * 3) * 16 + lane) * 4 : OOB,
                                            c.Mt < 10 ? OFF_SC + (c.cslot * 10 + c.Mt) * 1536 : WS_END, SC1);
    }
    SB();
  };
#define SEG(i)
  auto C = [&](int sub, bool more, int book, auto vmwait) {
    SEG(0);
    if (LATE) { vmwait(); SEG(1); WAIT_L0(); BAR(); SEG(2); }
    __builtin_amdgcn_s_setprio(1);
    SB();
#pragma unroll
    for (int k = 0; k < 4 * NT; ++k) {
      const int i = k >> 2, m = k & 3;
      acc[m][i] = TYPE == 1 ? mfma(fa[m], fb[i], acc[m][i]) : mfma(fb[i], fa[m], acc[m][i]);
      if (LATE) { if (more && i >= 1 && i < 5 && m < 2) rdBh(i - 1, m, sub ^ 1); }
      else if (k < 2 * (NT - 2)) rdBh(2 + (k >> 1), k & 1, sub);
      const int q = LATE ? (k < 2 ? k : m >= 2 && i < 5 ? 2 * i + m : -1) : k - 2 * (NT - 2);
      constexpr int QA = LATE ? 8 : 0, QU = LATE ? 0 : 3;
      if (book == 1) {
        if (AC) {
          if (q == QA) setm0(lA + (LATE ? sub ^ 1 : sub) * 8192);
          if (q == QA + 1) pieceA(LATE ? sub ^ 1 : sub);
        }
        if (q == QU) { c.oB += stepB; lB += L1_BST; PINS(c.oB); PINS(lB); }
        if (q == QU + 1) { c.oB = c.oB == limB ? 0u : c.oB; PINS(c.oB); }
        if (q == QU + 2) { lB = lB == lB0 + 4 * L1_BST ? lB0 : lB; PINS(lB); }
        if (sub) {
          if (q == QU + 3) { c.oA += 128; lA += L1_AST; PINS(c.oA); PINS(lA); }
          if (q == QU + 4) { c.oA = c.oA == 1280u ? 0u : c.oA; PINS(c.oA); }
          if (q == QU + 5) { lA = lA == lA0 + 3 * L1_AST ? lA0 : lA; PINS(lA); }
          if (q == QU + 6) { dA = lA == lA0 + L1_AST ? -2 * L1_AST : L1_AST; PINS(dA); }
          if (q == QU + 7) { rare -= 1; PINS(rare); }
        }
        if (!sub && q == QU + 3) { rare2 = LATE ? (rare + 2) & ~2 : rare; PINS(rare2); }
        if (q == 11) setm0(lB);
      }
      if (book && q == 10 && (LATE ? sub : !sub)) { dB = -dB; PINS(dB); }
      SB();
    }
    __builtin_amdgcn_s_setprio(0);
    SEG(3);
    if (!LATE) { vmwait(); SEG(1); WAIT_L0(); BAR(); SEG(2); }
  };
  constexpr int LM = AC ? 1 : 2;
  if (!LATE) { upB(2, 5); dB = -dB; }
  WAIT_V(PRO_WAIT); BAR();
  if (ABL_RD) {
#pragma unroll
    for (int i = 0; i < 4; ++i) fa[i] = lds_b128(va0 + i * 2048);
#pragma unroll
    for (int i = 0; i < NT; ++i) fb[i] = lds_trfrag(c.ldsb + (lane & 63) * 8 + i * 1024);
  }
  if (LATE) {
    if (AC) { setm0(lA); SB(); }
    rdB(0, 0); rdB(1, 0); SB();
    if (AC) pieceA(0);
  }
  setm0(lB);
  SB();
  if (LATE) { rdB(2, 0); rdB(3, 0); } else asm volatile("s_nop 0");
  SB();
  const int rend = c.bstar - 8;
#pragma unroll 1
  do {
    L(0, LM);
    if (__builtin_expect(rare == 0, 0)) stats(0);
    U(0);
    C(0, true, 1, []() { WAIT_V(8); });
    L(1, LM);
    if (__builtin_expect(rare2 == 0, 0)) {
      PINS(rare);
      if (rare == 0) stats(1);
      else if (LATE && c.w == 0 && lane == 0) set_flag(ws, OFF_F1 + (c.Mt * 16 + c.cslot) * 8, tag);
    }
    U(1);
    C(1, true, 1, []() { WAIT_V(8); });
  } while (rare != rend);
  const u32x4 rw4 = srd(ws, WS_END);
  u32x2 fl = ld8_hidden(rw4, foff, 0);
  L(0, 1);
  U(0);
  C(0, true, 2, []() { WAIT_V(8); });
  L(1, 0);
  WAIT_V(3);
  PIN(fl);
  if (__builtin_expect(!__all(__builtin_bit_cast(u64, fl) == tag), 0)) wait_flags(ws, foff, tag);
  dma_sc1(rw4, lane * 16, OFF_SC + c.cslot * 15360 + c.w * 1024, c.ldsb + L1_GC + c.w * 1024);
  dma_sc1(rw4, lane * 16, OFF_SC + c.cslot * 15360 + c.w * 1024 + 8192, c.ldsb + L1_GC + c.w * 1024 + 8192);
  dma_sc1(rw4, lane * 16, OFF_SR + c.Mt * 10240 + c.w * 1024, c.ldsb + L1_GR + c.w * 1024);
  if (LATE && c.w < 2) dma_sc1(rw4, lane * 16, OFF_SR + c.Mt * 10240 + c.w * 1024 + 8192, c.ldsb + L1_GR + c.w * 1024 + 8192);
  U(1);
  C(1, true, 2, []() {});
  L(0, 0);
  wd_prefetch<0, 4>(rw4, c.ldsb, c.j, c.w, lane);
  C(0, true, 0, []() { WAIT_V(4); });
  L(1, 0);
  {
    __builtin_amdgcn_s_setprio(2);
    if (LATE || c.w < 6) {
      const LAS float* src = (const LAS float*)(size_t)(c.ldsb + L1_GC + (c.w * 64 + lane) * 4);
      float v[10];
#pragma unroll
      for (int p = 0; p < 10; ++p) v[p] = src[p * 384];
      *(LAS float*)(size_t)(c.ldsb + L1_CS + (c.w * 64 + lane) * 4) = (((v[0] + v[1]) + (v[2] + v[3])) + ((v[4] + v[5]) + (v[6] + v[7]))) + (v[8] + v[9]);
    } else {
      const LAS f32x2* src = (const LAS f32x2*)(size_t)(c.ldsb + L1_GR + ((c.w - 6) * 64 + lane) * 8);
      f32x2 v[10];
#pragma unroll
      for (int p = 0; p < 10; ++p) v[p] = src[p * 128];
      const f32x2 s = ((((v[0] + v[1]) + (v[2] + v[3])) + ((v[4] + v[5]) + (v[6] + v[7]))) + (v[8] + v[9])) * (1.0f / kD);
      LAS float* d = (LAS float*)(size_t)(c.ldsb + L1_ST + ((c.w - 6) * 64 + lane) * 4);
      d[0] = s[0]; d[128] = __builtin_amdgcn_rsqf(fmaxf(__builtin_fmaf(-s[0], s[0], s[1]), 0.f) + 1e-5f);
    }
    __builtin_amdgcn_s_setprio(0);
  }
  SB();
  wd_prefetch<4, 6>(rw4, c.ldsb, c.j, c.w, lane);
  SB();
  C(1, false, 0, []() {});
}

template <int N, class F> DEVI void static_for(F&& f) {
  if constexpr (N > 0) { static_for<N - 1>(f); f(std::integral_constant<int, N - 1>{}); }
}
DEVI u32x4 pair16(u32x2 a, u32x2 b) {
  const auto s0 = __builtin_amdgcn_permlane32_swap(a[0], b[0], false, false);
  const auto s1 = __builtin_amdgcn_permlane32_swap(a[1], b[1], false, false);
  return u32x4{s0[0], s1[0], s0[1], s1[1]};
}
struct Watch {
  LAS unsigned* lf; char* ws; u64 tag; int mt2, j, w, lane;
  unsigned status;
  DEVI u64 fetch() {
    int l2 = lane;
    PIN(l2);
    const unsigned f0 = l2 < 32 ? OFF_FH + mt2 * 256 + (j >> 2) * 128 + (l2 < 11 ? l2 : 0) * 8 : OFF_F3 + (blockIdx.x & 7) * 128 + (l2 & 15) * 8;
    return __hip_atomic_load((GAS u64*)(ws + f0), RLX_AGENT);
  }
  DEVI bool eval(u64 v, bool giveup) {
    const u64 no = __ballot(v != tag);
    unsigned s = ((no & 0x1ffull) ? 0u : S_H) | ((no & 0x600ull) ? 0u : S_Q) | ((no >> 32) ? 0u : S_M);
    if (giveup) s = 7u;
    if (s == status) return false;
    status = s;
    __hip_atomic_store(lf + LF_STATUS, s, RLX_WG);
    return true;
  }
  DEVI bool poll(bool giveup) { return eval(fetch(), giveup); }
  DEVI bool has(unsigned bits) { return (~(unsigned)__builtin_amdgcn_readfirstlane(__hip_atomic_load(lf + LF_STATUS, RLX_WG)) & bits) == 0u; }
  int pcnt, plast, preps; unsigned pflag;
  DEVI void publish() {
    if ((int)lds_inc(lf + LF_PUB + pcnt, lane) == plast && lane < preps) set_flag(ws, pflag + lane * 128, tag);
    preps = 0;
  }
  DEVI void need(unsigned bits) {
    if (w != 7) { lds_wait_bits(lf + LF_STATUS, bits); return; }
#pragma unroll 1
    for (unsigned spins = 0; (~status & bits) != 0u; ++spins)
      if (!poll(spins >= (1u << 20))) __builtin_amdgcn_s_sleep(1);
    asm volatile("" ::: "memory");
  }
};

struct PF2 {
  char* ws; rsrc_t rws; unsigned ldsb; int j, w, lane;
  u32x4 (&br)[3][5];
  template <int N> DEVI void item() {
    if constexpr (N < 13) wd_prefetch<6 + N, 7 + N>(srd(ws, WS_END), ldsb, j, w, lane);
    else br[(N - 13) / 5][(N - 13) % 5] = ld16(rws, lane * 16, OFF_WD + (j * 54 + 30 + w + 8 * ((N - 13) / 5)) * 5120 + ((N - 13) % 5) * 1024);
    SB();
  }
  template <int N0, int N1> DEVI void items() { if constexpr (N0 < N1) { item<N0>(); items<N0 + 1, N1>(); } }
};

#define EPI_N 8
DEVI void epi_ffn(LAS char* lds, f32x4 (&acc)[4][6], char* ws, rsrc_t rws, Watch& wt, const P1& c, PF2& pf, int lane) {
  const int r = lane & 15, g = lane >> 4;
  pf.items<0, 2>();
  float nm[4], k1[4], k2[4], iv[4];
#pragma unroll
  for (int mi = 0; mi < 4; ++mi) {
    const LAS float* sp = (const LAS float*)(lds + L1_ST) + c.mw * 64 + mi * 16 + r;
    const float rs = sp[128], c0 = -2.0f * 0.7978845608028654f * 1.4426950408889634f;
    nm[mi] = -sp[0]; k1[mi] = c0 * rs; k2[mi] = c0 * 0.044715f * rs * rs * rs; iv[mi] = __builtin_amdgcn_rcpf(rs * rs);
  }
  const unsigned hb = OFF_H + ((c.Mt * 2 + c.mw) * 54 + 2 * c.cslot + (c.nw >> 1)) * 4096 + (c.nw & 1) * 512 + (lane & 31) * 16 + (lane >> 5) * 1024;
  u32x4 v[6];
  static_for<3>([&](auto I) {
    constexpr int i = I;
    const f32x4 cg = *(LAS f32x4*)(lds + L1_CS + ((c.nw * 6 + i) * 16 + 4 * g) * 4);
    const f32x4 cu = *(LAS f32x4*)(lds + L1_CS + ((c.nw * 6 + 3 + i) * 16 + 4 * g) * 4);
    u32x2 o[4];
    static_for<4>([&](auto MI) {
      constexpr int mi = MI, n = 4 * i + mi;
      if constexpr (2 + 2 * n < EPI_N) pf.template items<2 + 2 * n, 4 + 2 * n>();
#pragma unroll
      for (int hh = 0; hh < 2; ++hh) {
        const f32x2 G = fma2(bc2(nm[mi]), hh ? hi2(cg) : lo2(cg), hh ? hi2(acc[mi][i]) : lo2(acc[mi][i]));
        const f32x2 U = fma2(bc2(nm[mi]), hh ? hi2(cu) : lo2(cu), hh ? hi2(acc[mi][3 + i]) : lo2(acc[mi][3 + i]));
        const f32x2 z = G * fma2(G * G, bc2(k2[mi]), bc2(k1[mi]));
        const f32x2 d = fma2(f32x2{__builtin_amdgcn_exp2f(z[0]), __builtin_amdgcn_exp2f(z[1])}, bc2(iv[mi]), bc2(iv[mi]));
        o[mi][hh] = pk2(G * U * f32x2{__builtin_amdgcn_rcpf(d[0]), __builtin_amdgcn_rcpf(d[1])});
      }
      PIN(o[mi]);
      SB();
#define STN 4
#define STS 4
      if constexpr (n == STN || n == STN + STS) {
        constexpr int p = (n - STN) / STS;
        st16(rws, hb, p * 18 * 4096, v[2 * p]);
        st16(rws, hb, p * 18 * 4096 + 2048, v[2 * p + 1]);
        SB();
      }
    });
    v[2 * i] = pair16(o[0], o[1]); v[2 * i + 1] = pair16(o[2], o[3]);
    PIN(v[2 * i]); PIN(v[2 * i + 1]);
  });
  st16(rws, hb, 36 * 4096, v[4]);
  st16(rws, hb, 36 * 4096 + 2048, v[5]);
  STAMP(4);
  BAR();
  pf.items<EPI_N, 12>();
  WAIT_V(4);
  wt.pcnt = c.mw; wt.plast = 3; wt.preps = 2; wt.pflag = OFF_FH + (c.Mt * 2 + c.mw) * 256 + c.cslot * 8;
  wt.publish();
  STAMP(5);
  pf.items<12, 28>();
}

DEVI void epi_q(LAS char* lds, f32x4 (&acc)[4][6], char* ws, rsrc_t rws, Watch& wt, const P1& c, PF2& pf, int lane) {
  const int r = lane & 15, g = lane >> 4, qi = c.cslot - 13;
  f32x4 cs[5];
#pragma unroll
  for (int i = 0; i < 5; ++i) cs[i] = *(LAS f32x4*)(lds + L1_CS + ((c.nw * 6 + i) * 16 + 4 * g) * 4);
  pf.items<0, 8>();
  static_for<4>([&](auto MI) {
    constexpr int mi = MI;
    const LAS float* sp = (const LAS float*)(lds + L1_ST) + c.mw * 64 + mi * 16 + r;
    const f32x2 nm = bc2(-sp[0]), rs = bc2(sp[128]);
    static_for<5>([&](auto I) {
      constexpr int i = I;
      const f32x2 a = phi2(fma2(nm, lo2(cs[i]), lo2(acc[mi][i])), rs), b = phi2(fma2(nm, hi2(cs[i]), hi2(acc[mi][i])), rs);
      acc[mi][i] = f32x4{a[0], a[1], b[0], b[1]};
      PIN(acc[mi][i]);
      SB();
      pf.template item<8 + mi * 5 + i>();
    });
  });
  STAMP(3);
  LAS unsigned* lf = (LAS unsigned*)(lds + L2_FLAG);
  if (c.w == 4) { wait_flags(ws, OFF_FZ + (blockIdx.x & 7) * 512 + qi * 256 + (lane & 31) * 8, wt.tag); lds_inc(lf + LF_K, lane); }
  else lds_wait(lf + LF_K, 1u);
  STAMP(12);
  {
    const int q4 = (c.w & 1) * 64 + lane;
    f32x4 part[8];
#pragma unroll
    for (int p = 0; p < 8; ++p)
      part[p] = __builtin_bit_cast(f32x4, ld16(rws, q4 < 40 ? q4 * 16 : q4 < 80 ? q4 * 16 + 31 * 640 : OOB, OFF_ZP + (qi * 64 + (c.w >> 1) * 8 + p) * 640));
    SB();
    if (q4 < 80) *(LAS f32x4*)(lds + L2_RED + (c.w >> 1) * 1280 + q4 * 16) = ((part[0] + part[1]) + (part[2] + part[3])) + ((part[4] + part[5]) + (part[6] + part[7]));
    WAIT_L0(); BAR();
  }
  u64 fv = 0;
  if (c.w == 7) fv = wt.fetch();
  SB();
  STAMP(5);
  f32x4 z[5];
#pragma unroll
  for (int i = 0; i < 5; ++i) {
    const LAS char* p = lds + L2_RED + (c.nw * 80 + 20 * (2 * (g & 1) + (g >> 1)) + 4 * i) * 4;
    z[i] = (*(const LAS f32x4*)p + *(const LAS f32x4*)(p + 1280)) + (*(const LAS f32x4*)(p + 2560) + *(const LAS f32x4*)(p + 3840));
  }
  const unsigned qb = OFF_Q + ((c.Mt * 2 + c.mw) * 20 + (2 * qi + (c.nw >> 1)) * 5) * 4096;
  u32x2 keep;
#pragma unroll
  for (int mi = 0; mi < 4; ++mi) {
    f32x2 d0 = lo2(acc[mi][0]) * lo2(z[0]), d1 = hi2(acc[mi][0]) * hi2(z[0]);
#pragma unroll
    for (int i = 1; i < 5; ++i) { d0 = fma2(lo2(acc[mi][i]), lo2(z[i]), d0); d1 = fma2(hi2(acc[mi][i]), hi2(z[i]), d1); }
    d0 += d1;
    const f32x2 inv = bc2(__builtin_amdgcn_rcpf(d0[0] + d0[1] + 1e-6f));
    unsigned o[10];
#pragma unroll
    for (int i = 0; i < 5; ++i) { o[2 * i] = pk2(lo2(acc[mi][i]) * inv); o[2 * i + 1] = pk2(hi2(acc[mi][i]) * inv); }
    st16(rws, lane * 16 + mi * 1024, qb + (c.nw & 1) * 12288, u32x4{o[0], o[1], o[2], o[3]});
    st16(rws, lane * 16 + mi * 1024, qb + (c.nw & 1) * 12288 + 4096, u32x4{o[4], o[5], o[6], o[7]});
    if (mi & 1) st16(rws, (lane & 31) * 16 + (lane >> 5) * 1024 + (mi - 1) * 1024, qb + 8192 + (c.nw & 1) * 512, pair16(keep, u32x2{o[8], o[9]}));
    else keep = u32x2{o[8], o[9]};
  }
  STAMP(4);
  SB();
  if (c.w == 7) wt.eval(fv, false);
  WAIT_L0(); BAR();
  wt.pcnt = c.mw; wt.plast = 3; wt.preps = 2; wt.pflag = OFF_FH + (c.Mt * 2 + c.mw) * 256 + (9 + qi) * 8;
}

DEVI void epi_kv(LAS char* lds, f32x4 (&acc)[4][6], char* ws, rsrc_t rws, u64 tag, const P1& c, int lane) {
  const int cl = lane & 15, g = lane >> 4, kj = c.cslot - 9;
  float cs[5];
#pragma unroll
  for (int i = 0; i < 5; ++i) cs[i] = -*(LAS float*)(lds + L1_CS + ((c.nw * 6 + i) * 16 + cl) * 4);
  f32x4 mean[4], rstd[4];
#pragma unroll
  for (int mi = 0; mi < 4; ++mi) {
    mean[mi] = *(LAS f32x4*)(lds + L1_ST + (c.mw * 64 + mi * 16 + 4 * g) * 4);
    rstd[mi] = *(LAS f32x4*)(lds + L1_ST + 512 + (c.mw * 64 + mi * 16 + 4 * g) * 4);
  }
  u32x4 fk[2][3], fv[2][3];
  auto center = [&](int mi, int i, int hh) { return fma2(hh ? hi2(mean[mi]) : lo2(mean[mi]), bc2(cs[i]), hh ? hi2(acc[mi][i]) : lo2(acc[mi][i])); };
#pragma unroll
  for (int mi = 0; mi < 4; ++mi)
#pragma unroll
    for (int f = 0; f < 3; ++f)
#pragma unroll
      for (int hh = 0; hh < 2; ++hh)
        fk[mi >> 1][f][(mi & 1) * 2 + hh] = pk2(phi2(center(mi, f < 2 ? f : 4, hh), hh ? hi2(rstd[mi]) : lo2(rstd[mi])));
  const int uh = cl < 4 ? 16 + cl : 32 + cl;
  const f32x4 zero = {0.f, 0.f, 0.f, 0.f};
  {
    const u32x4 one = {0x3f803f80u, 0x3f803f80u, 0x3f803f80u, 0x3f803f80u};
    f32x4 z[3];
#pragma unroll
    for (int f = 0; f < 3; ++f) z[f] = mfma(one, fk[1][f], mfma(one, fk[0][f], zero));
    const float v = g == 0 ? z[0][0] : g == 1 ? z[1][0] : z[2][0];
    __builtin_amdgcn_raw_buffer_store_b32(__builtin_bit_cast(unsigned, v), rws, g < 2 ? (g * 20 + cl) * 4 : (g == 2 && cl < 8) ? uh * 4 : OOB,
                                          OFF_ZP + (kj * 32 + c.Mt * 2 + c.mw) * 640 + c.nw * 160, SC1);
    SB();
  }
#pragma unroll
  for (int mi = 0; mi < 4; ++mi)
#pragma unroll
    for (int f = 0; f < 3; ++f)
#pragma unroll
      for (int hh = 0; hh < 2; ++hh)
        fv[mi >> 1][f][(mi & 1) * 2 + hh] = pk2(center(mi, 2 + f, hh) * (hh ? hi2(rstd[mi]) : lo2(rstd[mi])));
  constexpr int KI[7] = {0, 0, 2, 1, 1, 2, 2}, VI[7] = {0, 2, 0, 1, 2, 1, 2};
  f32x4 p[7];
#pragma unroll
  for (int q = 0; q < 7; ++q) p[q] = mfma(fv[1][VI[q]], fk[1][KI[q]], mfma(fv[0][VI[q]], fk[0][KI[q]], zero));
  LAS char* ex = lds + L1_EX + c.w * 3584;
  auto put = [&](bool ok, int u, int e, f32x4 v) { *(LAS f32x4*)(ex + (ok ? (u * 20 + e) * 4 : 3200)) = v; };
  put(true, cl, 4 * g, p[0]);
  put(true, 20 + cl, 4 * g, p[3]);
  put(g >= 2, (g & 1) * 20 + cl, 16, g == 2 ? p[1] : p[4]);
  put(cl < 8, uh, 4 * g, cl < 4 ? p[2] : p[5]);
  put(cl < 8 && g == 2 + (cl >> 2), uh, 16, p[6]);
  WAIT_V(0); WAIT_L0(); BAR();
  if (c.w == 7 && lane < 8) set_flag(ws, OFF_FZ + lane * 512 + kj * 128 + c.Mt * 8, tag);
#pragma unroll
  for (int q = 0; q < 2; ++q) {
    const int o = (c.mw * 128 + q * 64 + lane) * 16;
    const LAS char* src = lds + L1_EX + c.nw * 3584 + (o < 3200 ? o : 0);
    const f32x4 v = *(const LAS f32x4*)src + *(const LAS f32x4*)(src + 4 * 3584);
    st16(rws, o < 3200 ? o : OOB, OFF_KVP + ((kj * 4 + c.nw) * 16 + c.Mt) * 3456, __builtin_bit_cast(u32x4, v));
  }
}

DEVI void retile(const char* wd, rsrc_t rws, unsigned ldsw, int id, int w, int lane) {
  const u32x4 rd = srd(wd, kF * 1280);
  const int jj = lane >> 3, g = lane >> 4;
  const unsigned vo = ((jj >> 2) * 8 + (jj & 3)) * 1280 + (lane & 7) * 16;
  const unsigned ra = ldsw + g * 1024 + ((lane & 15) >> 2) * 128 + (lane & 3) * 8;
  auto fetch = [&](int n) {
    const int it = id * 8 + w + n * 128, rb = it / 10, ln = it - rb * 10;
#pragma unroll
    for (int q = 0; q < 4; ++q)
      dma<0>(rd, vo, it < 540 ? (rb * 32 + 16 * (q >> 1) + 4 * (q & 1)) * 1280 + ln * 128 : OOB, ldsw + (n & 3) * 4096 + q * 1024);
  };
  auto put = [&](int n) {
    const int it = id * 8 + w + n * 128, rb = it / 10, ln = it - rb * 10, cg = rb / 6, uu = rb - cg * 6;
    const int ks = 18 * (uu >> 1) + 2 * cg + (uu & 1);
#pragma unroll
    for (int q = 0; q < 4; ++q) {
      const int nt = ln * 4 + q, j = nt / 5, t = nt - j * 5;
      st16(rws, lane * 16, it < 540 ? OFF_WD + ((j * 54 + ks) * 5 + t) * 1024 : OOB, lds_trfrag(ra + (n & 3) * 4096 + q * 32));
    }
  };
#pragma unroll 1
  for (int n = 0; n < 4; ++n) fetch(n);
  WAIT_V(0);
  put(0);
  WAIT_L0();
  fetch(4);
#pragma unroll 1
  for (int n = 1; n < 5; ++n) {
    if (n == 4) WAIT_V(0);
    put(n);
  }
}

DEVI void fold_kv(LAS char* lds, char* ws, rsrc_t rws, Watch& wt, const u32x4 (&wf)[2][5], int hp, int w, int lane) {
  const u64 tag = wt.tag;
  const int cl = lane & 15, g = lane >> 4;
  const int o = ((w & 3) * 64 + lane) * 16;
  unsigned po = o < 3200 ? o : OOB, ko[6][2];
#pragma unroll
  for (int f = 0; f < 6; ++f) {
    const int u = 20 * (f & 1) + (f < 4 ? (cl & 4) * 2 + 4 * (f >> 1) : 16) + (cl & 3);
    const bool mine = f >= 4 || (cl >> 3) == (f & 1);
    ko[f][0] = (unsigned)(size_t)lds + L2_RED + (mine && g < 3 ? u * 80 + g * 32 : 3360);
    ko[f][1] = (unsigned)(size_t)lds + L2_RED + (mine && g < 2 ? u * 80 + g * 32 + 16 : 3360);
    PIN(ko[f][0]); PIN(ko[f][1]);
  }
  unsigned vo = OFF_MP + (w * 20 + (hp >> 2) * 5) * 5120 + (3 * ((hp >> 1) & 1) + (g & 1)) * 5120 + (((hp & 1) + (g & 2)) * 16 + cl) * 16;
  unsigned vo2 = OFF_MP + (w * 20 + (hp >> 2) * 5 + 2) * 5120 + g * 1024 + ((hp & 3) * 16 + cl) * 16;
  PIN(po); PIN(vo); PIN(vo2);
  WAIT_V(0);
  if (w == 0) wait_flags(ws, OFF_FK + (blockIdx.x & 7) * 512 + (hp >> 2) * 128 + cl * 8, tag);
  BAR();
  STAMP(2);
  {
    f32x4 part[8];
#pragma unroll
    for (int p = 0; p < 8; ++p) part[p] = __builtin_bit_cast(f32x4, ld16(rws, po, OFF_KVP + (hp * 16 + (w >> 2) * 8 + p) * 3456));
    SB();
    *(LAS f32x4*)(lds + L2_RED + (w >> 2) * 4096 + o) = ((part[0] + part[1]) + (part[2] + part[3])) + ((part[4] + part[5]) + (part[6] + part[7]));
    WAIT_L0(); BAR();
  }
  STAMP(12);
  u64 fv = 0;
  if (w == 7) fv = wt.fetch();
  SB();
  u32x4 kf[6];
#pragma unroll
  for (int f = 0; f < 6; ++f) {
    const f32x4 lo = *(LAS f32x4*)(size_t)ko[f][0] + *(LAS f32x4*)(size_t)(ko[f][0] + 4096);
    const f32x4 hi = *(LAS f32x4*)(size_t)ko[f][1] + *(LAS f32x4*)(size_t)(ko[f][1] + 4096);
    kf[f] = u32x4{pk2(lo2(lo)), pk2(hi2(lo)), pk2(lo2(hi)), pk2(hi2(hi))};
  }
  const f32x4 zero = {0.f, 0.f, 0.f, 0.f};
  u32x4 r1;
#pragma unroll
  for (int i = 0; i < 5; ++i) {
    const f32x4 d1 = mfma(kf[1], wf[1][i], mfma(kf[0], wf[0][i], zero)), d2 = mfma(kf[3], wf[1][i], mfma(kf[2], wf[0][i], zero));
    const f32x4 d3 = mfma(kf[4], wf[0][i], zero), d4 = mfma(kf[5], wf[1][i], zero);
    st16(rws, vo, i * 1024, u32x4{pk2(lo2(d1)), pk2(hi2(d1)), pk2(lo2(d2)), pk2(hi2(d2))});
    const u32x4 t = {pk2(lo2(d3)), pk2(hi2(d3)), pk2(lo2(d4)), pk2(hi2(d4))};
    if (i == 0 || i == 4) r1 = t; else r1 = g == i ? t : r1;
    if (i == 3) st16(rws, vo2, 0, r1);
    if (i == 4) st16(rws, g == 0 ? vo2 + 4096 : OOB, 0, r1);
  }
  STAMP(3);
  SB();
  if (w == 7) wt.eval(fv, false);
  WAIT_L0(); BAR();
  wt.pcnt = 0; wt.plast = 7; wt.preps = 8; wt.pflag = OFF_F3 + hp * 8;
}

template <bool ATT>
DEVI void p2_steps(LAS char* lds, rsrc_t rws, Watch& wt, const u32x4 (&br)[3][5], f32x4 (&acc)[4][5], const u32x2 (&xr)[5],
                   int mt2, int j, int w, int lane) {
  LAS unsigned* lf = wt.lf;
  const unsigned ab = OFF_H + (mt2 * 54 + w) * 4096;
  u32x4 fa[12][4], fb[12][5];
  auto ldA = [&](int n) {
    const unsigned so = n >= 7 ? OFF_Q + (mt2 * 20 + (w - 4) * 5 + n - 7) * 4096
                               : n == 6 && w >= 6 ? OOB : ab + (n < 3 ? 30 + 8 * n : 8 * n - 24) * 4096;
#pragma unroll
    for (int m = 0; m < 4; ++m) fa[n][m] = ld16(rws, lane * 16, so + ((m + w) & 3) * 1024);
    SB();
  };
  unsigned lb = (unsigned)(size_t)lds + w * 5120 + lane * 16;
  auto rdB = [&](int n) {
    if (n == 5) { lb += 81920; PIN(lb); }
    if (ATT && n == 6) { lb -= w >= 6 ? 40960 : 0; PIN(lb); }
#pragma unroll
    for (int t = 0; t < 5; ++t) fb[n][t] = lds_b128(lb + ((n - 3) & 1) * 40960 + t * 1024);
    SB();
  };
  auto ld1 = [&](int kind, int n, int i) {
    if (kind == 0) {
      const unsigned so = n >= 7 ? OFF_Q + (mt2 * 20 + (w - 4) * 5 + n - 7) * 4096
                                 : n == 6 && w >= 6 ? OOB : ab + (n < 3 ? 30 + 8 * n : 8 * n - 24) * 4096;
      fa[n][i] = ld16(rws, lane * 16, so + ((i + w) & 3) * 1024);
    } else fb[n][i] = ld16(rws, lane * 16, OFF_MP + (j * 20 + (w - 4) * 5 + n - 7) * 5120 + i * 1024);
    SB();
  };
  LAS char* ex = lds + w * 5120 + lane * 16;
  constexpr int PRE = ATT ? 6 : 3;
#pragma unroll
  for (int n = 0; n < PRE; ++n) ldA(n);
  if (ATT) {
#pragma unroll
    for (int t = 0; t < 5; ++t) {
      const f32x2 x0 = unpk(xr[t][0]), x1 = unpk(xr[t][1]);
      acc[0][t] = f32x4{x0[0], x0[1], x1[0], x1[1]};
      PIN(acc[0][t]);
    }
    SB();
    if (w == 7 && wt.status != 7u) wt.poll(false);
    asm volatile("s_nop 1" ::: "memory");
  }
  constexpr int A1[12] = {ATT ? -1 : 3, ATT ? 6 : 5, ATT ? 7 : -1, -1, ATT ? 8 : -1, -1, ATT ? 9 : -1, 10, 11, -1, -1, -1};
  constexpr int A2[12] = {ATT ? -1 : 4, ATT ? -1 : 6, -1, -1, -1, -1, -1, -1, -1, -1, -1, -1};
  constexpr int M1[12] = {-1, -1, -1, ATT ? 7 : -1, -1, ATT ? 8 : -1, ATT ? 9 : -1, 10, 11, -1, -1, -1};
  static_for<(ATT ? 12 : 7)>([&](auto N) {
    constexpr int n = N;
    if (n == 2) lds_wait(lf + LF_DMA, 8u);
    if (ATT && n == 2) wt.need(S_Q);
    if (ATT && n == 3) { wt.need(S_M); STAMP(7); }
    if (n >= 2 && n < 6) rdB(n + 1);
    if (n == (ATT ? 11 : 6)) lds_wait(lf + LF_BC + ATT, 4u);
#pragma unroll
    for (int q = 0; q < 20; ++q) {
      constexpr int LASTN = ATT ? 11 : 6;
      const int m = n == LASTN ? (q / 5 + 1) & 3 : n < 7 ? q / 5 : q & 3, t = n < 7 || n == LASTN ? q % 5 : q >> 2;
      if (n == 0 && (m > 0 || !ATT)) mfma_new(acc[m][t], br[0][t], fa[0][m]);
      else mfma_acc(acc[m][t], n < 3 ? br[n < 3 ? n : 0][t] : fb[n][t], fa[n][m]);
      if (n == LASTN && q >= 10) {
        SB();
        *(LAS f32x4*)(ex + ((q - 10) / 5) * 40960 + (q % 5) * 1024) = acc[(q - 10) / 5 + 1][q % 5];
        SB();
      }
      if (q & 1) {
        const int p = q >> 1, na = (A1[n] >= 0 ? 4 : 0) + (A2[n] >= 0 ? 4 : 0);
        if (p < na) ld1(0, p < 4 ? A1[n] : A2[n], p & 3);
        else if (M1[n] >= 0 && p - na < 5) ld1(1, M1[n], p - na);
      }
    }
    SB();
    if (n == 0) STAMP(9);
    if (n == 6) STAMP(11);
    if (n == 5) { WAIT_L0(); lds_inc(lf + LF_BC + ATT, lane); }
  });
}

DEVI void phase2(LAS char* lds, const char* x, char* ws, rsrc_t rws, char* out, Watch& wt, u32x4 (&br)[3][5],
                 int mt2, int j, int w, int lane) {
  const int a = w & 3;
  LAS unsigned* lf = wt.lf;
  const unsigned obase = (mt2 * 64 + a * 16) * 1280 + j * 160;
  f32x4 acc[4][5];
  u32x2 xr[5];
  if (w >= 4) {
    const rsrc_t rx = __builtin_amdgcn_make_buffer_rsrc((void*)x, 0, 2048 * 1280, 0x00020000);
#pragma unroll
    for (int t = 0; t < 5; ++t) xr[t] = __builtin_amdgcn_raw_buffer_load_b64(rx, (lane & 15) * 1280 + (lane >> 4) * 8 + t * 32, obase, 0);
    SB();
  }
  if (wt.preps != 0) { if (w == 7 && !(wt.status & S_H)) wt.poll(false); WAIT_V(0); wt.publish(); STAMP(13); }
  else WAIT_V(15);
  lds_inc(lf + LF_DMA, lane);
  wt.need(S_H);
  STAMP(6);
  if (w < 4) {
    __builtin_amdgcn_s_setprio(2);
    p2_steps<false>(lds, rws, wt, br, acc, xr, mt2, j, w, lane);
    __builtin_amdgcn_s_setprio(0);
#pragma unroll
    for (int q = 0; q < 15; ++q) asm volatile("" : "=v"(br[q / 5][q % 5]));
  }
  PINS(w);
  if (w >= 4) { __builtin_amdgcn_s_setprio(1); p2_steps<true>(lds, rws, wt, br, acc, xr, mt2, j, w, lane); }
  asm volatile("s_nop 7" ::: "memory");
  SB();
  STAMP(14);
  LAS char* ex = lds + (w >> 2) * 20480 + lane * 16;
#pragma unroll
  for (int t = 0; t < 5; ++t) *(LAS f32x4*)(ex + 2 * 40960 + (a * 5 + t) * 1024) = acc[3][t];
  WAIT_L0();
  if (w < 4) { lds_inc(lf + LF_X, lane); lds_wait(lf + LF_X, 4u); }
  else BAR();
  STAMP(8);
#pragma unroll
  for (int t = 0; t < 5; ++t)
#pragma unroll
    for (int q = 1; q < 4; ++q) acc[0][t] += *(LAS f32x4*)(ex + (q - 1) * 40960 + (((a - q) & 3) * 5 + t) * 1024);
  LAS char* base = lds + L2_BASE + a * 5120 + lane * 16;
  if (w < 4) {
#pragma unroll
    for (int t = 0; t < 5; ++t) *(LAS f32x4*)(base + t * 1024) = acc[0][t];
    WAIT_L0();
  } else {
    const rsrc_t ro = __builtin_amdgcn_make_buffer_rsrc(out, 0, 2048 * 1280, 0x00020000);
#pragma unroll
    for (int t = 0; t < 5; ++t) {
      const f32x4 s = acc[0][t] + *(LAS f32x4*)(base + t * 1024);
      __builtin_amdgcn_raw_buffer_store_b64(u32x2{pk2(lo2(s)), pk2(hi2(s))}, ro, (lane & 15) * 1280 + (lane >> 4) * 8 + t * 32, obase, 0);
    }
  }
}

__global__ __launch_bounds__(512, 2) void fused(const char* x, const char* wqkv, const char* wo, const char* wg,
                                                const char* wu, const char* wd, char* ws, char* out) {
  extern __shared__ __attribute__((aligned(16))) char smem[];
  const char* pc0;
  asm volatile("s_getpc_b64 %0" : "=s"(pc0));
  asm volatile("" : "+s"(x), "+s"(wqkv), "+s"(wo), "+s"(wg), "+s"(wu), "+s"(wd), "+s"(ws), "+s"(out));
  LAS char* lds = (LAS char*)smem;
  const unsigned ldsb = (unsigned)(size_t)lds;
  const int bid = blockIdx.x, xcd = bid & 7, idx = bid >> 3;
  const int Mt = (xcd & 1) * 8 + (idx & 7), grp = xcd >> 1, pos4 = idx >> 3, slot2 = grp * 4 + pos4;
  const int cslot = (int)(0xCF76BE54AD329810ull >> (4 * slot2)) & 15;
  const int w = __builtin_amdgcn_readfirstlane(threadIdx.x >> 6), lane = threadIdx.x & 63;
  STAMP(0);
  const rsrc_t rws = __builtin_amdgcn_make_buffer_rsrc(ws, 0, WS_END, 0x00020000);
  u64 tag;
  Watch wt{(LAS unsigned*)(lds + L2_FLAG), ws, 0ull, Mt * 2 + (slot2 >> 3), slot2 & 7, w, lane, 0u, 0, 0, 0, 0u};
  u32x4 br[3][5];
  PF2 pf{ws, rws, ldsb, slot2 & 7, w, lane, br};
  if (cslot == 15) {
    tag = make_tag(ws);
    PINS(tag);
    wt.tag = tag;
    if (threadIdx.x < 16) ((LAS unsigned*)(lds + L2_FLAG))[threadIdx.x] = 0u;
    if (WARM) warm_up(x, wqkv, wo, wg, wu, wd, ws, pc0, ldsb + L1_GR + 8192, bid, w, lane);
    retile(wd, rws, ldsb + w * 16384, Mt, w, lane);
    WAIT_V(0); WAIT_L0(); BAR();
    if (threadIdx.x == 0) { set_flag(ws, OFF_FR + Mt * 8, tag); set_flag(ws, OFF_F1 + (Mt * 16 + 15) * 8, tag); }
    STAMP(5);
    u32x4 wf[2][5];
    {
      const u32x4 ro = srd(wo, kD * 1280);
#pragma unroll 1
      for (int q = 0; q < 10; ++q)
        dma<0>(ro, (lane >> 3) * 1280 + (lane & 7) * 16, (Mt * 40 + (q >> 1) * 8) * 1280 + ((w * 160) & ~127) + (q & 1) * 128, ldsb + w * 16384 + q * 1024);
      WAIT_V(0);
#pragma unroll
      for (int hh = 0; hh < 2; ++hh) {
        unsigned ra[2];
#pragma unroll
        for (int h2 = 0; h2 < 2; ++h2) {
          int row = hh * 20 + (lane >> 4) * 8 + h2 * 4 + ((lane & 15) >> 2);
          row = row < 40 ? row : 39;
          ra[h2] = ldsb + w * 16384 + (row >> 3) * 2048 + (row & 7) * 128 + (lane & 3) * 8;
        }
#pragma unroll
        for (int i = 0; i < 5; ++i) {
          const int cb = (w & 3) * 32 + i * 32, o = (cb >> 7) * 1024 + (cb & 127);
          const u32x2 lo = __builtin_bit_cast(u32x2, __builtin_amdgcn_ds_read_tr16_b64_v4i16((LAS s16x4*)(size_t)(ra[0] + o)));
          const u32x2 hi = __builtin_bit_cast(u32x2, __builtin_amdgcn_ds_read_tr16_b64_v4i16((LAS s16x4*)(size_t)(ra[1] + o)));
          wf[hh][i] = u32x4{lo[0], lo[1], hi[0], hi[1]};
        }
      }
    }
    wait_flags(ws, OFF_FR + (lane & 15) * 8, tag);
    WAIT_L0(); BAR();
    wd_prefetch<0, 6>(srd(ws, WS_END), ldsb, slot2 & 7, w, lane);
    pf.items<0, 28>();
    if (Mt == 0 && w == 7) {
#pragma unroll 1
      for (int q = 0; q < 4; ++q) wait_flags(ws, OFF_F1 + (q * 64 + lane) * 8, tag);
      if (lane == 0) set_flag(ws, OFF_E, tag);
    }
    fold_kv(lds, ws, rws, wt, wf, Mt, w, lane);
  } else {
    const int type = cslot < 9 ? 0 : cslot < 13 ? 1 : 2;
    P1 c;
    c.w = w; c.mw = w >> 2; c.nw = w & 3; c.Mt = Mt; c.cslot = cslot; c.j = slot2 & 7;
    c.ldsb = ldsb;
    {
#define ROTM 1
      const int kb = (Mt + cslot) % 10, rot = ROTM == 0 ? (kb >= 5 ? 5 : 0) : ROTM == 1 ? (kb > 7 ? 7 : kb) : (kb / 3) * 3 - (kb == 9 ? 3 : 0);
      c.bstar = kb - rot;
      const int seg = w >> 2;
      c.pitch = type == 0 ? kF * 2 : kQKV * 2;
      const int startb = type == 0 ? cslot * 384
                       : ((type == 1 ? (640 * (1 + seg) + 160 * (cslot - 9)) * 2 : 320 * (2 * (cslot - 13) + seg)) & ~127);
      const char* wb = type == 0 ? (seg ? wu : wg) : wqkv;
      c.rw = srd(wb + startb - 2048, 640 * c.pitch - startb + 2048);
      c.rx = srd(x + (size_t)Mt * 128 * 1280, 128 * 1280);
      const int i = lane >> 3, cp = lane & 7, pg = w & 3;
      c.voffB = (unsigned)(8 * pg + i) * c.pitch + 16 * (cp ^ ((i & 2) | ((pg & 1) << 2))) + 2048;
      c.voffA = (unsigned)(8 * w + i) * 1280 + 16 * (cp ^ ((4 * w + (i >> 1)) & 7));
      c.oB = rot * 64 * c.pitch; c.oA = rot * 128;
      dma<0>(c.rx, c.voffA, c.oA, c.ldsb + L1_A + w * 1024);
    }
    auto pro = [&](int s) {
      const unsigned lB = c.ldsb + L1_B + w * 3072, lA = c.ldsb + L1_A + w * 1024;
      dma<0>(c.rw, c.voffB, c.oB, lB + s * L1_BST);
      dma<128>(c.rw, c.voffB, c.oB, lB + s * L1_BST + 1024);
      dma<256>(c.rw, c.voffB, c.oB, lB + s * L1_BST + 2048);
      c.oB += 32 * c.pitch;
      dma<0>(c.rx, c.voffA, c.oA + ((s + 1) >> 1) * 128 + ((s + 1) & 1) * 81920, lA + (s + 1) * 8192);
      SB();
    };
#define PSPLIT 1
#pragma unroll
    for (int s = 0; s < PSPLIT; ++s) pro(s);
    tag = make_tag(ws);
    PINS(tag);
    wt.tag = tag;
    unsigned vb[5], va0;
    {
      const int g = lane >> 4, q = (lane & 15) >> 2, p = lane & 3, r = lane & 15, nw = c.nw;
      const int swz = (q & 2) | ((g & 1) << 2);
#pragma unroll
      for (int pos = 0; pos < 5; ++pos) {
        const int cf = 16 * nw + 4 * p;
        const int hb = (cslot & 1 ? 0 : 32) + 40 * nw;
        const int ck = pos < 4 ? (pos >> 1) * 192 + hb + 20 * (pos & 1) + 4 * p : (p >> 1) * 192 + hb + 20 * (p & 1) + 16;
        const int cq = (nw >> 1) * 224 + 20 * (4 * (nw & 1) + 2 * (p & 1) + (p >> 1)) + 4 * pos;
        const int col = type == 0 ? cf : type == 1 ? ck : cq;
        const int line = col >> 6, seg = line >= 3;
        vb[pos] = c.ldsb + L1_B + seg * (12288 - 3072) + (g * 3 + line) * 1024 + q * 128 + 16 * (((col & 63) >> 3) ^ swz) + 8 * ((col >> 2) & 1);
      }
      va0 = c.ldsb + L1_A + (c.mw * 64 + r) * 128 + 16 * (g ^ ((r >> 1) & 7));
    }
    if (threadIdx.x < 16) ((LAS unsigned*)(lds + L2_FLAG))[threadIdx.x] = 0u;
    f32x4 acc[4][6];
#pragma unroll
    for (int m = 0; m < 4; ++m)
#pragma unroll
      for (int i = 0; i < 6; ++i) acc[m][i] = f32x4{0.f, 0.f, 0.f, 0.f};
    unsigned foff = lane >= 20 ? OFF_FR + (lane & 15) * 8 : OFF_F1 + (lane < 10 ? lane * 16 + cslot : Mt * 16 + lane - 10) * 8;
    PIN(foff);
#pragma unroll
    for (int i = 0; i < 5; ++i) PIN(vb[i]);
    SB();
#pragma unroll
    for (int s = PSPLIT; s < 3; ++s) pro(s);
    c.oA += 256;
    if (WARM) warm_up(x, wqkv, wo, wg, wu, wd, ws, pc0, ldsb + L1_GR + 8192, bid, w, lane);
    SB();
#define P1_STAMP
#define P1_RUN(T) do { if (w < 4) p1_loop<T, true>(c, vb, va0, foff, acc, ws, rws, tag, lane); \
                       else p1_loop<T, false>(c, vb, va0, foff, acc, ws, rws, tag, lane); P1_STAMP; } while (0)
    if (type == 0) { P1_RUN(0); epi_ffn(lds, acc, ws, rws, wt, c, pf, lane); }
    else if (type == 2) { P1_RUN(2); epi_q(lds, acc, ws, rws, wt, c, pf, lane); }
    else {
      P1_RUN(1);
      epi_kv(lds, acc, ws, rws, tag, c, lane);
      STAMP(4);
      WAIT_V(0); WAIT_L0(); BAR();
      if (threadIdx.x < 8) set_flag(ws, OFF_FK + threadIdx.x * 512 + (cslot - 9) * 128 + Mt * 8, tag);
      STAMP(5);
      pf.items<0, 28>();
    }
  }
  phase2(lds, x, ws, rws, out, wt, br, Mt * 2 + (slot2 >> 3), slot2 & 7, w, lane);
  STAMP(10);
}

extern "C" void kernel_launch(void* const* d_in, const int* in_sizes, int n_in, void* d_out, int out_size,
                              void* d_ws, size_t ws_size, hipStream_t stream) {
  (void)in_sizes; (void)n_in; (void)out_size; (void)ws_size;
  (void)hipFuncSetAttribute((const void*)fused, hipFuncAttributeMaxDynamicSharedMemorySize, LDS_SIZE);
  fused<<<256, 512, LDS_SIZE, stream>>>((const char*)d_in[0], (const char*)d_in[1], (const char*)d_in[2], (const char*)d_in[4],
                                        (const char*)d_in[5], (const char*)d_in[6], (char*)d_ws, (char*)d_out);
}
```

```cpp
#include <hip/hip_runtime.h>
#include <stdint.h>
#include <type_traits>

#define GAS __attribute__((address_space(1)))
#define LAS __attribute__((address_space(3)))
typedef __attribute__((ext_vector_type(2))) __bf16 bf16x2;
typedef __attribute__((ext_vector_type(8))) __bf16 bf16x8;
typedef __attribute__((ext_vector_type(4))) short s16x4;
typedef __attribute__((ext_vector_type(4))) float f32x4;
typedef __attribute__((ext_vector_type(2))) float f32x2;
typedef __attribute__((ext_vector_type(4))) unsigned u32x4;
typedef __attribute__((ext_vector_type(2))) unsigned u32x2;
typedef unsigned long long u64;
typedef __amdgpu_buffer_rsrc_t rsrc_t;

#define DEVI __device__ __forceinline__
#define WAIT_V_(n) asm volatile("s_waitcnt vmcnt(" #n ")" ::: "memory")
#define WAIT_V(n) WAIT_V_(n)
template <int N> __device__ __forceinline__ void wait_v() { asm volatile("s_waitcnt vmcnt(%0)" :: "i"(N) : "memory"); }
#define WAIT_L0() do { __builtin_amdgcn_s_waitcnt(0xC07F); asm volatile("" ::: "memory"); } while (0)
#define SB() __builtin_amdgcn_sched_barrier(0)
#define PIN(x) asm volatile("" : "+v"(x))
#define PINS(x) asm volatile("" : "+s"(x))
#define BAR() do { asm volatile("" ::: "memory"); __builtin_amdgcn_s_barrier(); asm volatile("" ::: "memory"); } while (0)
#define RLX_AGENT __ATOMIC_RELAXED, __HIP_MEMORY_SCOPE_AGENT
#define RLX_WG __ATOMIC_RELAXED, __HIP_MEMORY_SCOPE_WORKGROUP
#define SC1 16
#define OUT_AUX 0

#define STAMP(i)

constexpr int kD = 640, kF = 1728, kQKV = 1920;
constexpr unsigned OFF_E   = 0;
constexpr unsigned OFF_F1  = 256;
constexpr unsigned OFF_FR  = OFF_F1 + 2048;
constexpr unsigned OFF_FK  = 2560;
constexpr unsigned OFF_F3  = OFF_FK + 4096;
constexpr unsigned OFF_FH  = OFF_F3 + 1024;
constexpr unsigned OFF_FZ  = 16384;
constexpr unsigned OFF_ZP  = OFF_FZ + 4096;
constexpr unsigned OFF_SC  = OFF_ZP + 4 * 32 * 640;
constexpr unsigned OFF_SR  = OFF_SC + 15 * 10 * 384 * 4;
constexpr unsigned OFF_KVP = OFF_SR + 16 * 10 * 256 * 4;
constexpr unsigned OFF_MP  = OFF_KVP + 16 * 16 * 3456;
constexpr unsigned OFF_WD  = OFF_MP + 8 * 20 * 5 * 1024;
constexpr unsigned OFF_Q   = OFF_WD + 8 * 54 * 5 * 1024;
constexpr unsigned OFF_H   = OFF_Q + 32 * 20 * 4096;
constexpr unsigned WS_END  = OFF_H + 32 * 54 * 4096;
constexpr unsigned OOB = 0x80000000u;
static_assert(OFF_FH + 8192 <= OFF_FZ && OFF_MP % 1024 == 0 && OFF_KVP % 128 == 0, "align");

constexpr int L1_B = 0, L1_BST = 24576;
constexpr int L1_A = 4 * L1_BST, L1_AST = 16384;
constexpr int L1_GR = L1_A + 3 * L1_AST;
constexpr int L1_GC = L1_A + L1_AST;
constexpr int L1_ST = 158720;
constexpr int L1_CS = L1_ST + 1024;
constexpr int L1_EX = 72 * 1024;
constexpr int L2_BASE = 120 * 1024;
constexpr int L2_RED = 150 * 1024;
constexpr int L2_FLAG = 162816;
constexpr int LDS_SIZE = L2_FLAG + 64;
enum { LF_STATUS = 0, LF_DMA = 1, LF_K = 2, LF_X = 3, LF_BC = 4, LF_PUB = 8 };
enum { S_H = 1, S_Q = 2, S_M = 4 };

DEVI f32x4 mfma(u32x4 a, u32x4 b, f32x4 c) {
  return __builtin_amdgcn_mfma_f32_16x16x32_bf16(__builtin_bit_cast(bf16x8, a), __builtin_bit_cast(bf16x8, b), c, 0, 0, 0);
}
DEVI void mfma_acc(f32x4& c, u32x4 a, u32x4 b) { asm volatile("v_mfma_f32_16x16x32_bf16 %0, %1, %2, %0" : "+v"(c) : "v"(a), "v"(b)); }
DEVI void mfma_new(f32x4& c, u32x4 a, u32x4 b) { asm volatile("v_mfma_f32_16x16x32_bf16 %0, %1, %2, 0" : "=&v"(c) : "v"(a), "v"(b)); }
DEVI unsigned pk2(f32x2 f) { return __builtin_bit_cast(unsigned, __builtin_convertvector(f, bf16x2)); }
DEVI unsigned pk2(float lo, float hi) { return pk2(f32x2{lo, hi}); }
DEVI f32x2 unpk(unsigned u) { return f32x2{__builtin_bit_cast(float, u << 16), __builtin_bit_cast(float, u & 0xffff0000u)}; }
DEVI f32x2 fma2(f32x2 a, f32x2 b, f32x2 c) { return __builtin_elementwise_fma(a, b, c); }
DEVI f32x2 bc2(float v) { return f32x2{v, v}; }
DEVI f32x2 lo2(f32x4 v) { return f32x2{v[0], v[1]}; }
DEVI f32x2 hi2(f32x4 v) { return f32x2{v[2], v[3]}; }
DEVI f32x2 phi2(f32x2 g, f32x2 rs) {
  const f32x2 t = fma2(g, rs, bc2(1e-6f)), l = g * (rs * 1.4426950408889634f);
  return f32x2{fmaxf(t[0], 1e-6f), fmaxf(t[1], 1e-6f)} +
         f32x2{__builtin_amdgcn_fmed3f(__builtin_amdgcn_exp2f(l[0]), 0.f, 1.f), __builtin_amdgcn_fmed3f(__builtin_amdgcn_exp2f(l[1]), 0.f, 1.f)};
}
DEVI u32x4 lds_b128(unsigned a) { return *(LAS u32x4*)(size_t)a; }
DEVI u32x4 lds_trfrag(unsigned a) {
  const u32x2 lo = __builtin_bit_cast(u32x2, __builtin_amdgcn_ds_read_tr16_b64_v4i16((LAS s16x4*)(size_t)a));
  const u32x2 hi = __builtin_bit_cast(u32x2, __builtin_amdgcn_ds_read_tr16_b64_v4i16((LAS s16x4*)(size_t)(a + 512)));
  return u32x4{lo[0], lo[1], hi[0], hi[1]};
}
DEVI u32x4 srd(const void* p, unsigned bytes) {
  const u64 a = (u64)p;
  return u32x4{(unsigned)a, (unsigned)(a >> 32) & 0xffffu, bytes, 0x00020000u};
}
DEVI void setm0(unsigned v) { asm volatile("s_mov_b32 m0, %0" :: "s"(v) : "memory"); }
template <int IMM>
DEVI void dma_n(u32x4 r, unsigned voff, unsigned soff) {
  asm volatile("buffer_load_dwordx4 %0, %1, %2 offen offset:%3 lds" :: "v"(voff), "s"(r), "s"(soff), "i"(IMM) : "memory");
}
template <int IMM>
DEVI void dma(u32x4 r, unsigned voff, unsigned soff, unsigned lds) {
  asm volatile("s_mov_b32 m0, %4\n\ts_nop 0\n\tbuffer_load_dwordx4 %0, %1, %2 offen offset:%3 lds"
               :: "v"(voff), "s"(r), "s"(soff), "i"(IMM), "s"(lds - IMM) : "memory");
}
DEVI void dma_sc1(u32x4 r, unsigned voff, unsigned soff, unsigned lds) {
  asm volatile("s_mov_b32 m0, %3\n\ts_nop 0\n\tbuffer_load_dwordx4 %0, %1, %2 offen sc1 lds"
               :: "v"(voff), "s"(r), "s"(soff), "s"(lds) : "memory");
}
DEVI void ld8_hidden(u32x2& v, u32x4 r, unsigned voff) {
  asm volatile("buffer_load_dwordx2 %0, %1, %2, 0 offen sc1" : "+v"(v) : "v"(voff), "s"(r) : "memory");
}
template <int N0, int N1>
DEVI void wd_prefetch(u32x4 rws, unsigned ldsb, int j, int w, int lane) {
#pragma unroll
  for (int n = N0; n < N1; ++n) {
    const unsigned a = (unsigned)(w + 8 * n) * 1024;
    if (n < 18 || w < 6) dma_sc1(rws, lane * 16, OFF_WD + j * 276480 + a, ldsb + a);
  }
}
DEVI void wait_flags(char* ws, unsigned off, u64 tag) {
#pragma unroll 1
  for (unsigned spins = 0; spins < (1u << 20); ++spins) {
    const u64 v = __hip_atomic_load((GAS u64*)(ws + off), RLX_AGENT);
    if (__all(v == tag)) break;
    __builtin_amdgcn_s_sleep(1);
  }
  asm volatile("" ::: "memory");
}
DEVI u64 make_tag(const char* ws) {
  return *(const __attribute__((address_space(4))) u64*)(ws + OFF_E) * 0x9E3779B97F4A7C15ull + 0xD1B54A32D192ED03ull;
}
DEVI void set_flag(char* ws, unsigned off, u64 tag) { __hip_atomic_store((GAS u64*)(ws + off), tag, RLX_AGENT); }
DEVI unsigned lds_inc(LAS unsigned* p, int lane) {
  unsigned o = 0;
  if (lane == 0) o = __hip_atomic_fetch_add(p, 1u, RLX_WG);
  return __builtin_amdgcn_readfirstlane(o);
}
DEVI void lds_wait_bits(LAS unsigned* p, unsigned bits) {
  while ((~(unsigned)__builtin_amdgcn_readfirstlane(__hip_atomic_load(p, RLX_WG)) & bits) != 0u) __builtin_amdgcn_s_sleep(1);
  asm volatile("" ::: "memory");
}
DEVI void lds_wait(LAS unsigned* p, unsigned want) {
  while ((unsigned)__builtin_amdgcn_readfirstlane(__hip_atomic_load(p, RLX_WG)) < want) __builtin_amdgcn_s_sleep(1);
  asm volatile("" ::: "memory");
}
DEVI u32x4 ld16(rsrc_t r, unsigned voff, unsigned soff) { return __builtin_amdgcn_raw_buffer_load_b128(r, voff, soff, SC1); }
DEVI void st16(rsrc_t r, unsigned voff, unsigned soff, u32x4 v) { __builtin_amdgcn_raw_buffer_store_b128(v, r, voff + soff, 0, SC1); }
DEVI void st8(rsrc_t r, unsigned voff, unsigned soff, u32x2 v) { __builtin_amdgcn_raw_buffer_store_b64(v, r, voff, soff, SC1); }

#define WARM 4
#define PRO_WAIT_(n) n
#define PRO_WAIT 9
constexpr unsigned CODE_BYTES = 60 * 1024;
DEVI void warm_up(const char* x, const char* wqkv, const char* wo, const char* wg, const char* wu, const char* wd,
                  const char* ws, const char* pc0, unsigned junk, int bid, int w, int lane) {
  const int cid = bid * 6 + w;
  const char* p = cid < 320 ? x : cid < 620 ? wqkv : cid < 890 ? wg : cid < 1160 ? wu : cid < 1430 ? wd : wo;
  unsigned o = (cid - (cid < 320 ? 0 : cid < 620 ? 320 : cid < 890 ? 620 : cid < 1160 ? 890 : cid < 1430 ? 1160 : 1430)) * 8192;
  unsigned n = (WARM & 1) && cid < 1530 ? o + 8192 : 0, sh = 7;
  if (w == 6) { p = ws; o = 0; n = (WARM & 2) ? WS_END : 0; sh = 18; }
  if (w == 7) { p = pc0; o = (bid >> 3) * 8192; n = (WARM & 4) ? CODE_BYTES : 0; }
  asm volatile("s_mov_b32 m0, %3\n\ts_nop 0\n\tbuffer_load_dword %0, %1, %2 offen lds"
               :: "v"(lane << sh), "s"(srd(p, n)), "s"(o), "s"(junk) : "memory");
}

struct P1 {
  u32x4 rw, rx;
  unsigned ldsb, pitch;
  unsigned voffB, voffA;
  unsigned oB, oA;
  int w, mw, nw, bstar;
  int Mt, cslot, j;
};

struct PF2 {
  char* ws; rsrc_t rws; unsigned ldsb; int j, w, lane;
  u32x4 (&br)[3][5];
  template <int N> DEVI void item() {
    if constexpr (N < 13) wd_prefetch<6 + N, 7 + N>(srd(ws, WS_END), ldsb, j, w, lane);
    else br[(N - 13) / 5][(N - 13) % 5] = ld16(rws, lane * 16, OFF_WD + (j * 54 + 30 + w + 8 * ((N - 13) / 5)) * 5120 + ((N - 13) % 5) * 1024);
    SB();
  }
  template <int N0, int N1> DEVI void items() { if constexpr (N0 < N1) { item<N0>(); items<N0 + 1, N1>(); } }
};

#define ABL_RD 0
#define EB 3
#define AINC 1
template <int TYPE, bool LATE>
DEVI void p1_loop(P1& c, unsigned (&vb)[5], unsigned va0, unsigned foff, f32x4 (&acc)[4][6], PF2& pf, char* ws, rsrc_t rws, u64 tag, int lane) {
  constexpr int NT = TYPE == 0 ? 6 : 5;
  constexpr bool AC = !LATE || AINC;
  u32x4 fa[4], fb[NT];
  unsigned va1 = va0 ^ 64;
  const unsigned stepB = 32 * c.pitch, limB = 640 * c.pitch;
  const unsigned lB0 = c.ldsb + L1_B + c.w * 3072, lA0 = c.ldsb + L1_A + c.w * 1024;
  unsigned lB = lB0 + 3 * L1_BST, lA = lA0 + 2 * L1_AST;
  unsigned vB1 = c.voffB - 896, vB2 = c.voffB - 1792, vA1 = c.voffA + 81920;
  PIN(vB1); PIN(vB2); PIN(vA1);
  int dA = L1_AST, dB = 2 * L1_BST, rare = c.bstar, rare2 = 1;
  f32x4 st[5];
  auto adB = [&](int i) { return TYPE == 0 ? vb[!LATE && i >= 2 ? 1 : 0] + (i / 3) * 12288 + (i % 3) * 1024 : vb[i < 5 ? i : 4]; };
  if (TYPE == 0) { vb[1] = vb[0]; PIN(vb[1]); }
  auto upB = [&](int i0, int i1) {
    if (TYPE == 0) { vb[i0 ? 1 : 0] += dB; PIN(vb[i0 ? 1 : 0]); }
    else {
#pragma unroll
      for (int i = i0; i < i1; ++i) { vb[i] += dB; PIN(vb[i]); }
    }
  };
  auto rdA = [&](int i, int sub) { if (ABL_RD & 1) { PIN(fa[i]); return; } fa[i] = lds_b128((sub ? va1 : va0) + i * 2048); };
  auto rdB = [&](int i, int sub) { if (ABL_RD & 2) { PIN(fb[i]); return; } fb[i] = lds_trfrag(adB(i) + sub * L1_BST); };
  auto rdBh = [&](int i, int hh, int sub) {
    if (ABL_RD & 2) { PIN(fb[i]); return; }
    const u32x2 v = __builtin_bit_cast(u32x2, __builtin_amdgcn_ds_read_tr16_b64_v4i16((LAS s16x4*)(size_t)(adB(i) + sub * L1_BST + hh * 512)));
    fb[i][2 * hh] = v[0]; fb[i][2 * hh + 1] = v[1];
  };
  auto pieceA = [&](int sub) { dma_n<0>(c.rx, sub ? vA1 : c.voffA, c.oA); };
  auto L = [&](int sub, int mode) {
    if (mode) dma_n<0>(c.rw, c.voffB, c.oB);
    SB(); rdA(0, sub); rdA(1, sub); SB();
    if (mode) dma_n<1024>(c.rw, vB1, c.oB);
    SB(); rdA(2, sub); rdA(3, sub); SB();
    if (mode) dma_n<2048>(c.rw, vB2, c.oB);
    SB(); rdB(LATE ? 4 : 0, sub); SB();
    if (mode == 2) setm0(lA + sub * 8192);
    SB(); if (!LATE) rdB(1, sub); else if (NT == 6) rdB(NT - 1, sub); else if (mode == 2) asm volatile("s_nop 0"); SB();
    if (mode == 2) pieceA(sub);
    if (!LATE && !sub) upB(2, 5);
    SB();
  };
  auto U = [&](int sub) {
    if (sub) { va1 += dA; PIN(va1); upB(0, LATE ? 5 : 2); }
    else { va0 += dA; PIN(va0); }
    SB();
  };
  auto stats = [&](int sub) {
    const u32x4 one = {0x3f803f80u, 0x3f803f80u, 0x3f803f80u, 0x3f803f80u};
    const u32x4 sa = lds_b128((sub ? va1 : va0) + c.nw * 2048);
    u32x4 sb[3];
#pragma unroll
    for (int i = 0; i < 3; ++i) { unsigned a = adB(LATE ? i : 3 + i); PIN(a); sb[i] = lds_trfrag(a + sub * L1_BST); }
    SB();
    if (sub == 0) {
#pragma unroll
      for (int i = 0; i < 5; ++i) st[i] = f32x4{0.f, 0.f, 0.f, 0.f};
    }
    st[0] = mfma(one, sa, st[0]);
    st[1] = mfma(sa, sa, st[1]);
#pragma unroll
    for (int i = 0; i < 3; ++i) st[2 + i] = mfma(one, sb[i], st[2 + i]);
    if (sub == 1) {
      const int cl = lane & 15, g = lane >> 4;
      const float sq = (cl & 2) ? ((cl & 1) ? st[1][3] : st[1][2]) : ((cl & 1) ? st[1][1] : st[1][0]);
      st8(rws, (cl >> 2) == g ? (c.mw * 64 + c.nw * 16 + cl) * 8 : OOB, c.cslot < 10 ? OFF_SR + (c.Mt * 10 + c.cslot) * 1024 : WS_END,
          u32x2{__builtin_bit_cast(unsigned, st[0][0]), __builtin_bit_cast(unsigned, sq)});
      const float v = g == 0 ? st[2][0] : g == 1 ? st[3][0] : st[4][0];
      __builtin_amdgcn_raw_buffer_store_b32(__builtin_bit_cast(unsigned, v), rws, g < 3 ? ((c.nw * 6 + c.mw * 3) * 16 + lane) * 4 : OOB,
                                            c.Mt < 10 ? OFF_SC + (c.cslot * 10 + c.Mt) * 1536 : WS_END, SC1);
    }
    SB();
  };
#define SEG(i)
  auto C = [&](int sub, bool more, int book, auto vmwait) {
    SEG(0);
    __builtin_amdgcn_s_setprio(1);
    if (LATE) { vmwait(); WAIT_L0(); BAR(); }
    SB();
#pragma unroll
    for (int k = 0; k < 4 * NT; ++k) {
      const int i = k >> 2, m = k & 3;
      acc[m][i] = TYPE == 1 ? mfma(fa[m], fb[i], acc[m][i]) : mfma(fb[i], fa[m], acc[m][i]);
      if (LATE) { if (more && i >= 1 && i < 5 && m < 2) rdBh(i - 1, m, sub ^ 1); }
      else if (k < 2 * (NT - 2)) rdBh(2 + (k >> 1), k & 1, sub);
      const int q = LATE ? (k < 2 ? k : m >= 2 && i < 5 ? 2 * i + m : -1) : k - 2 * (NT - 2);
      constexpr int QA = LATE ? 8 : 0, QU = LATE ? 0 : 3;
      if (book == 1) {
        if (AC) {
          if (q == QA) setm0(lA + (LATE ? sub ^ 1 : sub) * 8192);
          if (q == QA + 1) pieceA(LATE ? sub ^ 1 : sub);
        }
        if (q == QU) { c.oB += stepB; lB += L1_BST; PINS(c.oB); PINS(lB); }
        if (q == QU + 1) { c.oB = c.oB == limB ? 0u : c.oB; PINS(c.oB); }
        if (q == QU + 2) { lB = lB == lB0 + 4 * L1_BST ? lB0 : lB; PINS(lB); }
        if (sub) {
          if (q == QU + 3) { c.oA += 128; lA += L1_AST; PINS(c.oA); PINS(lA); }
          if (q == QU + 4) { c.oA = c.oA == 1280u ? 0u : c.oA; PINS(c.oA); }
          if (q == QU + 5) { lA = lA == lA0 + 3 * L1_AST ? lA0 : lA; PINS(lA); }
          if (q == QU + 6) { dA = lA == lA0 + L1_AST ? -2 * L1_AST : L1_AST; PINS(dA); }
          if (q == QU + 7) { rare -= 1; PINS(rare); }
        }
        if (!sub && q == QU + 3) { rare2 = LATE ? (rare + 2) & ~2 : rare; PINS(rare2); }
        if (q == 11) setm0(lB);
      }
      if (book && q == 10 && (LATE ? sub : !sub)) { dB = -dB; PINS(dB); }
      if (!LATE && EB > 0) {
        if (k == 4 * NT - 2 - EB) { vmwait(); WAIT_L0(); }
        if (k == 4 * NT - 1 - EB) { __builtin_amdgcn_s_setprio(2); BAR(); }
      }
      SB();
    }
    __builtin_amdgcn_s_setprio(0);
    if (!LATE && EB == 0) { vmwait(); WAIT_L0(); BAR(); }
  };
  constexpr int LM = AC ? 1 : 2;
  if (!LATE) { upB(2, 5); dB = -dB; }
  WAIT_V(PRO_WAIT); BAR();
  if (ABL_RD) {
#pragma unroll
    for (int i = 0; i < 4; ++i) fa[i] = lds_b128(va0 + i * 2048);
#pragma unroll
    for (int i = 0; i < NT; ++i) fb[i] = lds_trfrag(c.ldsb + (lane & 63) * 8 + i * 1024);
  }
  if (LATE) {
    if (AC) { setm0(lA); SB(); }
    rdB(0, 0); rdB(1, 0); SB();
    if (AC) pieceA(0);
  }
  setm0(lB);
  SB();
  if (LATE) { rdB(2, 0); rdB(3, 0); } else asm volatile("s_nop 0");
  SB();
  constexpr int X = TYPE == 0;
  const int rend = c.bstar - (X ? 7 : 8);
  const u32x4 rw4 = srd(ws, WS_END);
  u32x2 fl = {0u, 0u};
#pragma unroll 1
  do {
    if (X && __builtin_expect(rare == rend + 1, 0)) ld8_hidden(fl, rw4, foff);
    L(0, LM);
    if (__builtin_expect(rare == 0, 0)) stats(0);
    U(0);
    C(0, true, 1, []() { WAIT_V(8); });
    L(1, LM);
    if (__builtin_expect(rare2 == 0, 0)) {
      PINS(rare);
      if (rare == 0) stats(1);
      else if (LATE && c.w == 0 && lane == 0) set_flag(ws, OFF_F1 + (c.Mt * 16 + c.cslot) * 8, tag);
    }
    U(1);
    C(1, true, 1, []() { WAIT_V(8); });
  } while (rare != rend);
  auto check = [&]() {
    PIN(fl);
    if (__builtin_expect(!__all(__builtin_bit_cast(u64, fl) == tag), 0)) wait_flags(ws, foff, tag);
  };
  if (X) {
    L(0, LM);
    WAIT_V(11);
    check();
    pf.items<13, 16>();
    U(0);
    C(0, true, 1, []() { WAIT_V(11); });
    L(1, LM);
    pf.items<16, 19>();
    U(1);
    C(1, true, 1, []() { WAIT_V(14); });
  }
  if (!X) ld8_hidden(fl, rw4, foff);
  L(0, 1);
  if (X) pf.items<19, 22>();
  U(0);
  C(0, true, 2, []() { wait_v<X ? 13 : 8>(); });
  L(1, 0);
  wait_v<X ? 6 : 3>();
  if (!X) check();
  dma_sc1(rw4, lane * 16, OFF_SC + c.cslot * 15360 + c.w * 1024, c.ldsb + L1_GC + c.w * 1024);
  dma_sc1(rw4, lane * 16, OFF_SC + c.cslot * 15360 + c.w * 1024 + 8192, c.ldsb + L1_GC + c.w * 1024 + 8192);
  dma_sc1(rw4, lane * 16, OFF_SR + c.Mt * 10240 + c.w * 1024, c.ldsb + L1_GR + c.w * 1024);
  if (LATE && c.w < 2) dma_sc1(rw4, lane * 16, OFF_SR + c.Mt * 10240 + c.w * 1024 + 8192, c.ldsb + L1_GR + c.w * 1024 + 8192);
  if (X) pf.items<22, 24>();
  U(1);
  C(1, true, 2, []() {});
  L(0, 0);
  wd_prefetch<0, 4>(rw4, c.ldsb, c.j, c.w, lane);
  if (X) pf.items<24, 26>();
  C(0, true, 0, []() { wait_v<X ? 8 : 4>(); });
  L(1, 0);
  {
    __builtin_amdgcn_s_setprio(2);
    if (LATE || c.w < 6) {
      const LAS float* src = (const LAS float*)(size_t)(c.ldsb + L1_GC + (c.w * 64 + lane) * 4);
      float v[10];
#pragma unroll
      for (int p = 0; p < 10; ++p) v[p] = src[p * 384];
      *(LAS float*)(size_t)(c.ldsb + L1_CS + (c.w * 64 + lane) * 4) = (((v[0] + v[1]) + (v[2] + v[3])) + ((v[4] + v[5]) + (v[6] + v[7]))) + (v[8] + v[9]);
    } else {
      const LAS f32x2* src = (const LAS f32x2*)(size_t)(c.ldsb + L1_GR + ((c.w - 6) * 64 + lane) * 8);
      f32x2 v[10];
#pragma unroll
      for (int p = 0; p < 10; ++p) v[p] = src[p * 128];
      const f32x2 s = ((((v[0] + v[1]) + (v[2] + v[3])) + ((v[4] + v[5]) + (v[6] + v[7]))) + (v[8] + v[9])) * (1.0f / kD);
      LAS float* d = (LAS float*)(size_t)(c.ldsb + L1_ST + ((c.w - 6) * 64 + lane) * 4);
      d[0] = s[0]; d[128] = __builtin_amdgcn_rsqf(fmaxf(__builtin_fmaf(-s[0], s[0], s[1]), 0.f) + 1e-5f);
    }
    __builtin_amdgcn_s_setprio(0);
  }
  SB();
  wd_prefetch<4, 6>(rw4, c.ldsb, c.j, c.w, lane);
  if (X) pf.items<26, 28>();
  SB();
  C(1, false, 0, []() {});
}

template <int N, class F> DEVI void static_for(F&& f) {
  if constexpr (N > 0) { static_for<N - 1>(f); f(std::integral_constant<int, N - 1>{}); }
}
DEVI u32x4 pair16(u32x2 a, u32x2 b) {
  const auto s0 = __builtin_amdgcn_permlane32_swap(a[0], b[0], false, false);
  const auto s1 = __builtin_amdgcn_permlane32_swap(a[1], b[1], false, false);
  return u32x4{s0[0], s1[0], s0[1], s1[1]};
}
struct Watch {
  LAS unsigned* lf; char* ws; u64 tag; int mt2, j, w, lane;
  unsigned status;
  DEVI u64 fetch() {
    int l2 = lane;
    PIN(l2);
    const unsigned f0 = l2 < 32 ? OFF_FH + mt2 * 256 + (j >> 2) * 128 + (l2 < 11 ? l2 : 0) * 8 : OFF_F3 + (blockIdx.x & 7) * 128 + (l2 & 15) * 8;
    return __hip_atomic_load((GAS u64*)(ws + f0), RLX_AGENT);
  }
  DEVI bool eval(u64 v, bool giveup) {
    const u64 no = __ballot(v != tag);
    unsigned s = ((no & 0x1ffull) ? 0u : S_H) | ((no & 0x600ull) ? 0u : S_Q) | ((no >> 32) ? 0u : S_M);
    if (giveup) s = 7u;
    if (s == status) return false;
    status = s;
    __hip_atomic_store(lf + LF_STATUS, s, RLX_WG);
    return true;
  }
  DEVI bool poll(bool giveup) { return eval(fetch(), giveup); }
  DEVI bool has(unsigned bits) { return (~(unsigned)__builtin_amdgcn_readfirstlane(__hip_atomic_load(lf + LF_STATUS, RLX_WG)) & bits) == 0u; }
  int pcnt, plast, preps; unsigned pflag;
  DEVI void publish() {
    if ((int)lds_inc(lf + LF_PUB + pcnt, lane) == plast && lane < preps) set_flag(ws, pflag + lane * 128, tag);
    preps = 0;
  }
  DEVI void need(unsigned bits, int watcher = 7) {
    if (w != watcher) { lds_wait_bits(lf + LF_STATUS, bits); return; }
#pragma unroll 1
    for (unsigned spins = 0; (~status & bits) != 0u; ++spins)
      if (!poll(spins >= (1u << 20))) __builtin_amdgcn_s_sleep(1);
    asm volatile("" ::: "memory");
  }
};

#define EPI_N 12
DEVI void epi_ffn(LAS char* lds, f32x4 (&acc)[4][6], char* ws, rsrc_t rws, Watch& wt, const P1& c, PF2& pf, int lane) {
  const int r = lane & 15, g = lane >> 4;
  pf.items<0, 2>();
  float nm[4], k1[4], k2[4], iv[4];
#pragma unroll
  for (int mi = 0; mi < 4; ++mi) {
    const LAS float* sp = (const LAS float*)(lds + L1_ST) + c.mw * 64 + mi * 16 + r;
    const float rs = sp[128], c0 = -2.0f * 0.7978845608028654f * 1.4426950408889634f;
    nm[mi] = -sp[0]; k1[mi] = c0 * rs; k2[mi] = c0 * 0.044715f * rs * rs * rs; iv[mi] = __builtin_amdgcn_rcpf(rs * rs);
  }
  const unsigned hb = OFF_H + ((c.Mt * 2 + c.mw) * 54 + 2 * c.cslot + (c.nw >> 1)) * 4096 + (c.nw & 1) * 512 + (lane & 31) * 16 + (lane >> 5) * 1024;
  u32x4 v[6];
  static_for<3>([&](auto I) {
    constexpr int i = I;
    const f32x4 cg = *(LAS f32x4*)(lds + L1_CS + ((c.nw * 6 + i) * 16 + 4 * g) * 4);
    const f32x4 cu = *(LAS f32x4*)(lds + L1_CS + ((c.nw * 6 + 3 + i) * 16 + 4 * g) * 4);
    u32x2 o[4];
    static_for<4>([&](auto MI) {
      constexpr int mi = MI, n = 4 * i + mi;
      if constexpr (2 + 2 * n < EPI_N) pf.template items<2 + 2 * n, 4 + 2 * n>();
#pragma unroll
      for (int hh = 0; hh < 2; ++hh) {
        const f32x2 G = fma2(bc2(nm[mi]), hh ? hi2(cg) : lo2(cg), hh ? hi2(acc[mi][i]) : lo2(acc[mi][i]));
        const f32x2 U = fma2(bc2(nm[mi]), hh ? hi2(cu) : lo2(cu), hh ? hi2(acc[mi][3 + i]) : lo2(acc[mi][3 + i]));
        const f32x2 z = G * fma2(G * G, bc2(k2[mi]), bc2(k1[mi]));
        const f32x2 d = fma2(f32x2{__builtin_amdgcn_exp2f(z[0]), __builtin_amdgcn_exp2f(z[1])}, bc2(iv[mi]), bc2(iv[mi]));
        o[mi][hh] = pk2(G * U * f32x2{__builtin_amdgcn_rcpf(d[0]), __builtin_amdgcn_rcpf(d[1])});
      }
      PIN(o[mi]);
      SB();
#define STN 4
#define STS 4
      if constexpr (n == STN || n == STN + STS) {
        constexpr int p = (n - STN) / STS;
        st16(rws, hb, p * 18 * 4096, v[2 * p]);
        st16(rws, hb, p * 18 * 4096 + 2048, v[2 * p + 1]);
        SB();
      }
    });
    v[2 * i] = pair16(o[0], o[1]); v[2 * i + 1] = pair16(o[2], o[3]);
    PIN(v[2 * i]); PIN(v[2 * i + 1]);
  });
  st16(rws, hb, 36 * 4096, v[4]);
  st16(rws, hb, 36 * 4096 + 2048, v[5]);
  STAMP(4);
  BAR();
  WAIT_V(0);
  wt.pcnt = c.mw; wt.plast = 3; wt.preps = 2; wt.pflag = OFF_FH + (c.Mt * 2 + c.mw) * 256 + c.cslot * 8;
  wt.publish();
  STAMP(5);
  pf.items<EPI_N, 13>();
  WAIT_V(0);
}

DEVI void epi_q(LAS char* lds, f32x4 (&acc)[4][6], char* ws, rsrc_t rws, Watch& wt, const P1& c, PF2& pf, int lane) {
  const int r = lane & 15, g = lane >> 4, qi = c.cslot - 13;
  f32x4 cs[5];
#pragma unroll
  for (int i = 0; i < 5; ++i) cs[i] = *(LAS f32x4*)(lds + L1_CS + ((c.nw * 6 + i) * 16 + 4 * g) * 4);
  pf.items<0, 8>();
  static_for<4>([&](auto MI) {
    constexpr int mi = MI;
    const LAS float* sp = (const LAS float*)(lds + L1_ST) + c.mw * 64 + mi * 16 + r;
    const f32x2 nm = bc2(-sp[0]), rs = bc2(sp[128]);
    static_for<5>([&](auto I) {
      constexpr int i = I;
      const f32x2 a = phi2(fma2(nm, lo2(cs[i]), lo2(acc[mi][i])), rs), b = phi2(fma2(nm, hi2(cs[i]), hi2(acc[mi][i])), rs);
      acc[mi][i] = f32x4{a[0], a[1], b[0], b[1]};
      PIN(acc[mi][i]);
      SB();
      pf.template item<8 + mi * 5 + i>();
    });
  });
  STAMP(3);
  LAS unsigned* lf = (LAS unsigned*)(lds + L2_FLAG);
  if (c.w == 4) { wait_flags(ws, OFF_FZ + (blockIdx.x & 7) * 512 + qi * 256 + (lane & 31) * 8, wt.tag); lds_inc(lf + LF_K, lane); }
  else lds_wait(lf + LF_K, 1u);
  STAMP(12);
  {
    const int q4 = (c.w & 1) * 64 + lane;
    f32x4 part[8];
#pragma unroll
    for (int p = 0; p < 8; ++p)
      part[p] = __builtin_bit_cast(f32x4, ld16(rws, q4 < 40 ? q4 * 16 : q4 < 80 ? q4 * 16 + 31 * 640 : OOB, OFF_ZP + (qi * 64 + (c.w >> 1) * 8 + p) * 640));
    SB();
    if (q4 < 80) *(LAS f32x4*)(lds + L2_RED + (c.w >> 1) * 1280 + q4 * 16) = ((part[0] + part[1]) + (part[2] + part[3])) + ((part[4] + part[5]) + (part[6] + part[7]));
    WAIT_L0(); BAR();
  }
  u64 fv = 0;
  STAMP(5);
  f32x4 z[5];
#pragma unroll
  for (int i = 0; i < 5; ++i) {
    const LAS char* p = lds + L2_RED + (c.nw * 80 + 20 * (2 * (g & 1) + (g >> 1)) + 4 * i) * 4;
    z[i] = (*(const LAS f32x4*)p + *(const LAS f32x4*)(p + 1280)) + (*(const LAS f32x4*)(p + 2560) + *(const LAS f32x4*)(p + 3840));
  }
  const unsigned qb = OFF_Q + ((c.Mt * 2 + c.mw) * 20 + (2 * qi + (c.nw >> 1)) * 5) * 4096;
  u32x2 keep;
#pragma unroll
  for (int mi = 0; mi < 4; ++mi) {
    f32x2 d0 = lo2(acc[mi][0]) * lo2(z[0]), d1 = hi2(acc[mi][0]) * hi2(z[0]);
#pragma unroll
    for (int i = 1; i < 5; ++i) { d0 = fma2(lo2(acc[mi][i]), lo2(z[i]), d0); d1 = fma2(hi2(acc[mi][i]), hi2(z[i]), d1); }
    d0 += d1;
    const f32x2 inv = bc2(__builtin_amdgcn_rcpf(d0[0] + d0[1] + 1e-6f));
    unsigned o[10];
#pragma unroll
    for (int i = 0; i < 5; ++i) { o[2 * i] = pk2(lo2(acc[mi][i]) * inv); o[2 * i + 1] = pk2(hi2(acc[mi][i]) * inv); }
    st16(rws, lane * 16 + mi * 1024, qb + (c.nw & 1) * 12288, u32x4{o[0], o[1], o[2], o[3]});
    st16(rws, lane * 16 + mi * 1024, qb + (c.nw & 1) * 12288 + 4096, u32x4{o[4], o[5], o[6], o[7]});
    if (mi & 1) st16(rws, (lane & 31) * 16 + (lane >> 5) * 1024 + (mi - 1) * 1024, qb + 8192 + (c.nw & 1) * 512, pair16(keep, u32x2{o[8], o[9]}));
    else keep = u32x2{o[8], o[9]};
    if (mi == 0 && c.w == 7) { SB(); fv = wt.fetch(); SB(); }
  }
  STAMP(4);
  SB();
  if (c.w == 7) wt.eval(fv, false);
  WAIT_L0(); BAR();
  wt.pcnt = c.mw; wt.plast = 3; wt.preps = 2; wt.pflag = OFF_FH + (c.Mt * 2 + c.mw) * 256 + (9 + qi) * 8;
}

DEVI void epi_kv(LAS char* lds, f32x4 (&acc)[4][6], char* ws, rsrc_t rws, u64 tag, const P1& c, int lane) {
  const int cl = lane & 15, g = lane >> 4, kj = c.cslot - 9;
  float cs[5];
#pragma unroll
  for (int i = 0; i < 5; ++i) cs[i] = -*(LAS float*)(lds + L1_CS + ((c.nw * 6 + i) * 16 + cl) * 4);
  f32x4 mean[4], rstd[4];
#pragma unroll
  for (int mi = 0; mi < 4; ++mi) {
    mean[mi] = *(LAS f32x4*)(lds + L1_ST + (c.mw * 64 + mi * 16 + 4 * g) * 4);
    rstd[mi] = *(LAS f32x4*)(lds + L1_ST + 512 + (c.mw * 64 + mi * 16 + 4 * g) * 4);
  }
  u32x4 fk[2][3], fv[2][3];
  auto center = [&](int mi, int i, int hh) { return fma2(hh ? hi2(mean[mi]) : lo2(mean[mi]), bc2(cs[i]), hh ? hi2(acc[mi][i]) : lo2(acc[mi][i])); };
#pragma unroll
  for (int mi = 0; mi < 4; ++mi)
#pragma unroll
    for (int f = 0; f < 3; ++f)
#pragma unroll
      for (int hh = 0; hh < 2; ++hh)
        fk[mi >> 1][f][(mi & 1) * 2 + hh] = pk2(phi2(center(mi, f < 2 ? f : 4, hh), hh ? hi2(rstd[mi]) : lo2(rstd[mi])));
  const int uh = cl < 4 ? 16 + cl : 32 + cl;
  const f32x4 zero = {0.f, 0.f, 0.f, 0.f};
  {
    const u32x4 one = {0x3f803f80u, 0x3f803f80u, 0x3f803f80u, 0x3f803f80u};
    f32x4 z[3];
#pragma unroll
    for (int f = 0; f < 3; ++f) z[f] = mfma(one, fk[1][f], mfma(one, fk[0][f], zero));
    const float v = g == 0 ? z[0][0] : g == 1 ? z[1][0] : z[2][0];
    __builtin_amdgcn_raw_buffer_store_b32(__builtin_bit_cast(unsigned, v), rws, g < 2 ? (g * 20 + cl) * 4 : (g == 2 && cl < 8) ? uh * 4 : OOB,
                                          OFF_ZP + (kj * 32 + c.Mt * 2 + c.mw) * 640 + c.nw * 160, SC1);
    SB();
  }
#pragma unroll
  for (int mi = 0; mi < 4; ++mi)
#pragma unroll
    for (int f = 0; f < 3; ++f)
#pragma unroll
      for (int hh = 0; hh < 2; ++hh)
        fv[mi >> 1][f][(mi & 1) * 2 + hh] = pk2(center(mi, 2 + f, hh) * (hh ? hi2(rstd[mi]) : lo2(rstd[mi])));
  constexpr int KI[7] = {0, 0, 2, 1, 1, 2, 2}, VI[7] = {0, 2, 0, 1, 2, 1, 2};
  f32x4 p[7];
#pragma unroll
  for (int q = 0; q < 7; ++q) p[q] = mfma(fv[1][VI[q]], fk[1][KI[q]], mfma(fv[0][VI[q]], fk[0][KI[q]], zero));
  LAS char* ex = lds + L1_EX + c.w * 3584;
  auto put = [&](bool ok, int u, int e, f32x4 v) { *(LAS f32x4*)(ex + (ok ? (u * 20 + e) * 4 : 3200)) = v; };
  put(true, cl, 4 * g, p[0]);
  put(true, 20 + cl, 4 * g, p[3]);
  put(g >= 2, (g & 1) * 20 + cl, 16, g == 2 ? p[1] : p[4]);
  put(cl < 8, uh, 4 * g, cl < 4 ? p[2] : p[5]);
  put(cl < 8 && g == 2 + (cl >> 2), uh, 16, p[6]);
  WAIT_V(0); WAIT_L0(); BAR();
  if (c.w == 7 && lane < 8) set_flag(ws, OFF_FZ + lane * 512 + kj * 128 + c.Mt * 8, tag);
#pragma unroll
  for (int q = 0; q < 2; ++q) {
    const int o = (c.mw * 128 + q * 64 + lane) * 16;
    const LAS char* src = lds + L1_EX + c.nw * 3584 + (o < 3200 ? o : 0);
    const f32x4 v = *(const LAS f32x4*)src + *(const LAS f32x4*)(src + 4 * 3584);
    st16(rws, o < 3200 ? o : OOB, OFF_KVP + ((kj * 4 + c.nw) * 16 + c.Mt) * 3456, __builtin_bit_cast(u32x4, v));
  }
}

DEVI void retile(const char* wd, rsrc_t rws, unsigned ldsw, int id, int w, int lane) {
  const u32x4 rd = srd(wd, kF * 1280);
  const int jj = lane >> 3, g = lane >> 4;
  const unsigned vo = ((jj >> 2) * 8 + (jj & 3)) * 1280 + (lane & 7) * 16;
  const unsigned ra = ldsw + g * 1024 + ((lane & 15) >> 2) * 128 + (lane & 3) * 8;
  auto fetch = [&](int n) {
    const int it = id * 8 + w + n * 128, rb = it / 10, ln = it - rb * 10;
#pragma unroll
    for (int q = 0; q < 4; ++q)
      dma<0>(rd, vo, it < 540 ? (rb * 32 + 16 * (q >> 1) + 4 * (q & 1)) * 1280 + ln * 128 : OOB, ldsw + (n & 3) * 4096 + q * 1024);
  };
  auto put = [&](int n) {
    const int it = id * 8 + w + n * 128, rb = it / 10, ln = it - rb * 10, cg = rb / 6, uu = rb - cg * 6;
    const int ks = 18 * (uu >> 1) + 2 * cg + (uu & 1);
#pragma unroll
    for (int q = 0; q < 4; ++q) {
      const int nt = ln * 4 + q, j = nt / 5, t = nt - j * 5;
      st16(rws, lane * 16, it < 540 ? OFF_WD + ((j * 54 + ks) * 5 + t) * 1024 : OOB, lds_trfrag(ra + (n & 3) * 4096 + q * 32));
    }
  };
#pragma unroll 1
  for (int n = 0; n < 4; ++n) fetch(n);
  WAIT_V(0);
  put(0);
  WAIT_L0();
  fetch(4);
#pragma unroll 1
  for (int n = 1; n < 5; ++n) {
    if (n == 4) WAIT_V(0);
    put(n);
  }
}

DEVI void fold_kv(LAS char* lds, char* ws, rsrc_t rws, Watch& wt, const u32x4 (&wf)[2][5], int hp, int w, int lane) {
  const u64 tag = wt.tag;
  const int cl = lane & 15, g = lane >> 4;
  const int o = ((w & 3) * 64 + lane) * 16;
  unsigned po = o < 3200 ? o : OOB, ko[6][2];
#pragma unroll
  for (int f = 0; f < 6; ++f) {
    const int u = 20 * (f & 1) + (f < 4 ? (cl & 4) * 2 + 4 * (f >> 1) : 16) + (cl & 3);
    const bool mine = f >= 4 || (cl >> 3) == (f & 1);
    ko[f][0] = (unsigned)(size_t)lds + L2_RED + (mine && g < 3 ? u * 80 + g * 32 : 3360);
    ko[f][1] = (unsigned)(size_t)lds + L2_RED + (mine && g < 2 ? u * 80 + g * 32 + 16 : 3360);
    PIN(ko[f][0]); PIN(ko[f][1]);
  }
  unsigned vo = OFF_MP + (w * 20 + (hp >> 2) * 5) * 5120 + (3 * ((hp >> 1) & 1) + (g & 1)) * 5120 + (((hp & 1) + (g & 2)) * 16 + cl) * 16;
  unsigned vo2 = OFF_MP + (w * 20 + (hp >> 2) * 5 + 2) * 5120 + g * 1024 + ((hp & 3) * 16 + cl) * 16;
  PIN(po); PIN(vo); PIN(vo2);
  WAIT_V(0);
  if (w == 0) wait_flags(ws, OFF_FK + (blockIdx.x & 7) * 512 + (hp >> 2) * 128 + cl * 8, tag);
  BAR();
  STAMP(2);
  {
    f32x4 part[8];
#pragma unroll
    for (int p = 0; p < 8; ++p) part[p] = __builtin_bit_cast(f32x4, ld16(rws, po, OFF_KVP + (hp * 16 + (w >> 2) * 8 + p) * 3456));
    SB();
    *(LAS f32x4*)(lds + L2_RED + (w >> 2) * 4096 + o) = ((part[0] + part[1]) + (part[2] + part[3])) + ((part[4] + part[5]) + (part[6] + part[7]));
    WAIT_L0(); BAR();
  }
  STAMP(12);
  u64 fv = 0;
  if (w == 7) fv = wt.fetch();
  SB();
  u32x4 kf[6];
#pragma unroll
  for (int f = 0; f < 6; ++f) {
    const f32x4 lo = *(LAS f32x4*)(size_t)ko[f][0] + *(LAS f32x4*)(size_t)(ko[f][0] + 4096);
    const f32x4 hi = *(LAS f32x4*)(size_t)ko[f][1] + *(LAS f32x4*)(size_t)(ko[f][1] + 4096);
    kf[f] = u32x4{pk2(lo2(lo)), pk2(hi2(lo)), pk2(lo2(hi)), pk2(hi2(hi))};
  }
  const f32x4 zero = {0.f, 0.f, 0.f, 0.f};
  u32x4 r1;
#pragma unroll
  for (int i = 0; i < 5; ++i) {
    const f32x4 d1 = mfma(kf[1], wf[1][i], mfma(kf[0], wf[0][i], zero)), d2 = mfma(kf[3], wf[1][i], mfma(kf[2], wf[0][i], zero));
    const f32x4 d3 = mfma(kf[4], wf[0][i], zero), d4 = mfma(kf[5], wf[1][i], zero);
    st16(rws, vo, i * 1024, u32x4{pk2(lo2(d1)), pk2(hi2(d1)), pk2(lo2(d2)), pk2(hi2(d2))});
    const u32x4 t = {pk2(lo2(d3)), pk2(hi2(d3)), pk2(lo2(d4)), pk2(hi2(d4))};
    if (i == 0 || i == 4) r1 = t; else r1 = g == i ? t : r1;
    if (i == 3) st16(rws, vo2, 0, r1);
    if (i == 4) st16(rws, g == 0 ? vo2 + 4096 : OOB, 0, r1);
  }
  STAMP(3);
  SB();
  if (w == 7) wt.eval(fv, false);
  WAIT_L0(); BAR();
  wt.pcnt = 0; wt.plast = 7; wt.preps = 8; wt.pflag = OFF_F3 + hp * 8;
}

template <bool ATT>
DEVI void p2_steps(LAS char* lds, rsrc_t rws, Watch& wt, const u32x4 (&br)[3][5], f32x4 (&acc)[4][5], const u32x2 (&xr)[5],
                   int mt2, int j, int w, int lane) {
  LAS unsigned* lf = wt.lf;
  const unsigned ab = OFF_H + (mt2 * 54 + w) * 4096;
  u32x4 fa[12][4], fb[12][5];
  auto ldA = [&](int n) {
    const unsigned so = n >= 7 ? OFF_Q + (mt2 * 20 + (w - 4) * 5 + n - 7) * 4096
                               : n == 6 && w >= 6 ? OOB : ab + (n < 3 ? 30 + 8 * n : 8 * n - 24) * 4096;
#pragma unroll
    for (int m = 0; m < 4; ++m) fa[n][m] = ld16(rws, lane * 16, so + ((m + w) & 3) * 1024);
    SB();
  };
  unsigned lb = (unsigned)(size_t)lds + w * 5120 + lane * 16;
  auto rdB = [&](int n) {
    if (n == 5) { lb += 81920; PIN(lb); }
    if (ATT && n == 6) { lb -= w >= 6 ? 40960 : 0; PIN(lb); }
#pragma unroll
    for (int t = 0; t < 5; ++t) fb[n][t] = lds_b128(lb + ((n - 3) & 1) * 40960 + t * 1024);
    SB();
  };
  auto ld1 = [&](int kind, int n, int i) {
    if (kind == 0) {
      const unsigned so = n >= 7 ? OFF_Q + (mt2 * 20 + (w - 4) * 5 + n - 7) * 4096
                                 : n == 6 && w >= 6 ? OOB : ab + (n < 3 ? 30 + 8 * n : 8 * n - 24) * 4096;
      fa[n][i] = ld16(rws, lane * 16, so + ((i + w) & 3) * 1024);
    } else fb[n][i] = ld16(rws, lane * 16, OFF_MP + (j * 20 + (w - 4) * 5 + n - 7) * 5120 + i * 1024);
    SB();
  };
  LAS char* ex = lds + w * 5120 + lane * 16;
  constexpr int PRE = ATT ? 6 : 3;
#pragma unroll
  for (int n = 0; n < PRE; ++n) {
    ldA(n);
    if (n == 1 && wt.preps != 0) { WAIT_V(8); wt.publish(); STAMP(13); }
  }
  if (ATT) {
#pragma unroll
    for (int t = 0; t < 5; ++t) {
      const f32x2 x0 = unpk(xr[t][0]), x1 = unpk(xr[t][1]);
      acc[0][t] = f32x4{x0[0], x0[1], x1[0], x1[1]};
      PIN(acc[0][t]);
    }
    SB();
    asm volatile("s_nop 1" ::: "memory");
  }
  constexpr int A1[12] = {ATT ? -1 : 3, ATT ? 6 : 5, ATT ? 7 : -1, -1, ATT ? 8 : -1, -1, ATT ? 9 : -1, 10, 11, -1, -1, -1};
  constexpr int A2[12] = {ATT ? -1 : 4, ATT ? -1 : 6, -1, -1, -1, -1, -1, -1, -1, -1, -1, -1};
  constexpr int M1[12] = {-1, -1, -1, ATT ? 7 : -1, -1, ATT ? 8 : -1, ATT ? 9 : -1, 10, 11, -1, -1, -1};
  u64 fv = 0;
  static_for<(ATT ? 12 : 7)>([&](auto N) {
    constexpr int n = N;
    if (n == 2) lds_wait(lf + LF_DMA, 8u);
    if (ATT && n == 2) lds_wait_bits(lf + LF_STATUS, S_Q);
    if (ATT && n == 3) { lds_wait_bits(lf + LF_STATUS, S_M); STAMP(7); }
    if (!ATT && (n == 3 || n == 5) && w == 3) { if (n == 5) wt.eval(fv, false); fv = wt.fetch(); SB(); }
    if (n >= 2 && n < 6) rdB(n + 1);
    if (n == (ATT ? 11 : 6)) lds_wait(lf + LF_BC + ATT, 4u);
#pragma unroll
    for (int q = 0; q < 20; ++q) {
      constexpr int LASTN = ATT ? 11 : 6;
      const int m = n == LASTN ? (q / 5 + 1) & 3 : n < 7 ? q / 5 : q & 3, t = n < 7 || n == LASTN ? q % 5 : q >> 2;
      if (n == 0 && (m > 0 || !ATT)) mfma_new(acc[m][t], br[0][t], fa[0][m]);
      else mfma_acc(acc[m][t], n < 3 ? br[n < 3 ? n : 0][t] : fb[n][t], fa[n][m]);
      if (n == LASTN && q >= 10) {
        SB();
        *(LAS f32x4*)(ex + ((q - 10) / 5) * 40960 + (q % 5) * 1024) = acc[(q - 10) / 5 + 1][q % 5];
        SB();
      }
      if (q & 1) {
        const int p = q >> 1, na = (A1[n] >= 0 ? 4 : 0) + (A2[n] >= 0 ? 4 : 0);
        if (p < na) ld1(0, p < 4 ? A1[n] : A2[n], p & 3);
        else if (M1[n] >= 0 && p - na < 5) ld1(1, M1[n], p - na);
      }
    }
    SB();
    if (n == 0) STAMP(9);
    if (n == 6) STAMP(11);
    if (n == 5) { WAIT_L0(); lds_inc(lf + LF_BC + ATT, lane); }
  });
  if (!ATT && w == 3) wt.eval(fv, false);
}

DEVI void phase2(LAS char* lds, const char* x, char* ws, rsrc_t rws, char* out, Watch& wt, u32x4 (&br)[3][5],
                 int mt2, int j, int w, int lane) {
  const int a = w & 3;
  LAS unsigned* lf = wt.lf;
  const unsigned obase = (mt2 * 64 + a * 16) * 1280 + j * 160;
  f32x4 acc[4][5];
  u32x2 xr[5];
  if (w >= 4) {
    const rsrc_t rx = __builtin_amdgcn_make_buffer_rsrc((void*)x, 0, 2048 * 1280, 0x00020000);
#pragma unroll
    for (int t = 0; t < 5; ++t) xr[t] = __builtin_amdgcn_raw_buffer_load_b64(rx, (lane & 15) * 1280 + (lane >> 4) * 8 + t * 32, obase, 0);
    SB();
  }
  if (wt.preps != 0) { if (!wt.has(S_H)) { if (w == 7) wt.poll(false); WAIT_V(0); wt.publish(); STAMP(13); } }
  else WAIT_V(15);
  lds_inc(lf + LF_DMA, lane);
  wt.need(S_H);
  STAMP(6);
  if (w < 4) {
    __builtin_amdgcn_s_setprio(2);
    p2_steps<false>(lds, rws, wt, br, acc, xr, mt2, j, w, lane);
    __builtin_amdgcn_s_setprio(0);
#pragma unroll
    for (int q = 0; q < 15; ++q) asm volatile("" : "=v"(br[q / 5][q % 5]));
  }
  PINS(w);
  if (w >= 4) { __builtin_amdgcn_s_setprio(1); p2_steps<true>(lds, rws, wt, br, acc, xr, mt2, j, w, lane); }
  asm volatile("s_nop 7" ::: "memory");
  SB();
  STAMP(14);
  LAS char* ex = lds + (w >> 2) * 20480 + lane * 16;
#pragma unroll
  for (int t = 0; t < 5; ++t) *(LAS f32x4*)(ex + 2 * 40960 + (a * 5 + t) * 1024) = acc[3][t];
  WAIT_L0();
  if (w < 4) { lds_inc(lf + LF_X, lane); if (w == 3) wt.need(S_Q | S_M, 3); lds_wait(lf + LF_X, 4u); }
  else BAR();
  STAMP(8);
#pragma unroll
  for (int t = 0; t < 5; ++t)
#pragma unroll
    for (int q = 1; q < 4; ++q) acc[0][t] += *(LAS f32x4*)(ex + (q - 1) * 40960 + (((a - q) & 3) * 5 + t) * 1024);
  LAS char* base = lds + L2_BASE + a * 5120 + lane * 16;
  if (w < 4) {
#pragma unroll
    for (int t = 0; t < 5; ++t) *(LAS f32x4*)(base + t * 1024) = acc[0][t];
    WAIT_L0();
  } else {
    const rsrc_t ro = __builtin_amdgcn_make_buffer_rsrc(out, 0, 2048 * 1280, 0x00020000);
    u32x2 o[5];
#pragma unroll
    for (int t = 0; t < 5; ++t) {
      const f32x4 s = acc[0][t] + *(LAS f32x4*)(base + t * 1024);
      o[t] = u32x2{pk2(lo2(s)), pk2(hi2(s))};
    }
    const unsigned orow = obase + (lane & 15) * 1280;
#pragma unroll
    for (int t = 0; t < 4; t += 2) {
      const auto s0 = __builtin_amdgcn_permlane16_swap(o[t][0], o[t + 1][0], false, false);
      const auto s1 = __builtin_amdgcn_permlane16_swap(o[t][1], o[t + 1][1], false, false);
      __builtin_amdgcn_raw_buffer_store_b128(u32x4{s0[0], s1[0], s0[1], s1[1]}, ro, orow + ((lane >> 4) & 1) * 32 + (lane >> 5) * 16 + t * 32, 0, OUT_AUX);
    }
    __builtin_amdgcn_raw_buffer_store_b64(o[4], ro, orow + (lane >> 4) * 8 + 128, 0, OUT_AUX);
  }
}

__global__ __launch_bounds__(512, 2) void fused(const char* x, const char* wqkv, const char* wo, const char* wg,
                                                const char* wu, const char* wd, char* ws, char* out) {
  extern __shared__ __attribute__((aligned(16))) char smem[];
  const char* pc0;
  asm volatile("s_getpc_b64 %0" : "=s"(pc0));
  asm volatile("" : "+s"(x), "+s"(wqkv), "+s"(wo), "+s"(wg), "+s"(wu), "+s"(wd), "+s"(ws), "+s"(out));
  LAS char* lds = (LAS char*)smem;
  const unsigned ldsb = (unsigned)(size_t)lds;
  const int bid = blockIdx.x, xcd = bid & 7, idx = bid >> 3;
  const int Mt = (xcd & 1) * 8 + (idx & 7), grp = xcd >> 1, pos4 = idx >> 3, slot2 = grp * 4 + pos4;
  const int cslot = (int)(0xCF76BE54AD329810ull >> (4 * slot2)) & 15;
  const int w = __builtin_amdgcn_readfirstlane(threadIdx.x >> 6), lane = threadIdx.x & 63;
  STAMP(0);
  const rsrc_t rws = __builtin_amdgcn_make_buffer_rsrc(ws, 0, WS_END, 0x00020000);
  u64 tag;
  Watch wt{(LAS unsigned*)(lds + L2_FLAG), ws, 0ull, Mt * 2 + (slot2 >> 3), slot2 & 7, w, lane, 0u, 0, 0, 0, 0u};
  u32x4 br[3][5];
  PF2 pf{ws, rws, ldsb, slot2 & 7, w, lane, br};
  if (cslot == 15) {
    tag = make_tag(ws);
    PINS(tag);
    wt.tag = tag;
    if (threadIdx.x < 16) ((LAS unsigned*)(lds + L2_FLAG))[threadIdx.x] = 0u;
    if (WARM) warm_up(x, wqkv, wo, wg, wu, wd, ws, pc0, ldsb + L1_GR + 8192, bid, w, lane);
    retile(wd, rws, ldsb + w * 16384, Mt, w, lane);
    WAIT_V(0); WAIT_L0(); BAR();
    if (threadIdx.x == 0) { set_flag(ws, OFF_FR + Mt * 8, tag); set_flag(ws, OFF_F1 + (Mt * 16 + 15) * 8, tag); }
    STAMP(5);
    u32x4 wf[2][5];
    {
      const u32x4 ro = srd(wo, kD * 1280);
#pragma unroll 1
      for (int q = 0; q < 10; ++q)
        dma<0>(ro, (lane >> 3) * 1280 + (lane & 7) * 16, (Mt * 40 + (q >> 1) * 8) * 1280 + ((w * 160) & ~127) + (q & 1) * 128, ldsb + w * 16384 + q * 1024);
      WAIT_V(0);
#pragma unroll
      for (int hh = 0; hh < 2; ++hh) {
        unsigned ra[2];
#pragma unroll
        for (int h2 = 0; h2 < 2; ++h2) {
          int row = hh * 20 + (lane >> 4) * 8 + h2 * 4 + ((lane & 15) >> 2);
          row = row < 40 ? row : 39;
          ra[h2] = ldsb + w * 16384 + (row >> 3) * 2048 + (row & 7) * 128 + (lane & 3) * 8;
        }
#pragma unroll
        for (int i = 0; i < 5; ++i) {
          const int cb = (w & 3) * 32 + i * 32, o = (cb >> 7) * 1024 + (cb & 127);
          const u32x2 lo = __builtin_bit_cast(u32x2, __builtin_amdgcn_ds_read_tr16_b64_v4i16((LAS s16x4*)(size_t)(ra[0] + o)));
          const u32x2 hi = __builtin_bit_cast(u32x2, __builtin_amdgcn_ds_read_tr16_b64_v4i16((LAS s16x4*)(size_t)(ra[1] + o)));
          wf[hh][i] = u32x4{lo[0], lo[1], hi[0], hi[1]};
        }
      }
    }
    wait_flags(ws, OFF_FR + (lane & 15) * 8, tag);
    WAIT_L0(); BAR();
    wd_prefetch<0, 6>(srd(ws, WS_END), ldsb, slot2 & 7, w, lane);
    pf.items<0, 28>();
    if (Mt == 0 && w == 7) {
#pragma unroll 1
      for (int q = 0; q < 4; ++q) wait_flags(ws, OFF_F1 + (q * 64 + lane) * 8, tag);
      if (lane == 0) set_flag(ws, OFF_E, tag);
    }
    fold_kv(lds, ws, rws, wt, wf, Mt, w, lane);
  } else {
    const int type = cslot < 9 ? 0 : cslot < 13 ? 1 : 2;
    P1 c;
    c.w = w; c.mw = w >> 2; c.nw = w & 3; c.Mt = Mt; c.cslot = cslot; c.j = slot2 & 7;
    c.ldsb = ldsb;
    {
#define ROTM 1
      const int kb = (Mt + cslot) % 10, rot = ROTM == 0 ? (kb >= 5 ? 5 : 0) : ROTM == 1 ? (kb > 7 ? 7 : kb) : (kb / 3) * 3 - (kb == 9 ? 3 : 0);
      c.bstar = kb - rot;
      const int seg = w >> 2;
      c.pitch = type == 0 ? kF * 2 : kQKV * 2;
      const int startb = type == 0 ? cslot * 384
                       : ((type == 1 ? (640 * (1 + seg) + 160 * (cslot - 9)) * 2 : 320 * (2 * (cslot - 13) + seg)) & ~127);
      const char* wb = type == 0 ? (seg ? wu : wg) : wqkv;
      c.rw = srd(wb + startb - 2048, 640 * c.pitch - startb + 2048);
      c.rx = srd(x + (size_t)Mt * 128 * 1280, 128 * 1280);
      const int i = lane >> 3, cp = lane & 7, pg = w & 3;
      c.voffB = (unsigned)(8 * pg + i) * c.pitch + 16 * (cp ^ ((i & 2) | ((pg & 1) << 2))) + 2048;
      c.voffA = (unsigned)(8 * w + i) * 1280 + 16 * (cp ^ ((4 * w + (i >> 1)) & 7));
      c.oB = rot * 64 * c.pitch; c.oA = rot * 128;
      dma<0>(c.rx, c.voffA, c.oA, c.ldsb + L1_A + w * 1024);
    }
    auto pro = [&](int s) {
      const unsigned lB = c.ldsb + L1_B + w * 3072, lA = c.ldsb + L1_A + w * 1024;
      dma<0>(c.rw, c.voffB, c.oB, lB + s * L1_BST);
      dma<128>(c.rw, c.voffB, c.oB, lB + s * L1_BST + 1024);
      dma<256>(c.rw, c.voffB, c.oB, lB + s * L1_BST + 2048);
      c.oB += 32 * c.pitch;
      dma<0>(c.rx, c.voffA, c.oA + ((s + 1) >> 1) * 128 + ((s + 1) & 1) * 81920, lA + (s + 1) * 8192);
      SB();
    };
#define PSPLIT 1
#pragma unroll
    for (int s = 0; s < PSPLIT; ++s) pro(s);
    tag = make_tag(ws);
    PINS(tag);
    wt.tag = tag;
    unsigned vb[5], va0;
    {
      const int g = lane >> 4, q = (lane & 15) >> 2, p = lane & 3, r = lane & 15, nw = c.nw;
      const int swz = (q & 2) | ((g & 1) << 2);
#pragma unroll
      for (int pos = 0; pos < 5; ++pos) {
        const int cf = 16 * nw + 4 * p;
        const int hb = (cslot & 1 ? 0 : 32) + 40 * nw;
        const int ck = pos < 4 ? (pos >> 1) * 192 + hb + 20 * (pos & 1) + 4 * p : (p >> 1) * 192 + hb + 20 * (p & 1) + 16;
        const int cq = (nw >> 1) * 224 + 20 * (4 * (nw & 1) + 2 * (p & 1) + (p >> 1)) + 4 * pos;
        const int col = type == 0 ? cf : type == 1 ? ck : cq;
        const int line = col >> 6, seg = line >= 3;
        vb[pos] = c.ldsb + L1_B + seg * (12288 - 3072) + (g * 3 + line) * 1024 + q * 128 + 16 * (((col & 63) >> 3) ^ swz) + 8 * ((col >> 2) & 1);
      }
      va0 = c.ldsb + L1_A + (c.mw * 64 + r) * 128 + 16 * (g ^ ((r >> 1) & 7));
    }
    if (threadIdx.x < 16) ((LAS unsigned*)(lds + L2_FLAG))[threadIdx.x] = 0u;
    f32x4 acc[4][6];
#pragma unroll
    for (int m = 0; m < 4; ++m)
#pragma unroll
      for (int i = 0; i < 6; ++i) acc[m][i] = f32x4{0.f, 0.f, 0.f, 0.f};
    unsigned foff = lane >= 20 ? OFF_FR + (lane & 15) * 8 : OFF_F1 + (lane < 10 ? lane * 16 + cslot : Mt * 16 + lane - 10) * 8;
    PIN(foff);
#pragma unroll
    for (int i = 0; i < 5; ++i) PIN(vb[i]);
    SB();
#pragma unroll
    for (int s = PSPLIT; s < 3; ++s) pro(s);
    c.oA += 256;
    if (WARM) warm_up(x, wqkv, wo, wg, wu, wd, ws, pc0, ldsb + L1_GR + 8192, bid, w, lane);
    SB();
#define P1_STAMP
#define P1_RUN(T) do { if (w < 4) p1_loop<T, true>(c, vb, va0, foff, acc, pf, ws, rws, tag, lane); \
                       else p1_loop<T, false>(c, vb, va0, foff, acc, pf, ws, rws, tag, lane); P1_STAMP; } while (0)
    if (type == 0) { P1_RUN(0); epi_ffn(lds, acc, ws, rws, wt, c, pf, lane); }
    else if (type == 2) { P1_RUN(2); epi_q(lds, acc, ws, rws, wt, c, pf, lane); }
    else {
      P1_RUN(1);
      epi_kv(lds, acc, ws, rws, tag, c, lane);
      STAMP(4);
      WAIT_V(0); WAIT_L0(); BAR();
      if (threadIdx.x < 8) set_flag(ws, OFF_FK + threadIdx.x * 512 + (cslot - 9) * 128 + Mt * 8, tag);
      STAMP(5);
      pf.items<0, 28>();
    }
  }
  phase2(lds, x, ws, rws, out, wt, br, Mt * 2 + (slot2 >> 3), slot2 & 7, w, lane);
  STAMP(10);
}

extern "C" void kernel_launch(void* const* d_in, const int* in_sizes, int n_in, void* d_out, int out_size,
                              void* d_ws, size_t ws_size, hipStream_t stream) {
  (void)in_sizes; (void)n_in; (void)out_size; (void)ws_size;
  (void)hipFuncSetAttribute((const void*)fused, hipFuncAttributeMaxDynamicSharedMemorySize, LDS_SIZE);
  fused<<<256, 512, LDS_SIZE, stream>>>((const char*)d_in[0], (const char*)d_in[1], (const char*)d_in[2], (const char*)d_in[4],
                                        (const char*)d_in[5], (const char*)d_in[6], (char*)d_ws, (char*)d_out);
}
```

```cpp
#include <hip/hip_runtime.h>
#include <stdint.h>
#include <type_traits>

#define GAS __attribute__((address_space(1)))
#define LAS __attribute__((address_space(3)))
typedef __attribute__((ext_vector_type(2))) __bf16 bf16x2;
typedef __attribute__((ext_vector_type(8))) __bf16 bf16x8;
typedef __attribute__((ext_vector_type(4))) short s16x4;
typedef __attribute__((ext_vector_type(4))) float f32x4;
typedef __attribute__((ext_vector_type(2))) float f32x2;
typedef __attribute__((ext_vector_type(4))) unsigned u32x4;
typedef __attribute__((ext_vector_type(2))) unsigned u32x2;
typedef unsigned long long u64;
typedef __amdgpu_buffer_rsrc_t rsrc_t;

#define DEVI __device__ __forceinline__
#define WAIT_V_(n) asm volatile("s_waitcnt vmcnt(" #n ")" ::: "memory")
#define WAIT_V(n) WAIT_V_(n)
template <int N> __device__ __forceinline__ void wait_v() { asm volatile("s_waitcnt vmcnt(%0)" :: "i"(N) : "memory"); }
#define WAIT_L0() do { __builtin_amdgcn_s_waitcnt(0xC07F); asm volatile("" ::: "memory"); } while (0)
#define SB() __builtin_amdgcn_sched_barrier(0)
#define PIN(x) asm volatile("" : "+v"(x))
#define PINS(x) asm volatile("" : "+s"(x))
#define BAR() do { asm volatile("" ::: "memory"); __builtin_amdgcn_s_barrier(); asm volatile("" ::: "memory"); } while (0)
#define RLX_AGENT __ATOMIC_RELAXED, __HIP_MEMORY_SCOPE_AGENT
#define RLX_WG __ATOMIC_RELAXED, __HIP_MEMORY_SCOPE_WORKGROUP
#define SC1 16
#define OUT_AUX 0

#define STAMP(i)

constexpr int kD = 640, kF = 1728, kQKV = 1920;
constexpr unsigned OFF_E   = 0;
constexpr unsigned OFF_F1  = 256;
constexpr unsigned OFF_FR  = OFF_F1 + 2048;
constexpr unsigned OFF_FK  = 2560;
constexpr unsigned OFF_F3  = OFF_FK + 4096;
constexpr unsigned OFF_FH  = OFF_F3 + 1024;
constexpr unsigned OFF_FZ  = 16384;
constexpr unsigned OFF_ZP  = OFF_FZ + 4096;
constexpr unsigned OFF_SC  = OFF_ZP + 4 * 32 * 640;
constexpr unsigned OFF_SR  = OFF_SC + 15 * 10 * 384 * 4;
constexpr unsigned OFF_KVP = OFF_SR + 16 * 10 * 256 * 4;
constexpr unsigned OFF_MP  = OFF_KVP + 16 * 16 * 3456;
constexpr unsigned OFF_WD  = OFF_MP + 8 * 20 * 5 * 1024;
constexpr unsigned OFF_Q   = OFF_WD + 8 * 54 * 5 * 1024;
constexpr unsigned OFF_H   = OFF_Q + 32 * 20 * 4096;
constexpr unsigned WS_END  = OFF_H + 32 * 54 * 4096;
constexpr unsigned OOB = 0x80000000u;
static_assert(OFF_FH + 8192 <= OFF_FZ && OFF_MP % 1024 == 0 && OFF_KVP % 128 == 0, "align");

constexpr int L1_B = 0, L1_BST = 24576;
constexpr int L1_A = 4 * L1_BST, L1_AST = 16384;
constexpr int L1_GR = L1_A + 3 * L1_AST;
constexpr int L1_GC = L1_A + L1_AST;
constexpr int L1_ST = 158720;
constexpr int L1_CS = L1_ST + 1024;
constexpr int L1_EX = 72 * 1024;
constexpr int L2_BASE = 120 * 1024;
constexpr int L2_RED = 150 * 1024;
constexpr int L2_FLAG = 162816;
constexpr int LDS_SIZE = L2_FLAG + 64;
enum { LF_STATUS = 0, LF_DMA = 1, LF_K = 2, LF_X = 3, LF_BC = 4, LF_PUB = 8 };
enum { S_H = 1, S_Q = 2, S_M = 4 };

DEVI f32x4 mfma(u32x4 a, u32x4 b, f32x4 c) {
  return __builtin_amdgcn_mfma_f32_16x16x32_bf16(__builtin_bit_cast(bf16x8, a), __builtin_bit_cast(bf16x8, b), c, 0, 0, 0);
}
DEVI void mfma_acc(f32x4& c, u32x4 a, u32x4 b) { asm volatile("v_mfma_f32_16x16x32_bf16 %0, %1, %2, %0" : "+v"(c) : "v"(a), "v"(b)); }
DEVI void mfma_new(f32x4& c, u32x4 a, u32x4 b) { asm volatile("v_mfma_f32_16x16x32_bf16 %0, %1, %2, 0" : "=&v"(c) : "v"(a), "v"(b)); }
DEVI unsigned pk2(f32x2 f) { return __builtin_bit_cast(unsigned, __builtin_convertvector(f, bf16x2)); }
DEVI unsigned pk2(float lo, float hi) { return pk2(f32x2{lo, hi}); }
DEVI f32x2 unpk(unsigned u) { return f32x2{__builtin_bit_cast(float, u << 16), __builtin_bit_cast(float, u & 0xffff0000u)}; }
DEVI f32x2 fma2(f32x2 a, f32x2 b, f32x2 c) { return __builtin_elementwise_fma(a, b, c); }
DEVI f32x2 bc2(float v) { return f32x2{v, v}; }
DEVI f32x2 lo2(f32x4 v) { return f32x2{v[0], v[1]}; }
DEVI f32x2 hi2(f32x4 v) { return f32x2{v[2], v[3]}; }
DEVI f32x2 phi2(f32x2 g, f32x2 rs) {
  const f32x2 t = fma2(g, rs, bc2(1e-6f)), l = g * (rs * 1.4426950408889634f);
  return f32x2{fmaxf(t[0], 1e-6f), fmaxf(t[1], 1e-6f)} +
         f32x2{__builtin_amdgcn_fmed3f(__builtin_amdgcn_exp2f(l[0]), 0.f, 1.f), __builtin_amdgcn_fmed3f(__builtin_amdgcn_exp2f(l[1]), 0.f, 1.f)};
}
DEVI u32x4 lds_b128(unsigned a) { return *(LAS u32x4*)(size_t)a; }
DEVI u32x4 lds_trfrag(unsigned a) {
  const u32x2 lo = __builtin_bit_cast(u32x2, __builtin_amdgcn_ds_read_tr16_b64_v4i16((LAS s16x4*)(size_t)a));
  const u32x2 hi = __builtin_bit_cast(u32x2, __builtin_amdgcn_ds_read_tr16_b64_v4i16((LAS s16x4*)(size_t)(a + 512)));
  return u32x4{lo[0], lo[1], hi[0], hi[1]};
}
DEVI u32x4 srd(const void* p, unsigned bytes) {
  const u64 a = (u64)p;
  return u32x4{(unsigned)a, (unsigned)(a >> 32) & 0xffffu, bytes, 0x00020000u};
}
DEVI void setm0(unsigned v) { asm volatile("s_mov_b32 m0, %0" :: "s"(v) : "memory"); }
template <int IMM>
DEVI void dma_n(u32x4 r, unsigned voff, unsigned soff) {
  asm volatile("buffer_load_dwordx4 %0, %1, %2 offen offset:%3 lds" :: "v"(voff), "s"(r), "s"(soff), "i"(IMM) : "memory");
}
template <int IMM>
DEVI void dma(u32x4 r, unsigned voff, unsigned soff, unsigned lds) {
  asm volatile("s_mov_b32 m0, %4\n\ts_nop 0\n\tbuffer_load_dwordx4 %0, %1, %2 offen offset:%3 lds"
               :: "v"(voff), "s"(r), "s"(soff), "i"(IMM), "s"(lds - IMM) : "memory");
}
DEVI void dma_sc1(u32x4 r, unsigned voff, unsigned soff, unsigned lds) {
  asm volatile("s_mov_b32 m0, %3\n\ts_nop 0\n\tbuffer_load_dwordx4 %0, %1, %2 offen sc1 lds"
               :: "v"(voff), "s"(r), "s"(soff), "s"(lds) : "memory");
}
DEVI void ld8_hidden(u32x2& v, u32x4 r, unsigned voff) {
  asm volatile("buffer_load_dwordx2 %0, %1, %2, 0 offen sc1" : "+v"(v) : "v"(voff), "s"(r) : "memory");
}
template <int N0, int N1>
DEVI void wd_prefetch(u32x4 rws, unsigned ldsb, int j, int w, int lane) {
#pragma unroll
  for (int n = N0; n < N1; ++n) {
    const unsigned a = (unsigned)(w + 8 * n) * 1024;
    if (n < 18 || w < 6) dma_sc1(rws, lane * 16, OFF_WD + j * 276480 + a, ldsb + a);
  }
}
DEVI void wait_flags(char* ws, unsigned off, u64 tag) {
#pragma unroll 1
  for (unsigned spins = 0; spins < (1u << 20); ++spins) {
    const u64 v = __hip_atomic_load((GAS u64*)(ws + off), RLX_AGENT);
    if (__all(v == tag)) break;
    __builtin_amdgcn_s_sleep(1);
  }
  asm volatile("" ::: "memory");
}
DEVI u64 make_tag(u32x2 e) {
  const u64 v = (u64)(unsigned)__builtin_amdgcn_readfirstlane(e[0]) | (u64)(unsigned)__builtin_amdgcn_readfirstlane(e[1]) << 32;
  return v * 0x9E3779B97F4A7C15ull + 0xD1B54A32D192ED03ull;
}
DEVI void set_flag(char* ws, unsigned off, u64 tag) { __hip_atomic_store((GAS u64*)(ws + off), tag, RLX_AGENT); }
DEVI unsigned lds_inc(LAS unsigned* p, int lane) {
  unsigned o = 0;
  if (lane == 0) o = __hip_atomic_fetch_add(p, 1u, RLX_WG);
  return __builtin_amdgcn_readfirstlane(o);
}
DEVI void lds_wait_bits(LAS unsigned* p, unsigned bits) {
  while ((~(unsigned)__builtin_amdgcn_readfirstlane(__hip_atomic_load(p, RLX_WG)) & bits) != 0u) __builtin_amdgcn_s_sleep(1);
  asm volatile("" ::: "memory");
}
DEVI void lds_wait(LAS unsigned* p, unsigned want) {
  while ((unsigned)__builtin_amdgcn_readfirstlane(__hip_atomic_load(p, RLX_WG)) < want) __builtin_amdgcn_s_sleep(1);
  asm volatile("" ::: "memory");
}
DEVI u32x4 ld16(rsrc_t r, unsigned voff, unsigned soff) { return __builtin_amdgcn_raw_buffer_load_b128(r, voff, soff, SC1); }
DEVI void st16(rsrc_t r, unsigned voff, unsigned soff, u32x4 v) { __builtin_amdgcn_raw_buffer_store_b128(v, r, voff + soff, 0, SC1); }
DEVI void st8(rsrc_t r, unsigned voff, unsigned soff, u32x2 v) { __builtin_amdgcn_raw_buffer_store_b64(v, r, voff, soff, SC1); }

constexpr unsigned CODE_BYTES = 60 * 1024;
DEVI void warm_code(const char* pc0, unsigned junk, int chunk, int lane) {
  asm volatile("s_mov_b32 m0, %3\n\ts_nop 0\n\tbuffer_load_dword %0, %1, %2 offen lds"
               :: "v"(lane << 7), "s"(srd(pc0, CODE_BYTES)), "s"(chunk * 8192), "s"(junk) : "memory");
}

struct P1 {
  u32x4 rw, rx;
  unsigned ldsb, pitch;
  unsigned voffB, voffA;
  unsigned oB, oA;
  int w, mw, nw, bstar;
  int Mt, cslot, j;
};

struct PF2 {
  char* ws; rsrc_t rws; unsigned ldsb; int j, w, lane, r0;
  u32x4 (&br)[3][5];
  template <int N> DEVI void item() {
    if constexpr (N < 13) wd_prefetch<6 + N, 7 + N>(srd(ws, WS_END), ldsb, j, w, lane);
    else br[(N - 13) / 5][(N - 13) % 5] = ld16(rws, lane * 16, OFF_WD + (j * 54 + 30 + w + 8 * (((N - 13) / 5 + r0) % 3)) * 5120 + ((N - 13) % 5) * 1024);
    SB();
  }
  template <int N0, int N1> DEVI void items() { if constexpr (N0 < N1) { item<N0>(); items<N0 + 1, N1>(); } }
};

#define SPR 1
#define EB 3
#define AINC 1
template <int TYPE, bool LATE>
DEVI void p1_loop(P1& c, unsigned (&vb)[5], unsigned va0, unsigned foff, f32x4 (&acc)[4][6], PF2& pf, char* ws, rsrc_t rws, u64 tag, int lane) {
  constexpr int NT = TYPE == 0 ? 6 : 5;
  constexpr bool AC = !LATE || AINC;
  u32x4 fa[4], fb[NT];
  unsigned va1 = va0 ^ 64;
  const unsigned stepB = 32 * c.pitch, limB = 640 * c.pitch;
  const unsigned lB0 = c.ldsb + L1_B + c.w * 3072, lA0 = c.ldsb + L1_A + c.w * 1024;
  unsigned lB = lB0 + 3 * L1_BST, lA = lA0 + 2 * L1_AST;
  unsigned vB1 = c.voffB - 896, vB2 = c.voffB - 1792, vA1 = c.voffA + 81920;
  PIN(vB1); PIN(vB2); PIN(vA1);
  int dA = L1_AST, dB = 2 * L1_BST, rare = c.bstar, rare2 = 1;
  f32x4 st[5];
  auto adB = [&](int i) { return TYPE == 0 ? vb[!LATE && i >= 2 ? 1 : 0] + (i / 3) * 12288 + (i % 3) * 1024 : vb[i < 5 ? i : 4]; };
  if (TYPE == 0) { vb[1] = vb[0]; PIN(vb[1]); }
  auto upB = [&](int i0, int i1) {
    if (TYPE == 0) { vb[i0 ? 1 : 0] += dB; PIN(vb[i0 ? 1 : 0]); }
    else {
#pragma unroll
      for (int i = i0; i < i1; ++i) { vb[i] += dB; PIN(vb[i]); }
    }
  };
  auto rdA = [&](int i, int sub) { fa[i] = lds_b128((sub ? va1 : va0) + i * 2048); };
  auto rdB = [&](int i, int sub) { fb[i] = lds_trfrag(adB(i) + sub * L1_BST); };
  auto rdBh = [&](int i, int hh, int sub) {
    const u32x2 v = __builtin_bit_cast(u32x2, __builtin_amdgcn_ds_read_tr16_b64_v4i16((LAS s16x4*)(size_t)(adB(i) + sub * L1_BST + hh * 512)));
    fb[i][2 * hh] = v[0]; fb[i][2 * hh + 1] = v[1];
  };
  auto pieceA = [&](int sub) { dma_n<0>(c.rx, sub ? vA1 : c.voffA, c.oA); };
  auto L = [&](int sub, int mode) {
    if (mode) dma_n<0>(c.rw, c.voffB, c.oB);
    SB(); rdA(0, sub); rdA(1, sub); SB();
    if (mode) dma_n<1024>(c.rw, vB1, c.oB);
    SB(); rdA(2, sub); rdA(3, sub); SB();
    if (mode) dma_n<2048>(c.rw, vB2, c.oB);
    SB(); rdB(LATE ? 4 : 0, sub); SB();
    if (mode == 2) setm0(lA + sub * 8192);
    SB(); if (!LATE) rdB(1, sub); else if (NT == 6) rdB(NT - 1, sub); else if (mode == 2) asm volatile("s_nop 0"); SB();
    if (mode == 2) pieceA(sub);
    if (!LATE && !sub) upB(2, 5);
    SB();
  };
  auto U = [&](int sub) {
    if (sub) { va1 += dA; PIN(va1); upB(0, LATE ? 5 : 2); }
    else { va0 += dA; PIN(va0); }
    SB();
  };
  auto stats = [&](int sub) {
    const u32x4 one = {0x3f803f80u, 0x3f803f80u, 0x3f803f80u, 0x3f803f80u};
    const u32x4 sa = lds_b128((sub ? va1 : va0) + c.nw * 2048);
    u32x4 sb[3];
#pragma unroll
    for (int i = 0; i < 3; ++i) { unsigned a = adB(LATE ? i : 3 + i); PIN(a); sb[i] = lds_trfrag(a + sub * L1_BST); }
    SB();
    if (sub == 0) {
#pragma unroll
      for (int i = 0; i < 5; ++i) st[i] = f32x4{0.f, 0.f, 0.f, 0.f};
    }
    st[0] = mfma(one, sa, st[0]);
    st[1] = mfma(sa, sa, st[1]);
#pragma unroll
    for (int i = 0; i < 3; ++i) st[2 + i] = mfma(one, sb[i], st[2 + i]);
    if (sub == 1) {
      const int cl = lane & 15, g = lane >> 4;
      const float sq = (cl & 2) ? ((cl & 1) ? st[1][3] : st[1][2]) : ((cl & 1) ? st[1][1] : st[1][0]);
      st8(rws, (cl >> 2) == g ? (c.mw * 64 + c.nw * 16 + cl) * 8 : OOB, c.cslot < 10 ? OFF_SR + (c.Mt * 10 + c.cslot) * 1024 : WS_END,
          u32x2{__builtin_bit_cast(unsigned, st[0][0]), __builtin_bit_cast(unsigned, sq)});
      const float v = g == 0 ? st[2][0] : g == 1 ? st[3][0] : st[4][0];
      __builtin_amdgcn_raw_buffer_store_b32(__builtin_bit_cast(unsigned, v), rws, g < 3 ? ((c.nw * 6 + c.mw * 3) * 16 + lane) * 4 : OOB,
                                            c.Mt < 10 ? OFF_SC + (c.cslot * 10 + c.Mt) * 1536 : WS_END, SC1);
    }
    SB();
  };
#define SEG(i)
  auto C = [&](int sub, bool more, int book, auto vmwait) {
    SEG(0);
    __builtin_amdgcn_s_setprio(1);
    if (LATE) { vmwait(); WAIT_L0(); BAR(); }
    SB();
#pragma unroll
    for (int k = 0; k < 4 * NT; ++k) {
      const int i = k >> 2, m = k & 3, ms = (i & 1) ? 3 - m : m;
      acc[ms][i] = TYPE == 1 ? mfma(fa[ms], fb[i], acc[ms][i]) : mfma(fb[i], fa[ms], acc[ms][i]);
      if (LATE) { if (more && i >= 1 && i < 5 && m < 2) rdBh(i - 1, m, sub ^ 1); }
      else if (k < 2 * (NT - 2)) rdBh(2 + (k >> 1), k & 1, sub);
      const int q = LATE ? (k < 2 ? k : m >= 2 && i < 5 ? 2 * i + m : -1) : k - 2 * (NT - 2);
      constexpr int QA = LATE ? 8 : 0, QU = LATE ? 0 : 3;
      if (book == 1) {
        if (AC) {
          if (q == QA) setm0(lA + (LATE ? sub ^ 1 : sub) * 8192);
          if (q == QA + 1) pieceA(LATE ? sub ^ 1 : sub);
        }
        if (q == QU) { c.oB += stepB; lB += L1_BST; PINS(c.oB); PINS(lB); }
        if (q == QU + 1) { c.oB = c.oB == limB ? 0u : c.oB; PINS(c.oB); }
        if (q == QU + 2) { lB = lB == lB0 + 4 * L1_BST ? lB0 : lB; PINS(lB); }
        if (sub) {
          if (q == QU + 3) { c.oA += 128; lA += L1_AST; PINS(c.oA); PINS(lA); }
          if (q == QU + 4) { c.oA = c.oA == 1280u ? 0u : c.oA; PINS(c.oA); }
          if (q == QU + 5) { lA = lA == lA0 + 3 * L1_AST ? lA0 : lA; PINS(lA); }
          if (q == QU + 6) { dA = lA == lA0 + L1_AST ? -2 * L1_AST : L1_AST; PINS(dA); }
          if (q == QU + 7) { rare -= 1; PINS(rare); }
        }
        if (!sub && q == QU + 3) { rare2 = LATE ? (rare + 2) & ~2 : rare; PINS(rare2); }
        if (q == 11) setm0(lB);
      }
      if (book && q == 10 && (LATE ? sub : !sub)) { dB = -dB; PINS(dB); }
      if (!LATE && EB > 0) {
        if (k == 4 * NT - 2 - EB) { vmwait(); WAIT_L0(); }
        if (k == 4 * NT - 1 - EB) { __builtin_amdgcn_s_setprio(2); BAR(); }
      }
      SB();
    }
    __builtin_amdgcn_s_setprio(0);
    if (!LATE && EB == 0) { vmwait(); WAIT_L0(); BAR(); }
  };
  constexpr int LM = AC ? 1 : 2;
  if (!LATE) { upB(2, 5); dB = -dB; }
  BAR();
  if (LATE) {
    if (AC) { setm0(lA); SB(); }
    rdB(0, 0); rdB(1, 0); SB();
    if (AC) pieceA(0);
  }
  setm0(lB);
  SB();
  if (LATE) { rdB(2, 0); rdB(3, 0); } else asm volatile("s_nop 0");
  SB();
  constexpr int X = TYPE == 0;
  const int rend = c.bstar - (X ? 7 : 8);
  const u32x4 rw4 = srd(ws, WS_END);
  u32x2 fl = {0u, 0u};
#pragma unroll 1
  do {
    if (X && __builtin_expect(rare == rend + 1, 0)) ld8_hidden(fl, rw4, foff);
    L(0, LM);
    if (__builtin_expect(rare == 0, 0)) stats(0);
    U(0);
    C(0, true, 1, []() { WAIT_V(8); });
    L(1, LM);
    if (__builtin_expect(rare2 == 0, 0)) {
      PINS(rare);
      if (rare == 0) stats(1);
      else if (LATE && c.w == 0 && lane == 0) set_flag(ws, OFF_F1 + (c.Mt * 16 + c.cslot) * 8, tag);
    }
    U(1);
    C(1, true, 1, []() { WAIT_V(8); });
  } while (rare != rend);
  auto check = [&]() {
    PIN(fl);
    if (__builtin_expect(!__all(__builtin_bit_cast(u64, fl) == tag), 0)) wait_flags(ws, foff, tag);
  };
  if (X) {
    L(0, LM);
    WAIT_V(11);
    check();
    pf.items<13, 16>();
    U(0);
    C(0, true, 1, []() { WAIT_V(11); });
    L(1, LM);
    pf.items<16, 19>();
    U(1);
    C(1, true, 1, []() { WAIT_V(14); });
  }
  if (!X) ld8_hidden(fl, rw4, foff);
  L(0, 1);
  if (X) pf.items<19, 22>();
  U(0);
  C(0, true, 2, []() { wait_v<X ? 13 : 8>(); });
  L(1, 0);
  wait_v<X ? 6 : 3>();
  if (!X) check();
  dma_sc1(rw4, lane * 16, OFF_SC + c.cslot * 15360 + c.w * 1024, c.ldsb + L1_GC + c.w * 1024);
  dma_sc1(rw4, lane * 16, OFF_SC + c.cslot * 15360 + c.w * 1024 + 8192, c.ldsb + L1_GC + c.w * 1024 + 8192);
  dma_sc1(rw4, lane * 16, OFF_SR + c.Mt * 10240 + c.w * 1024, c.ldsb + L1_GR + c.w * 1024);
  if (LATE && c.w < 2) dma_sc1(rw4, lane * 16, OFF_SR + c.Mt * 10240 + c.w * 1024 + 8192, c.ldsb + L1_GR + c.w * 1024 + 8192);
  if (X) pf.items<22, 24>();
  U(1);
  C(1, true, 2, []() {});
  L(0, 0);
  if (TYPE != 1) wd_prefetch<0, 4>(rw4, c.ldsb, c.j, c.w, lane);
  if (X) pf.items<24, 26>();
  C(0, true, 0, []() { wait_v<X ? 8 : TYPE != 1 ? 4 : 0>(); });
  L(1, 0);
  {
    __builtin_amdgcn_s_setprio(2);
    if (LATE || c.w < 6) {
      const LAS float* src = (const LAS float*)(size_t)(c.ldsb + L1_GC + (c.w * 64 + lane) * 4);
      float v[10];
#pragma unroll
      for (int p = 0; p < 10; ++p) v[p] = src[p * 384];
      *(LAS float*)(size_t)(c.ldsb + L1_CS + (c.w * 64 + lane) * 4) = (((v[0] + v[1]) + (v[2] + v[3])) + ((v[4] + v[5]) + (v[6] + v[7]))) + (v[8] + v[9]);
    } else {
      const LAS f32x2* src = (const LAS f32x2*)(size_t)(c.ldsb + L1_GR + ((c.w - 6) * 64 + lane) * 8);
      f32x2 v[10];
#pragma unroll
      for (int p = 0; p < 10; ++p) v[p] = src[p * 128];
      const f32x2 s = ((((v[0] + v[1]) + (v[2] + v[3])) + ((v[4] + v[5]) + (v[6] + v[7]))) + (v[8] + v[9])) * (1.0f / kD);
      LAS float* d = (LAS float*)(size_t)(c.ldsb + L1_ST + ((c.w - 6) * 64 + lane) * 4);
      d[0] = s[0]; d[128] = __builtin_amdgcn_rsqf(fmaxf(__builtin_fmaf(-s[0], s[0], s[1]), 0.f) + 1e-5f);
    }
    __builtin_amdgcn_s_setprio(0);
  }
  SB();
  if (TYPE != 1) wd_prefetch<4, 6>(rw4, c.ldsb, c.j, c.w, lane);
  if (X) pf.items<26, 28>();
  SB();
  C(1, false, 0, []() {});
}

template <int N, class F> DEVI void static_for(F&& f) {
  if constexpr (N > 0) { static_for<N - 1>(f); f(std::integral_constant<int, N - 1>{}); }
}
DEVI u32x4 pair16(u32x2 a, u32x2 b) {
  const auto s0 = __builtin_amdgcn_permlane32_swap(a[0], b[0], false, false);
  const auto s1 = __builtin_amdgcn_permlane32_swap(a[1], b[1], false, false);
  return u32x4{s0[0], s1[0], s0[1], s1[1]};
}
struct Watch {
  LAS unsigned* lf; char* ws; u64 tag; int mt2, j, w, lane;
  unsigned status;
  DEVI u64 fetch() {
    int l2 = lane;
    PIN(l2);
    const unsigned f0 = l2 < 32 ? OFF_FH + mt2 * 256 + (j >> 2) * 128 + (l2 < 11 ? l2 : 0) * 8 : OFF_F3 + (blockIdx.x & 7) * 128 + (l2 & 15) * 8;
    return __hip_atomic_load((GAS u64*)(ws + f0), RLX_AGENT);
  }
  DEVI bool eval(u64 v, bool giveup) {
    const u64 no = __ballot(v != tag);
    unsigned s = ((no & 0x1ffull) ? 0u : S_H) | ((no & 0x600ull) ? 0u : S_Q) | ((no >> 32) ? 0u : S_M);
    if (giveup) s = 7u;
    if (s == status) return false;
    status = s;
    __hip_atomic_store(lf + LF_STATUS, s, RLX_WG);
    return true;
  }
  DEVI bool poll(bool giveup) { return eval(fetch(), giveup); }
  DEVI bool has(unsigned bits) { return (~(unsigned)__builtin_amdgcn_readfirstlane(__hip_atomic_load(lf + LF_STATUS, RLX_WG)) & bits) == 0u; }
  int pcnt, plast, preps; unsigned pflag;
  DEVI void publish() {
    if ((int)lds_inc(lf + LF_PUB + pcnt, lane) == plast && lane < preps) set_flag(ws, pflag + lane * 128, tag);
    preps = 0;
  }
  DEVI void need(unsigned bits, int watcher = 7) {
    if (w != watcher) { lds_wait_bits(lf + LF_STATUS, bits); return; }
#pragma unroll 1
    for (unsigned spins = 0; (~status & bits) != 0u; ++spins)
      if (!poll(spins >= (1u << 20))) __builtin_amdgcn_s_sleep(1);
    asm volatile("" ::: "memory");
  }
};

#define EPI_N 12
DEVI void epi_ffn(LAS char* lds, f32x4 (&acc)[4][6], char* ws, rsrc_t rws, Watch& wt, const P1& c, PF2& pf, int lane) {
  const int r = lane & 15, g = lane >> 4;
  pf.items<0, 2>();
  float nm[4], k1[4], k2[4], iv[4];
#pragma unroll
  for (int mi = 0; mi < 4; ++mi) {
    const LAS float* sp = (const LAS float*)(lds + L1_ST) + c.mw * 64 + mi * 16 + r;
    const float rs = sp[128], c0 = -2.0f * 0.7978845608028654f * 1.4426950408889634f;
    nm[mi] = -sp[0]; k1[mi] = c0 * rs; k2[mi] = c0 * 0.044715f * rs * rs * rs; iv[mi] = __builtin_amdgcn_rcpf(rs * rs);
  }
  const unsigned hb = OFF_H + ((c.Mt * 2 + c.mw) * 54 + 2 * c.cslot + (c.nw >> 1)) * 4096 + (c.nw & 1) * 512 + (lane & 31) * 16 + (lane >> 5) * 1024;
  u32x4 v[6];
  static_for<3>([&](auto I) {
    constexpr int i = I;
    const f32x4 cg = *(LAS f32x4*)(lds + L1_CS + ((c.nw * 6 + i) * 16 + 4 * g) * 4);
    const f32x4 cu = *(LAS f32x4*)(lds + L1_CS + ((c.nw * 6 + 3 + i) * 16 + 4 * g) * 4);
    u32x2 o[4];
    static_for<4>([&](auto MI) {
      constexpr int mi = MI, n = 4 * i + mi;
      if constexpr (2 + 2 * n < EPI_N) pf.template items<2 + 2 * n, 4 + 2 * n>();
#pragma unroll
      for (int hh = 0; hh < 2; ++hh) {
        const f32x2 G = fma2(bc2(nm[mi]), hh ? hi2(cg) : lo2(cg), hh ? hi2(acc[mi][i]) : lo2(acc[mi][i]));
        const f32x2 U = fma2(bc2(nm[mi]), hh ? hi2(cu) : lo2(cu), hh ? hi2(acc[mi][3 + i]) : lo2(acc[mi][3 + i]));
        const f32x2 z = G * fma2(G * G, bc2(k2[mi]), bc2(k1[mi]));
        const f32x2 d = fma2(f32x2{__builtin_amdgcn_exp2f(z[0]), __builtin_amdgcn_exp2f(z[1])}, bc2(iv[mi]), bc2(iv[mi]));
        o[mi][hh] = pk2(G * U * f32x2{__builtin_amdgcn_rcpf(d[0]), __builtin_amdgcn_rcpf(d[1])});
      }
      PIN(o[mi]);
      SB();
#define STN 4
#define STS 4
      if constexpr (n == STN || n == STN + STS) {
        constexpr int p = (n - STN) / STS;
        st16(rws, hb, p * 18 * 4096, v[2 * p]);
        st16(rws, hb, p * 18 * 4096 + 2048, v[2 * p + 1]);
        SB();
      }
    });
    v[2 * i] = pair16(o[0], o[1]); v[2 * i + 1] = pair16(o[2], o[3]);
    PIN(v[2 * i]); PIN(v[2 * i + 1]);
  });
  st16(rws, hb, 36 * 4096, v[4]);
  st16(rws, hb, 36 * 4096 + 2048, v[5]);
  STAMP(4);
  BAR();
  WAIT_V(0);
  wt.pcnt = c.mw; wt.plast = 3; wt.preps = 2; wt.pflag = OFF_FH + (c.Mt * 2 + c.mw) * 256 + c.cslot * 8;
  wt.publish();
  STAMP(5);
  pf.items<EPI_N, 13>();
  WAIT_V(0);
}

DEVI void epi_q(LAS char* lds, f32x4 (&acc)[4][6], char* ws, rsrc_t rws, Watch& wt, const P1& c, PF2& pf, int lane) {
  const int r = lane & 15, g = lane >> 4, qi = c.cslot - 13;
  f32x4 cs[5];
#pragma unroll
  for (int i = 0; i < 5; ++i) cs[i] = *(LAS f32x4*)(lds + L1_CS + ((c.nw * 6 + i) * 16 + 4 * g) * 4);
  pf.items<0, 8>();
  static_for<4>([&](auto MI) {
    constexpr int mi = MI;
    const LAS float* sp = (const LAS float*)(lds + L1_ST) + c.mw * 64 + mi * 16 + r;
    const f32x2 nm = bc2(-sp[0]), rs = bc2(sp[128]);
    static_for<5>([&](auto I) {
      constexpr int i = I;
      const f32x2 a = phi2(fma2(nm, lo2(cs[i]), lo2(acc[mi][i])), rs), b = phi2(fma2(nm, hi2(cs[i]), hi2(acc[mi][i])), rs);
      acc[mi][i] = f32x4{a[0], a[1], b[0], b[1]};
      PIN(acc[mi][i]);
      SB();
      pf.template item<8 + mi * 5 + i>();
    });
  });
  STAMP(3);
  LAS unsigned* lf = (LAS unsigned*)(lds + L2_FLAG);
  if (c.w == 4) { wait_flags(ws, OFF_FZ + (blockIdx.x & 7) * 512 + qi * 256 + (lane & 31) * 8, wt.tag); lds_inc(lf + LF_K, lane); }
  else lds_wait(lf + LF_K, 1u);
  STAMP(12);
  {
    const int q4 = (c.w & 1) * 64 + lane;
    u32x4 gr[8];
#pragma unroll 1
    for (unsigned spins = 0; spins < (1u << 20); ++spins) {
#pragma unroll
      for (int p = 0; p < 8; ++p)
        gr[p] = ld16(rws, q4 < 40 ? q4 * 16 : q4 < 80 ? q4 * 16 + 31 * 640 : OOB, OFF_ZP + (qi * 64 + (c.w >> 1) * 8 + p) * 640);
      SB();
      unsigned bad = 0;
#pragma unroll
      for (int p = 0; p < 8; ++p) bad |= (gr[p][1] ^ (unsigned)wt.tag) | (gr[p][3] ^ (unsigned)wt.tag);
      if (__all(bad == 0u || q4 >= 80)) break;
      asm volatile("" ::: "memory");
    }
    f32x4 part[8];
#pragma unroll
    for (int p = 0; p < 8; ++p) { const f32x2 a = unpk(gr[p][0]), b = unpk(gr[p][2]); part[p] = f32x4{a[0], a[1], b[0], b[1]}; }
    if (q4 < 80) *(LAS f32x4*)(lds + L2_RED + (c.w >> 1) * 1280 + q4 * 16) = ((part[0] + part[1]) + (part[2] + part[3])) + ((part[4] + part[5]) + (part[6] + part[7]));
    WAIT_L0(); BAR();
  }
  u64 fv = 0;
  STAMP(5);
  f32x4 z[5];
#pragma unroll
  for (int i = 0; i < 5; ++i) {
    const LAS char* p = lds + L2_RED + (c.nw * 80 + 20 * (2 * (g & 1) + (g >> 1)) + 4 * i) * 4;
    z[i] = (*(const LAS f32x4*)p + *(const LAS f32x4*)(p + 1280)) + (*(const LAS f32x4*)(p + 2560) + *(const LAS f32x4*)(p + 3840));
  }
  const unsigned qb = OFF_Q + ((c.Mt * 2 + c.mw) * 20 + (2 * qi + (c.nw >> 1)) * 5) * 4096;
  u32x2 keep;
#pragma unroll
  for (int mi = 0; mi < 4; ++mi) {
    f32x2 d0 = lo2(acc[mi][0]) * lo2(z[0]), d1 = hi2(acc[mi][0]) * hi2(z[0]);
#pragma unroll
    for (int i = 1; i < 5; ++i) { d0 = fma2(lo2(acc[mi][i]), lo2(z[i]), d0); d1 = fma2(hi2(acc[mi][i]), hi2(z[i]), d1); }
    d0 += d1;
    const f32x2 inv = bc2(__builtin_amdgcn_rcpf(d0[0] + d0[1] + 1e-6f));
    unsigned o[10];
#pragma unroll
    for (int i = 0; i < 5; ++i) { o[2 * i] = pk2(lo2(acc[mi][i]) * inv); o[2 * i + 1] = pk2(hi2(acc[mi][i]) * inv); }
    st16(rws, lane * 16 + mi * 1024, qb + (c.nw & 1) * 12288, u32x4{o[0], o[1], o[2], o[3]});
    st16(rws, lane * 16 + mi * 1024, qb + (c.nw & 1) * 12288 + 4096, u32x4{o[4], o[5], o[6], o[7]});
    if (mi & 1) st16(rws, (lane & 31) * 16 + (lane >> 5) * 1024 + (mi - 1) * 1024, qb + 8192 + (c.nw & 1) * 512, pair16(keep, u32x2{o[8], o[9]}));
    else keep = u32x2{o[8], o[9]};
    if (mi == 0 && c.w == 7) { SB(); fv = wt.fetch(); SB(); }
  }
  STAMP(4);
  SB();
  if (c.w == 7) wt.eval(fv, false);
  WAIT_L0(); BAR();
  wt.pcnt = c.mw; wt.plast = 3; wt.preps = 2; wt.pflag = OFF_FH + (c.Mt * 2 + c.mw) * 256 + (9 + qi) * 8;
}

DEVI void epi_kv(LAS char* lds, f32x4 (&acc)[4][6], char* ws, rsrc_t rws, u64 tag, const P1& c, int lane) {
  const int cl = lane & 15, g = lane >> 4, kj = c.cslot - 9;
  float cs[5];
#pragma unroll
  for (int i = 0; i < 5; ++i) cs[i] = -*(LAS float*)(lds + L1_CS + ((c.nw * 6 + i) * 16 + cl) * 4);
  f32x4 mean[4], rstd[4];
#pragma unroll
  for (int mi = 0; mi < 4; ++mi) {
    mean[mi] = *(LAS f32x4*)(lds + L1_ST + (c.mw * 64 + mi * 16 + 4 * g) * 4);
    rstd[mi] = *(LAS f32x4*)(lds + L1_ST + 512 + (c.mw * 64 + mi * 16 + 4 * g) * 4);
  }
  u32x4 fk[2][3], fv[2][3];
  auto center = [&](int mi, int i, int hh) { return fma2(hh ? hi2(mean[mi]) : lo2(mean[mi]), bc2(cs[i]), hh ? hi2(acc[mi][i]) : lo2(acc[mi][i])); };
#pragma unroll
  for (int mi = 0; mi < 4; ++mi)
#pragma unroll
    for (int f = 0; f < 3; ++f)
#pragma unroll
      for (int hh = 0; hh < 2; ++hh)
        fk[mi >> 1][f][(mi & 1) * 2 + hh] = pk2(phi2(center(mi, f < 2 ? f : 4, hh), hh ? hi2(rstd[mi]) : lo2(rstd[mi])));
  const int uh = cl < 4 ? 16 + cl : 32 + cl;
  const f32x4 zero = {0.f, 0.f, 0.f, 0.f};
  {
    const u32x4 one = {0x3f803f80u, 0x3f803f80u, 0x3f803f80u, 0x3f803f80u};
    f32x4 z[3];
#pragma unroll
    for (int f = 0; f < 3; ++f) z[f] = mfma(one, fk[1][f], mfma(one, fk[0][f], zero));
    const float v = g == 0 ? z[0][0] : g == 1 ? z[1][0] : z[2][0];
    const float vn = __builtin_bit_cast(float, __builtin_amdgcn_mov_dpp(__builtin_bit_cast(int, v), 0xB1, 0xf, 0xf, true));
    st8(rws, (cl & 1) ? OOB : g < 2 ? (g * 20 + cl) * 4 : (g == 2 && cl < 8) ? uh * 4 : OOB,
        OFF_ZP + (kj * 32 + c.Mt * 2 + c.mw) * 640 + c.nw * 160, u32x2{pk2(v, vn), (unsigned)tag});
    if (lds_inc((LAS unsigned*)(lds + L2_FLAG) + LF_PUB, lane) == 7u && lane < 8) set_flag(ws, OFF_FZ + lane * 512 + kj * 128 + c.Mt * 8, tag);
    SB();
  }
#pragma unroll
  for (int mi = 0; mi < 4; ++mi)
#pragma unroll
    for (int f = 0; f < 3; ++f)
#pragma unroll
      for (int hh = 0; hh < 2; ++hh)
        fv[mi >> 1][f][(mi & 1) * 2 + hh] = pk2(center(mi, 2 + f, hh) * (hh ? hi2(rstd[mi]) : lo2(rstd[mi])));
  constexpr int KI[7] = {0, 0, 2, 1, 1, 2, 2}, VI[7] = {0, 2, 0, 1, 2, 1, 2};
  f32x4 p[7];
#pragma unroll
  for (int q = 0; q < 7; ++q) p[q] = mfma(fv[1][VI[q]], fk[1][KI[q]], mfma(fv[0][VI[q]], fk[0][KI[q]], zero));
  LAS char* ex = lds + L1_EX + c.w * 3584;
  auto put = [&](bool ok, int u, int e, f32x4 v) { *(LAS f32x4*)(ex + (ok ? (u * 20 + e) * 4 : 3200)) = v; };
  put(true, cl, 4 * g, p[0]);
  put(true, 20 + cl, 4 * g, p[3]);
  put(g >= 2, (g & 1) * 20 + cl, 16, g == 2 ? p[1] : p[4]);
  put(cl < 8, uh, 4 * g, cl < 4 ? p[2] : p[5]);
  put(cl < 8 && g == 2 + (cl >> 2), uh, 16, p[6]);
  WAIT_L0(); BAR();
#pragma unroll
  for (int q = 0; q < 2; ++q) {
    const int o = (c.mw * 128 + q * 64 + lane) * 16;
    const LAS char* src = lds + L1_EX + c.nw * 3584 + (o < 3200 ? o : 0);
    const f32x4 v = *(const LAS f32x4*)src + *(const LAS f32x4*)(src + 4 * 3584);
    st16(rws, o < 3200 ? o : OOB, OFF_KVP + ((kj * 4 + c.nw) * 16 + c.Mt) * 3456, __builtin_bit_cast(u32x4, v));
  }
  SB();
  STAMP(4);
  LAS unsigned* lf = (LAS unsigned*)(lds + L2_FLAG);
  WAIT_V(0);
  if (lds_inc(lf + LF_PUB + 1, lane) == 7u && lane < 8) set_flag(ws, OFF_FK + lane * 512 + kj * 128 + c.Mt * 8, tag);
  BAR();
}

DEVI void retile(const char* wd, rsrc_t rws, unsigned ldsw, int id, int w, int lane) {
  const u32x4 rd = srd(wd, kF * 1280);
  const int jj = lane >> 3, g = lane >> 4;
  const unsigned vo = ((jj >> 2) * 8 + (jj & 3)) * 1280 + (lane & 7) * 16;
  const unsigned ra = ldsw + g * 1024 + ((lane & 15) >> 2) * 128 + (lane & 3) * 8;
  auto fetch = [&](int n) {
    const int it = id * 8 + w + n * 128, rb = it / 10, ln = it - rb * 10;
#pragma unroll
    for (int q = 0; q < 4; ++q)
      dma<0>(rd, vo, it < 540 ? (rb * 32 + 16 * (q >> 1) + 4 * (q & 1)) * 1280 + ln * 128 : OOB, ldsw + (n & 3) * 4096 + q * 1024);
  };
  auto put = [&](int n) {
    const int it = id * 8 + w + n * 128, rb = it / 10, ln = it - rb * 10, cg = rb / 6, uu = rb - cg * 6;
    const int ks = 18 * (uu >> 1) + 2 * cg + (uu & 1);
#pragma unroll
    for (int q = 0; q < 4; ++q) {
      const int nt = ln * 4 + q, j = nt / 5, t = nt - j * 5;
      st16(rws, lane * 16, it < 540 ? OFF_WD + ((j * 54 + ks) * 5 + t) * 1024 : OOB, lds_trfrag(ra + (n & 3) * 4096 + q * 32));
    }
  };
#pragma unroll 1
  for (int n = 0; n < 4; ++n) fetch(n);
  WAIT_V(0);
  put(0);
  WAIT_L0();
  fetch(4);
#pragma unroll 1
  for (int n = 1; n < 5; ++n) {
    if (n == 4) WAIT_V(0);
    put(n);
  }
}

DEVI void fold_kv(LAS char* lds, char* ws, rsrc_t rws, Watch& wt, const u32x4 (&wf)[2][5], int hp, int w, int lane) {
  const u64 tag = wt.tag;
  const int cl = lane & 15, g = lane >> 4;
  const int o = ((w & 3) * 64 + lane) * 16;
  unsigned po = o < 3200 ? o : OOB, ko[6][2];
#pragma unroll
  for (int f = 0; f < 6; ++f) {
    const int u = 20 * (f & 1) + (f < 4 ? (cl & 4) * 2 + 4 * (f >> 1) : 16) + (cl & 3);
    const bool mine = f >= 4 || (cl >> 3) == (f & 1);
    ko[f][0] = (unsigned)(size_t)lds + L2_RED + (mine && g < 3 ? u * 80 + g * 32 : 3360);
    ko[f][1] = (unsigned)(size_t)lds + L2_RED + (mine && g < 2 ? u * 80 + g * 32 + 16 : 3360);
    PIN(ko[f][0]); PIN(ko[f][1]);
  }
  unsigned vo = OFF_MP + (w * 20 + (hp >> 2) * 5) * 5120 + (3 * ((hp >> 1) & 1) + (g & 1)) * 5120 + (((hp & 1) + (g & 2)) * 16 + cl) * 16;
  unsigned vo2 = OFF_MP + (w * 20 + (hp >> 2) * 5 + 2) * 5120 + g * 1024 + ((hp & 3) * 16 + cl) * 16;
  PIN(po); PIN(vo); PIN(vo2);
  WAIT_V(0);
  if (w == 0) wait_flags(ws, OFF_FK + (blockIdx.x & 7) * 512 + (hp >> 2) * 128 + cl * 8, tag);
  BAR();
  STAMP(2);
  {
    f32x4 part[8];
#pragma unroll
    for (int p = 0; p < 8; ++p) part[p] = __builtin_bit_cast(f32x4, ld16(rws, po, OFF_KVP + (hp * 16 + (w >> 2) * 8 + p) * 3456));
    SB();
    *(LAS f32x4*)(lds + L2_RED + (w >> 2) * 4096 + o) = ((part[0] + part[1]) + (part[2] + part[3])) + ((part[4] + part[5]) + (part[6] + part[7]));
    WAIT_L0(); BAR();
  }
  STAMP(12);
  u64 fv = 0;
  if (w == 7) fv = wt.fetch();
  SB();
  u32x4 kf[6];
#pragma unroll
  for (int f = 0; f < 6; ++f) {
    const f32x4 lo = *(LAS f32x4*)(size_t)ko[f][0] + *(LAS f32x4*)(size_t)(ko[f][0] + 4096);
    const f32x4 hi = *(LAS f32x4*)(size_t)ko[f][1] + *(LAS f32x4*)(size_t)(ko[f][1] + 4096);
    kf[f] = u32x4{pk2(lo2(lo)), pk2(hi2(lo)), pk2(lo2(hi)), pk2(hi2(hi))};
  }
  const f32x4 zero = {0.f, 0.f, 0.f, 0.f};
  u32x4 r1;
#pragma unroll
  for (int i = 0; i < 5; ++i) {
    const f32x4 d1 = mfma(kf[1], wf[1][i], mfma(kf[0], wf[0][i], zero)), d2 = mfma(kf[3], wf[1][i], mfma(kf[2], wf[0][i], zero));
    const f32x4 d3 = mfma(kf[4], wf[0][i], zero), d4 = mfma(kf[5], wf[1][i], zero);
    st16(rws, vo, i * 1024, u32x4{pk2(lo2(d1)), pk2(hi2(d1)), pk2(lo2(d2)), pk2(hi2(d2))});
    const u32x4 t = {pk2(lo2(d3)), pk2(hi2(d3)), pk2(lo2(d4)), pk2(hi2(d4))};
    if (i == 0 || i == 4) r1 = t; else r1 = g == i ? t : r1;
    if (i == 3) st16(rws, vo2, 0, r1);
    if (i == 4) st16(rws, g == 0 ? vo2 + 4096 : OOB, 0, r1);
  }
  STAMP(3);
  SB();
  if (w == 7) wt.eval(fv, false);
  WAIT_L0(); BAR();
  wt.pcnt = 0; wt.plast = 7; wt.preps = 8; wt.pflag = OFF_F3 + hp * 8;
}

#define ASLEEP 0
#define FPRE 3
template <bool ATT>
DEVI void p2_steps(LAS char* lds, rsrc_t rws, Watch& wt, const u32x4 (&br)[3][5], f32x4 (&acc)[4][5], const u32x2 (&xr)[5],
                   int mt2, int j, int w, int lane) {
  LAS unsigned* lf = wt.lf;
  const unsigned ab = OFF_H + (mt2 * 54 + w) * 4096;
  u32x4 fa[12][4], fb[12][5];
  auto ldA = [&](int n) {
    const unsigned so = n >= 7 ? OFF_Q + (mt2 * 20 + (w - 4) * 5 + n - 7) * 4096
                               : n == 6 && w >= 6 ? OOB : ab + (n < 3 ? 30 + 8 * ((n + (mt2 >> 1)) % 3) : 8 * n - 24) * 4096;
#pragma unroll
    for (int m = 0; m < 4; ++m) fa[n][m] = ld16(rws, lane * 16, so + ((m + w) & 3) * 1024);
    SB();
  };
  unsigned lb = (unsigned)(size_t)lds + w * 5120 + lane * 16;
  auto rdB = [&](int n) {
    if (n == 5) { lb += 81920; PIN(lb); }
    if (ATT && n == 6) { lb -= w >= 6 ? 40960 : 0; PIN(lb); }
#pragma unroll
    for (int t = 0; t < 5; ++t) fb[n][t] = lds_b128(lb + ((n - 3) & 1) * 40960 + t * 1024);
    SB();
  };
  auto ld1 = [&](int kind, int n, int i) {
    if (kind == 0) {
      const unsigned so = n >= 7 ? OFF_Q + (mt2 * 20 + (w - 4) * 5 + n - 7) * 4096
                                 : n == 6 && w >= 6 ? OOB : ab + (n < 3 ? 30 + 8 * ((n + (mt2 >> 1)) % 3) : 8 * n - 24) * 4096;
      fa[n][i] = ld16(rws, lane * 16, so + ((i + w) & 3) * 1024);
    } else fb[n][i] = ld16(rws, lane * 16, OFF_MP + (j * 20 + (w - 4) * 5 + n - 7) * 5120 + i * 1024);
    SB();
  };
  LAS char* ex = lds + w * 5120 + lane * 16;
  constexpr int PRE = ATT ? 6 : FPRE;
  if (ATT && ASLEEP && wt.preps == 0) __builtin_amdgcn_s_sleep(ASLEEP);
#pragma unroll
  for (int n = 0; n < PRE; ++n) {
    ldA(n);
    if (n == 1 && wt.preps != 0) { WAIT_V(8); wt.publish(); STAMP(13); }
  }
  if (ATT) {
#pragma unroll
    for (int t = 0; t < 5; ++t) {
      const f32x2 x0 = unpk(xr[t][0]), x1 = unpk(xr[t][1]);
      acc[0][t] = f32x4{x0[0], x0[1], x1[0], x1[1]};
      PIN(acc[0][t]);
    }
    SB();
    asm volatile("s_nop 1" ::: "memory");
  }
  constexpr int A1[12] = {ATT ? -1 : FPRE, ATT ? 6 : FPRE == 3 ? 5 : FPRE == 4 ? 6 : -1, ATT ? 7 : -1, -1, ATT ? 8 : -1, -1, ATT ? 9 : -1, 10, 11, -1, -1, -1};
  constexpr int A2[12] = {ATT ? -1 : FPRE + 1, ATT || FPRE > 3 ? -1 : 6, -1, -1, -1, -1, -1, -1, -1, -1, -1, -1};
  constexpr int M1[12] = {-1, -1, -1, ATT ? 7 : -1, -1, ATT ? 8 : -1, ATT ? 9 : -1, 10, 11, -1, -1, -1};
  u64 fv = 0;
  static_for<(ATT ? 12 : 7)>([&](auto N) {
    constexpr int n = N;
    if (n == 2) lds_wait(lf + LF_DMA, 8u);
    if (ATT && n == 2) lds_wait_bits(lf + LF_STATUS, S_Q);
    if (ATT && n == 3) { lds_wait_bits(lf + LF_STATUS, S_M); STAMP(7); }
    if (!ATT && (n == 3 || n == 5) && w == 3) { if (n == 5) wt.eval(fv, false); fv = wt.fetch(); SB(); }
    if (n >= 2 && n < 6) rdB(n + 1);
    if (n == (ATT ? 11 : 6)) lds_wait(lf + LF_BC + ATT, 4u);
#pragma unroll
    for (int q = 0; q < 20; ++q) {
      constexpr int LASTN = ATT ? 11 : 6;
      const int m = n == LASTN ? (q / 5 + 1) & 3 : n < 7 ? q / 5 : q & 3, t = n < 7 || n == LASTN ? q % 5 : q >> 2;
      if (n == 0 && (m > 0 || !ATT)) mfma_new(acc[m][t], br[0][t], fa[0][m]);
      else mfma_acc(acc[m][t], n < 3 ? br[n < 3 ? n : 0][t] : fb[n][t], fa[n][m]);
      if (n == LASTN && q >= 10) {
        SB();
        *(LAS f32x4*)(ex + ((q - 10) / 5) * 40960 + (q % 5) * 1024) = acc[(q - 10) / 5 + 1][q % 5];
        SB();
      }
      if (q & 1) {
        const int p = q >> 1, na = (A1[n] >= 0 ? 4 : 0) + (A2[n] >= 0 ? 4 : 0);
        if (p < na) ld1(0, p < 4 ? A1[n] : A2[n], p & 3);
        else if (M1[n] >= 0 && p - na < 5) ld1(1, M1[n], p - na);
      }
    }
    SB();
    if (n == 0) STAMP(9);
    if (n == 6) STAMP(11);
    if (n == 5) { WAIT_L0(); lds_inc(lf + LF_BC + ATT, lane); }
  });
  if (!ATT && w == 3) wt.eval(fv, false);
}

DEVI void phase2(LAS char* lds, const char* x, char* ws, rsrc_t rws, char* out, Watch& wt, u32x4 (&br)[3][5],
                 int mt2, int j, int w, int lane) {
  const int a = w & 3;
  LAS unsigned* lf = wt.lf;
  const unsigned obase = (mt2 * 64 + a * 16) * 1280 + j * 160;
  f32x4 acc[4][5];
  u32x2 xr[5];
  if (w >= 4) {
    const rsrc_t rx = __builtin_amdgcn_make_buffer_rsrc((void*)x, 0, 2048 * 1280, 0x00020000);
#pragma unroll
    for (int t = 0; t < 5; ++t) xr[t] = __builtin_amdgcn_raw_buffer_load_b64(rx, (lane & 15) * 1280 + (lane >> 4) * 8 + t * 32, obase, 0);
    SB();
  }
  if (wt.preps != 0) { if (!wt.has(S_H)) { if (w == 7) wt.poll(false); WAIT_V(0); wt.publish(); STAMP(13); } }
  else WAIT_V(15);
  lds_inc(lf + LF_DMA, lane);
  wt.need(S_H);
  STAMP(6);
  if (w < 4) {
    __builtin_amdgcn_s_setprio(2);
    p2_steps<false>(lds, rws, wt, br, acc, xr, mt2, j, w, lane);
    __builtin_amdgcn_s_setprio(0);
#pragma unroll
    for (int q = 0; q < 15; ++q) asm volatile("" : "=v"(br[q / 5][q % 5]));
  }
  PINS(w);
  if (w >= 4) { __builtin_amdgcn_s_setprio(1); p2_steps<true>(lds, rws, wt, br, acc, xr, mt2, j, w, lane); }
  asm volatile("s_nop 7" ::: "memory");
  SB();
  STAMP(14);
  LAS char* ex = lds + (w >> 2) * 20480 + lane * 16;
#pragma unroll
  for (int t = 0; t < 5; ++t) *(LAS f32x4*)(ex + 2 * 40960 + (a * 5 + t) * 1024) = acc[3][t];
  WAIT_L0();
  if (w < 4) { lds_inc(lf + LF_X, lane); if (w == 3) wt.need(S_Q | S_M, 3); lds_wait(lf + LF_X, 4u); }
  else BAR();
  STAMP(8);
#pragma unroll
  for (int t = 0; t < 5; ++t)
#pragma unroll
    for (int q = 1; q < 4; ++q) acc[0][t] += *(LAS f32x4*)(ex + (q - 1) * 40960 + (((a - q) & 3) * 5 + t) * 1024);
  LAS char* base = lds + L2_BASE + a * 5120 + lane * 16;
  if (w < 4) {
#pragma unroll
    for (int t = 0; t < 5; ++t) *(LAS f32x4*)(base + t * 1024) = acc[0][t];
    WAIT_L0();
  } else {
    const rsrc_t ro = __builtin_amdgcn_make_buffer_rsrc(out, 0, 2048 * 1280, 0x00020000);
    u32x2 o[5];
#pragma unroll
    for (int t = 0; t < 5; ++t) {
      const f32x4 s = acc[0][t] + *(LAS f32x4*)(base + t * 1024);
      o[t] = u32x2{pk2(lo2(s)), pk2(hi2(s))};
    }
    const unsigned orow = obase + (lane & 15) * 1280;
#pragma unroll
    for (int t = 0; t < 4; t += 2) {
      const auto s0 = __builtin_amdgcn_permlane16_swap(o[t][0], o[t + 1][0], false, false);
      const auto s1 = __builtin_amdgcn_permlane16_swap(o[t][1], o[t + 1][1], false, false);
      __builtin_amdgcn_raw_buffer_store_b128(u32x4{s0[0], s1[0], s0[1], s1[1]}, ro, orow + ((lane >> 4) & 1) * 32 + (lane >> 5) * 16 + t * 32, 0, OUT_AUX);
    }
    __builtin_amdgcn_raw_buffer_store_b64(o[4], ro, orow + (lane >> 4) * 8 + 128, 0, OUT_AUX);
  }
}

__global__ __launch_bounds__(512, 2) void fused(const char* x, const char* wqkv, const char* wo, const char* wg,
                                                const char* wu, const char* wd, char* ws, char* out) {
  extern __shared__ __attribute__((aligned(16))) char smem[];
  const char* pc0;
  asm volatile("s_getpc_b64 %0" : "=s"(pc0));
  asm volatile("" : "+s"(x), "+s"(wqkv), "+s"(wo), "+s"(wg), "+s"(wu), "+s"(wd), "+s"(ws), "+s"(out));
  LAS char* lds = (LAS char*)smem;
  const unsigned ldsb = (unsigned)(size_t)lds;
  const int bid = blockIdx.x, xcd = bid & 7, idx = bid >> 3;
  const int Mt = (xcd & 1) * 8 + (idx & 7), grp = xcd >> 1, pos4 = idx >> 3, slot2 = grp * 4 + pos4;
  const int cslot = (int)(0xCF76BE54AD329810ull >> (4 * (pos4 == 2 ? (slot2 + 4 * (idx >> SPR)) & 15 : slot2))) & 15;
  const int w = __builtin_amdgcn_readfirstlane(threadIdx.x >> 6), lane = threadIdx.x & 63;
  STAMP(0);
  const rsrc_t rws = __builtin_amdgcn_make_buffer_rsrc(ws, 0, WS_END, 0x00020000);
  u64 tag;
  u32x2 epoch = {0u, 0u};
  ld8_hidden(epoch, srd(ws, WS_END), OFF_E);
  Watch wt{(LAS unsigned*)(lds + L2_FLAG), ws, 0ull, Mt * 2 + (slot2 >> 3), slot2 & 7, w, lane, 0u, 0, 0, 0, 0u};
  u32x4 br[3][5];
  PF2 pf{ws, rws, ldsb, slot2 & 7, w, lane, Mt % 3, br};
  if (cslot == 15) {
    if (threadIdx.x < 16) ((LAS unsigned*)(lds + L2_FLAG))[threadIdx.x] = 0u;
    retile(wd, rws, ldsb + w * 16384, Mt, w, lane);
    WAIT_V(0);
    PIN(epoch);
    wt.tag = tag = make_tag(epoch);
    WAIT_L0(); BAR();
    if (threadIdx.x == 0) { set_flag(ws, OFF_FR + Mt * 8, tag); set_flag(ws, OFF_F1 + (Mt * 16 + 15) * 8, tag); }
    STAMP(5);
    u32x4 wf[2][5];
    {
      const u32x4 ro = srd(wo, kD * 1280);
#pragma unroll 1
      for (int q = 0; q < 10; ++q)
        dma<0>(ro, (lane >> 3) * 1280 + (lane & 7) * 16, (Mt * 40 + (q >> 1) * 8) * 1280 + ((w * 160) & ~127) + (q & 1) * 128, ldsb + w * 16384 + q * 1024);
      WAIT_V(0);
#pragma unroll
      for (int hh = 0; hh < 2; ++hh) {
        unsigned ra[2];
#pragma unroll
        for (int h2 = 0; h2 < 2; ++h2) {
          int row = hh * 20 + (lane >> 4) * 8 + h2 * 4 + ((lane & 15) >> 2);
          row = row < 40 ? row : 39;
          ra[h2] = ldsb + w * 16384 + (row >> 3) * 2048 + (row & 7) * 128 + (lane & 3) * 8;
        }
#pragma unroll
        for (int i = 0; i < 5; ++i) {
          const int cb = (w & 3) * 32 + i * 32, o = (cb >> 7) * 1024 + (cb & 127);
          const u32x2 lo = __builtin_bit_cast(u32x2, __builtin_amdgcn_ds_read_tr16_b64_v4i16((LAS s16x4*)(size_t)(ra[0] + o)));
          const u32x2 hi = __builtin_bit_cast(u32x2, __builtin_amdgcn_ds_read_tr16_b64_v4i16((LAS s16x4*)(size_t)(ra[1] + o)));
          wf[hh][i] = u32x4{lo[0], lo[1], hi[0], hi[1]};
        }
      }
    }
    wait_flags(ws, OFF_FR + (lane & 15) * 8, tag);
    WAIT_L0(); BAR();
    wd_prefetch<0, 6>(srd(ws, WS_END), ldsb, slot2 & 7, w, lane);
    pf.items<0, 28>();
    if (Mt == 0 && w == 7) {
#pragma unroll 1
      for (int q = 0; q < 4; ++q) wait_flags(ws, OFF_F1 + (q * 64 + lane) * 8, tag);
      if (lane == 0) set_flag(ws, OFF_E, tag);
    }
    fold_kv(lds, ws, rws, wt, wf, Mt, w, lane);
  } else {
    const int type = cslot < 9 ? 0 : cslot < 13 ? 1 : 2;
    P1 c;
    c.w = w; c.mw = w >> 2; c.nw = w & 3; c.Mt = Mt; c.cslot = cslot; c.j = slot2 & 7;
    c.ldsb = ldsb;
    {
#define ROTM 1
      const int kb = (Mt + cslot) % 10, rot = ROTM == 0 ? (kb >= 5 ? 5 : 0) : ROTM == 1 ? (kb > 7 ? 7 : kb) : (kb / 3) * 3 - (kb == 9 ? 3 : 0);
      c.bstar = kb - rot;
      const int seg = w >> 2;
      c.pitch = type == 0 ? kF * 2 : kQKV * 2;
      const int startb = type == 0 ? cslot * 384
                       : ((type == 1 ? (640 * (1 + seg) + 160 * (cslot - 9)) * 2 : 320 * (2 * (cslot - 13) + seg)) & ~127);
      const char* wb = type == 0 ? (seg ? wu : wg) : wqkv;
      c.rw = srd(wb + startb - 2048, 640 * c.pitch - startb + 2048);
      c.rx = srd(x + (size_t)Mt * 128 * 1280, 128 * 1280);
      const int i = lane >> 3, cp = lane & 7, pg = w & 3;
      c.voffB = (unsigned)(8 * pg + i) * c.pitch + 16 * (cp ^ ((i & 2) | ((pg & 1) << 2))) + 2048;
      c.voffA = (unsigned)(8 * w + i) * 1280 + 16 * (cp ^ ((4 * w + (i >> 1)) & 7));
      c.oB = rot * 64 * c.pitch; c.oA = rot * 128;
      dma<0>(c.rx, c.voffA, c.oA, c.ldsb + L1_A + w * 1024);
    }
    auto pro = [&](int s) {
      const unsigned lB = c.ldsb + L1_B + w * 3072, lA = c.ldsb + L1_A + w * 1024;
      dma<0>(c.rw, c.voffB, c.oB, lB + s * L1_BST);
      dma<128>(c.rw, c.voffB, c.oB, lB + s * L1_BST + 1024);
      dma<256>(c.rw, c.voffB, c.oB, lB + s * L1_BST + 2048);
      c.oB += 32 * c.pitch;
      dma<0>(c.rx, c.voffA, c.oA + ((s + 1) >> 1) * 128 + ((s + 1) & 1) * 81920, lA + (s + 1) * 8192);
      SB();
    };
#define PSPLIT 1
#pragma unroll
    for (int s = 0; s < PSPLIT; ++s) pro(s);
    unsigned vb[5], va0;
    {
      const int g = lane >> 4, q = (lane & 15) >> 2, p = lane & 3, r = lane & 15, nw = c.nw;
      const int swz = (q & 2) | ((g & 1) << 2);
#pragma unroll
      for (int pos = 0; pos < 5; ++pos) {
        const int cf = 16 * nw + 4 * p;
        const int hb = (cslot & 1 ? 0 : 32) + 40 * nw;
        const int ck = pos < 4 ? (pos >> 1) * 192 + hb + 20 * (pos & 1) + 4 * p : (p >> 1) * 192 + hb + 20 * (p & 1) + 16;
        const int cq = (nw >> 1) * 224 + 20 * (4 * (nw & 1) + 2 * (p & 1) + (p >> 1)) + 4 * pos;
        const int col = type == 0 ? cf : type == 1 ? ck : cq;
        const int line = col >> 6, seg = line >= 3;
        vb[pos] = c.ldsb + L1_B + seg * (12288 - 3072) + (g * 3 + line) * 1024 + q * 128 + 16 * (((col & 63) >> 3) ^ swz) + 8 * ((col >> 2) & 1);
      }
      va0 = c.ldsb + L1_A + (c.mw * 64 + r) * 128 + 16 * (g ^ ((r >> 1) & 7));
    }
    if (threadIdx.x < 16) ((LAS unsigned*)(lds + L2_FLAG))[threadIdx.x] = 0u;
    f32x4 acc[4][6];
#pragma unroll
    for (int m = 0; m < 4; ++m)
#pragma unroll
      for (int i = 0; i < 6; ++i) acc[m][i] = f32x4{0.f, 0.f, 0.f, 0.f};
    unsigned foff = lane >= 20 ? OFF_FR + (lane & 15) * 8 : OFF_F1 + (lane < 10 ? lane * 16 + cslot : Mt * 16 + lane - 10) * 8;
    PIN(foff);
#pragma unroll
    for (int i = 0; i < 5; ++i) PIN(vb[i]);
    SB();
#pragma unroll
    for (int s = PSPLIT; s < 3; ++s) pro(s);
    c.oA += 256;
    if (w == 7 && idx < 8) warm_code(pc0, ldsb + L1_GR + 8192, idx, lane);
    SB();
    WAIT_V(8);
    PIN(epoch);
    wt.tag = tag = make_tag(epoch);
#define P1_STAMP
#define P1_RUN(T) do { if (w < 4) p1_loop<T, true>(c, vb, va0, foff, acc, pf, ws, rws, tag, lane); \
                       else p1_loop<T, false>(c, vb, va0, foff, acc, pf, ws, rws, tag, lane); P1_STAMP; } while (0)
    if (type == 0) { P1_RUN(0); epi_ffn(lds, acc, ws, rws, wt, c, pf, lane); }
    else if (type == 2) { P1_RUN(2); epi_q(lds, acc, ws, rws, wt, c, pf, lane); }
    else {
      P1_RUN(1);
      epi_kv(lds, acc, ws, rws, tag, c, lane);
      STAMP(5);
      wd_prefetch<0, 6>(srd(ws, WS_END), ldsb, c.j, w, lane);
      pf.items<0, 28>();
    }
  }
  phase2(lds, x, ws, rws, out, wt, br, Mt * 2 + (slot2 >> 3), slot2 & 7, w, lane);
  STAMP(10);
}

extern "C" void kernel_launch(void* const* d_in, const int* in_sizes, int n_in, void* d_out, int out_size,
                              void* d_ws, size_t ws_size, hipStream_t stream) {
  (void)in_sizes; (void)n_in; (void)out_size; (void)ws_size;
  (void)hipFuncSetAttribute((const void*)fused, hipFuncAttributeMaxDynamicSharedMemorySize, LDS_SIZE);
  fused<<<256, 512, LDS_SIZE, stream>>>((const char*)d_in[0], (const char*)d_in[1], (const char*)d_in[2], (const char*)d_in[4],
                                        (const char*)d_in[5], (const char*)d_in[6], (char*)d_ws, (char*)d_out);
}
```

```cpp
#include <hip/hip_runtime.h>
#include <stdint.h>
#include <type_traits>

#define GAS __attribute__((address_space(1)))
#define LAS __attribute__((address_space(3)))
typedef __attribute__((ext_vector_type(2))) __bf16 bf16x2;
typedef __attribute__((ext_vector_type(8))) __bf16 bf16x8;
typedef __attribute__((ext_vector_type(4))) short s16x4;
typedef __attribute__((ext_vector_type(4))) float f32x4;
typedef __attribute__((ext_vector_type(2))) float f32x2;
typedef __attribute__((ext_vector_type(4))) unsigned u32x4;
typedef __attribute__((ext_vector_type(2))) unsigned u32x2;
typedef unsigned long long u64;
typedef __amdgpu_buffer_rsrc_t rsrc_t;

#define DEVI __device__ __forceinline__
#define WAIT_V_(n) asm volatile("s_waitcnt vmcnt(" #n ")" ::: "memory")
#define WAIT_V(n) WAIT_V_(n)
template <int N> __device__ __forceinline__ void wait_v() { asm volatile("s_waitcnt vmcnt(%0)" :: "i"(N) : "memory"); }
#define WAIT_L0() do { __builtin_amdgcn_s_waitcnt(0xC07F); asm volatile("" ::: "memory"); } while (0)
#define SB() __builtin_amdgcn_sched_barrier(0)
#define PIN(x) asm volatile("" : "+v"(x))
#define PINS(x) asm volatile("" : "+s"(x))
#define BAR() do { asm volatile("" ::: "memory"); __builtin_amdgcn_s_barrier(); asm volatile("" ::: "memory"); } while (0)
#define RLX_AGENT __ATOMIC_RELAXED, __HIP_MEMORY_SCOPE_AGENT
#define RLX_WG __ATOMIC_RELAXED, __HIP_MEMORY_SCOPE_WORKGROUP
#define SC1 16
#define OUT_AUX 0

#define STAMP(i)

constexpr int kD = 640, kF = 1728, kQKV = 1920;
constexpr unsigned OFF_E   = 0;
constexpr unsigned OFF_F1  = 256;
constexpr unsigned OFF_FR  = OFF_F1 + 2048;
constexpr unsigned OFF_FK  = 2560;
constexpr unsigned OFF_F3  = OFF_FK + 4096;
constexpr unsigned OFF_FH  = OFF_F3 + 1024;
constexpr unsigned OFF_FZ  = 16384;
constexpr unsigned OFF_ZP  = OFF_FZ + 4096;
constexpr unsigned OFF_SC  = OFF_ZP + 4 * 32 * 640;
constexpr unsigned OFF_SR  = OFF_SC + 15 * 10 * 384 * 4;
constexpr unsigned OFF_KVP = OFF_SR + 16 * 10 * 256 * 4;
constexpr unsigned OFF_MP  = OFF_KVP + 16 * 16 * 3456;
constexpr unsigned OFF_WD  = OFF_MP + 8 * 20 * 5 * 1024;
constexpr unsigned OFF_Q   = OFF_WD + 8 * 54 * 5 * 1024;
constexpr unsigned OFF_H   = OFF_Q + 32 * 20 * 4096;
constexpr unsigned WS_END  = OFF_H + 32 * 54 * 4096;
constexpr unsigned OOB = 0x80000000u;
static_assert(OFF_FH + 8192 <= OFF_FZ && OFF_MP % 1024 == 0 && OFF_KVP % 128 == 0, "align");

constexpr int L1_B = 0, L1_BST = 24576;
constexpr int L1_A = 4 * L1_BST, L1_AST = 16384;
constexpr int L1_GR = L1_A + 3 * L1_AST;
constexpr int L1_GC = L1_A + L1_AST;
constexpr int L1_ST = 158720;
constexpr int L1_CS = L1_ST + 1024;
constexpr int L1_EX = 72 * 1024;
constexpr int L2_BASE = 120 * 1024;
constexpr int L2_RED = 150 * 1024;
constexpr int L2_FLAG = 162816;
constexpr int LDS_SIZE = L2_FLAG + 64;
enum { LF_STATUS = 0, LF_DMA = 1, LF_K = 2, LF_X = 3, LF_BC = 4, LF_PUB = 8 };
enum { S_H = 1, S_Q = 2, S_M = 4 };

DEVI f32x4 mfma(u32x4 a, u32x4 b, f32x4 c) {
  return __builtin_amdgcn_mfma_f32_16x16x32_bf16(__builtin_bit_cast(bf16x8, a), __builtin_bit_cast(bf16x8, b), c, 0, 0, 0);
}
DEVI void mfma_acc(f32x4& c, u32x4 a, u32x4 b) { asm volatile("v_mfma_f32_16x16x32_bf16 %0, %1, %2, %0" : "+v"(c) : "v"(a), "v"(b)); }
DEVI void mfma_new(f32x4& c, u32x4 a, u32x4 b) { asm volatile("v_mfma_f32_16x16x32_bf16 %0, %1, %2, 0" : "=&v"(c) : "v"(a), "v"(b)); }
DEVI unsigned pk2(f32x2 f) { return __builtin_bit_cast(unsigned, __builtin_convertvector(f, bf16x2)); }
DEVI unsigned pk2(float lo, float hi) { return pk2(f32x2{lo, hi}); }
DEVI f32x2 unpk(unsigned u) { return f32x2{__builtin_bit_cast(float, u << 16), __builtin_bit_cast(float, u & 0xffff0000u)}; }
DEVI f32x2 fma2(f32x2 a, f32x2 b, f32x2 c) { return __builtin_elementwise_fma(a, b, c); }
DEVI f32x2 bc2(float v) { return f32x2{v, v}; }
DEVI f32x2 lo2(f32x4 v) { return f32x2{v[0], v[1]}; }
DEVI f32x2 hi2(f32x4 v) { return f32x2{v[2], v[3]}; }
DEVI f32x2 phi2(f32x2 g, f32x2 rs) {
  const f32x2 t = fma2(g, rs, bc2(1e-6f)), l = g * (rs * 1.4426950408889634f);
  return f32x2{fmaxf(t[0], 1e-6f), fmaxf(t[1], 1e-6f)} +
         f32x2{__builtin_amdgcn_fmed3f(__builtin_amdgcn_exp2f(l[0]), 0.f, 1.f), __builtin_amdgcn_fmed3f(__builtin_amdgcn_exp2f(l[1]), 0.f, 1.f)};
}
DEVI u32x4 lds_b128(unsigned a) { return *(LAS u32x4*)(size_t)a; }
DEVI u32x4 lds_trfrag(unsigned a) {
  const u32x2 lo = __builtin_bit_cast(u32x2, __builtin_amdgcn_ds_read_tr16_b64_v4i16((LAS s16x4*)(size_t)a));
  const u32x2 hi = __builtin_bit_cast(u32x2, __builtin_amdgcn_ds_read_tr16_b64_v4i16((LAS s16x4*)(size_t)(a + 512)));
  return u32x4{lo[0], lo[1], hi[0], hi[1]};
}
DEVI u32x4 srd(const void* p, unsigned bytes) {
  const u64 a = (u64)p;
  return u32x4{(unsigned)a, (unsigned)(a >> 32) & 0xffffu, bytes, 0x00020000u};
}
DEVI void setm0(unsigned v) { asm volatile("s_mov_b32 m0, %0" :: "s"(v) : "memory"); }
template <int IMM>
DEVI void dma_n(u32x4 r, unsigned voff, unsigned soff) {
  asm volatile("buffer_load_dwordx4 %0, %1, %2 offen offset:%3 lds" :: "v"(voff), "s"(r), "s"(soff), "i"(IMM) : "memory");
}
template <int IMM>
DEVI void dma(u32x4 r, unsigned voff, unsigned soff, unsigned lds) {
  asm volatile("s_mov_b32 m0, %4\n\ts_nop 0\n\tbuffer_load_dwordx4 %0, %1, %2 offen offset:%3 lds"
               :: "v"(voff), "s"(r), "s"(soff), "i"(IMM), "s"(lds - IMM) : "memory");
}
DEVI void dma_sc1(u32x4 r, unsigned voff, unsigned soff, unsigned lds) {
  asm volatile("s_mov_b32 m0, %3\n\ts_nop 0\n\tbuffer_load_dwordx4 %0, %1, %2 offen sc1 lds"
               :: "v"(voff), "s"(r), "s"(soff), "s"(lds) : "memory");
}
DEVI void ld8_hidden(u32x2& v, u32x4 r, unsigned voff) {
  asm volatile("buffer_load_dwordx2 %0, %1, %2, 0 offen sc1" : "+v"(v) : "v"(voff), "s"(r) : "memory");
}
template <int N0, int N1>
DEVI void wd_prefetch(u32x4 rws, unsigned ldsb, int j, int w, int lane) {
#pragma unroll
  for (int n = N0; n < N1; ++n) {
    const unsigned a = (unsigned)(w + 8 * n) * 1024;
    if (n < 18 || w < 6) dma_sc1(rws, lane * 16, OFF_WD + j * 276480 + a, ldsb + a);
  }
}
DEVI void wait_flags(char* ws, unsigned off, u64 tag) {
#pragma unroll 1
  for (unsigned spins = 0; spins < (1u << 20); ++spins) {
    const u64 v = __hip_atomic_load((GAS u64*)(ws + off), RLX_AGENT);
    if (__all(v == tag)) break;
    __builtin_amdgcn_s_sleep(1);
  }
  asm volatile("" ::: "memory");
}
DEVI u64 make_tag(u32x2 e) {
  const u64 v = (u64)(unsigned)__builtin_amdgcn_readfirstlane(e[0]) | (u64)(unsigned)__builtin_amdgcn_readfirstlane(e[1]) << 32;
  return v * 0x9E3779B97F4A7C15ull + 0xD1B54A32D192ED03ull;
}
DEVI void set_flag(char* ws, unsigned off, u64 tag) { __hip_atomic_store((GAS u64*)(ws + off), tag, RLX_AGENT); }
DEVI unsigned lds_inc(LAS unsigned* p, int lane) {
  unsigned o = 0;
  if (lane == 0) o = __hip_atomic_fetch_add(p, 1u, RLX_WG);
  return __builtin_amdgcn_readfirstlane(o);
}
DEVI void lds_wait_bits(LAS unsigned* p, unsigned bits) {
  while ((~(unsigned)__builtin_amdgcn_readfirstlane(__hip_atomic_load(p, RLX_WG)) & bits) != 0u) __builtin_amdgcn_s_sleep(1);
  asm volatile("" ::: "memory");
}
DEVI void lds_wait(LAS unsigned* p, unsigned want) {
  while ((unsigned)__builtin_amdgcn_readfirstlane(__hip_atomic_load(p, RLX_WG)) < want) __builtin_amdgcn_s_sleep(1);
  asm volatile("" ::: "memory");
}
DEVI u32x4 ld16(rsrc_t r, unsigned voff, unsigned soff) { return __builtin_amdgcn_raw_buffer_load_b128(r, voff, soff, SC1); }
DEVI void st16(rsrc_t r, unsigned voff, unsigned soff, u32x4 v) { __builtin_amdgcn_raw_buffer_store_b128(v, r, voff + soff, 0, SC1); }
DEVI void st8(rsrc_t r, unsigned voff, unsigned soff, u32x2 v) { __builtin_amdgcn_raw_buffer_store_b64(v, r, voff, soff, SC1); }

constexpr unsigned CODE_BYTES = 60 * 1024;
DEVI void warm_code(const char* pc0, unsigned junk, int chunk, int lane) {
  asm volatile("s_mov_b32 m0, %3\n\ts_nop 0\n\tbuffer_load_dword %0, %1, %2 offen lds"
               :: "v"(lane << 7), "s"(srd(pc0, CODE_BYTES)), "s"(chunk * 8192), "s"(junk) : "memory");
}

struct P1 {
  u32x4 rw, rx;
  unsigned ldsb, pitch;
  unsigned voffB, voffA;
  unsigned oB, oA;
  int w, mw, nw, bstar;
  int Mt, cslot, j;
};

struct PF2 {
  char* ws; rsrc_t rws; unsigned ldsb; int j, w, lane, r0;
  u32x4 (&br)[3][5];
  template <int N> DEVI void item() {
    if constexpr (N < 13) wd_prefetch<6 + N, 7 + N>(srd(ws, WS_END), ldsb, j, w, lane);
    else br[(N - 13) / 5][(N - 13) % 5] = ld16(rws, lane * 16, OFF_WD + (j * 54 + 30 + w + 8 * (((N - 13) / 5 + r0) % 3)) * 5120 + ((N - 13) % 5) * 1024);
    SB();
  }
  template <int N0, int N1> DEVI void items() { if constexpr (N0 < N1) { item<N0>(); items<N0 + 1, N1>(); } }
};

#define SPR 1
#define EB 0
#define AINC 1
template <int TYPE, bool LATE>
DEVI void p1_loop(P1& c, unsigned (&vb)[5], unsigned va0, unsigned foff, f32x4 (&acc)[4][6], PF2& pf, char* ws, rsrc_t rws, u64 tag, int lane) {
  constexpr int NT = TYPE == 0 ? 6 : 5;
  constexpr bool AC = !LATE || AINC;
  u32x4 fa[4], fb[NT];
  unsigned va1 = va0 ^ 64;
  const unsigned stepB = 32 * c.pitch, limB = 640 * c.pitch;
  const unsigned lB0 = c.ldsb + L1_B + c.w * 3072, lA0 = c.ldsb + L1_A + c.w * 1024;
  unsigned lB = lB0 + 3 * L1_BST, lA = lA0 + 2 * L1_AST;
  unsigned vB1 = c.voffB - 896, vB2 = c.voffB - 1792, vA1 = c.voffA + 81920;
  PIN(vB1); PIN(vB2); PIN(vA1);
  int dA = L1_AST, dB = 2 * L1_BST, rare = c.bstar, rare2 = 1;
  f32x4 st[5];
  auto adB = [&](int i) { return TYPE == 0 ? vb[!LATE && i >= 2 ? 1 : 0] + (i / 3) * 12288 + (i % 3) * 1024 : vb[i < 5 ? i : 4]; };
  if (TYPE == 0) { vb[1] = vb[0]; PIN(vb[1]); }
  auto upB = [&](int i0, int i1) {
    if (TYPE == 0) { vb[i0 ? 1 : 0] += dB; PIN(vb[i0 ? 1 : 0]); }
    else {
#pragma unroll
      for (int i = i0; i < i1; ++i) { vb[i] += dB; PIN(vb[i]); }
    }
  };
  auto rdA = [&](int i, int sub) { fa[i] = lds_b128((sub ? va1 : va0) + i * 2048); };
  auto rdB = [&](int i, int sub) { fb[i] = lds_trfrag(adB(i) + sub * L1_BST); };
  auto rdBh = [&](int i, int hh, int sub) {
    const u32x2 v = __builtin_bit_cast(u32x2, __builtin_amdgcn_ds_read_tr16_b64_v4i16((LAS s16x4*)(size_t)(adB(i) + sub * L1_BST + hh * 512)));
    fb[i][2 * hh] = v[0]; fb[i][2 * hh + 1] = v[1];
  };
  auto pieceA = [&](int sub) { dma_n<0>(c.rx, sub ? vA1 : c.voffA, c.oA); };
  auto L = [&](int sub, int mode) {
    if (mode) dma_n<0>(c.rw, c.voffB, c.oB);
    SB(); rdA(0, sub); rdA(1, sub); SB();
    if (mode) dma_n<1024>(c.rw, vB1, c.oB);
    SB(); rdA(2, sub); rdA(3, sub); SB();
    if (mode) dma_n<2048>(c.rw, vB2, c.oB);
    SB(); rdB(LATE ? 4 : 0, sub); SB();
    if (mode == 2) setm0(lA + sub * 8192);
    SB(); if (!LATE) rdB(1, sub); else if (NT == 6) rdB(NT - 1, sub); else if (mode == 2) asm volatile("s_nop 0"); SB();
    if (mode == 2) pieceA(sub);
    if (!LATE && !sub) upB(2, 5);
    SB();
  };
  auto U = [&](int sub) {
    if (sub) { va1 += dA; PIN(va1); upB(0, LATE ? 5 : 2); }
    else { va0 += dA; PIN(va0); }
    SB();
  };
  auto stats = [&](int sub) {
    const u32x4 one = {0x3f803f80u, 0x3f803f80u, 0x3f803f80u, 0x3f803f80u};
    const u32x4 sa = lds_b128((sub ? va1 : va0) + c.nw * 2048);
    u32x4 sb[3];
#pragma unroll
    for (int i = 0; i < 3; ++i) { unsigned a = adB(LATE ? i : 3 + i); PIN(a); sb[i] = lds_trfrag(a + sub * L1_BST); }
    SB();
    if (sub == 0) {
#pragma unroll
      for (int i = 0; i < 5; ++i) st[i] = f32x4{0.f, 0.f, 0.f, 0.f};
    }
    st[0] = mfma(one, sa, st[0]);
    st[1] = mfma(sa, sa, st[1]);
#pragma unroll
    for (int i = 0; i < 3; ++i) st[2 + i] = mfma(one, sb[i], st[2 + i]);
    if (sub == 1) {
      const int cl = lane & 15, g = lane >> 4;
      const float sq = (cl & 2) ? ((cl & 1) ? st[1][3] : st[1][2]) : ((cl & 1) ? st[1][1] : st[1][0]);
      st8(rws, (cl >> 2) == g ? (c.mw * 64 + c.nw * 16 + cl) * 8 : OOB, c.cslot < 10 ? OFF_SR + (c.Mt * 10 + c.cslot) * 1024 : WS_END,
          u32x2{__builtin_bit_cast(unsigned, st[0][0]), __builtin_bit_cast(unsigned, sq)});
      const float v = g == 0 ? st[2][0] : g == 1 ? st[3][0] : st[4][0];
      __builtin_amdgcn_raw_buffer_store_b32(__builtin_bit_cast(unsigned, v), rws, g < 3 ? ((c.nw * 6 + c.mw * 3) * 16 + lane) * 4 : OOB,
                                            c.Mt < 10 ? OFF_SC + (c.cslot * 10 + c.Mt) * 1536 : WS_END, SC1);
    }
    SB();
  };
#define SEG(i)
  auto C = [&](int sub, bool more, int book, auto vmwait) {
    SEG(0);
    __builtin_amdgcn_s_setprio(1);
    if (LATE) { vmwait(); WAIT_L0(); BAR(); }
    SB();
#pragma unroll
    for (int k = 0; k < 4 * NT; ++k) {
      const int i = k >> 2, m = k & 3, ms = (i & 1) ? 3 - m : m;
      acc[ms][i] = TYPE == 1 ? mfma(fa[ms], fb[i], acc[ms][i]) : mfma(fb[i], fa[ms], acc[ms][i]);
      if (LATE) { if (more && i >= 1 && i < 5 && m < 2) rdBh(i - 1, m, sub ^ 1); }
      else if (k < 2 * (NT - 2)) rdBh(2 + (k >> 1), k & 1, sub);
      const int q = LATE ? (k < 2 ? k : m >= 2 && i < 5 ? 2 * i + m : -1) : k - 2 * (NT - 2);
      constexpr int QA = LATE ? 8 : 0, QU = LATE ? 0 : 3;
      if (book == 1) {
        if (AC) {
          if (q == QA) setm0(lA + (LATE ? sub ^ 1 : sub) * 8192);
          if (q == QA + 1) pieceA(LATE ? sub ^ 1 : sub);
        }
        if (q == QU) { c.oB += stepB; lB += L1_BST; PINS(c.oB); PINS(lB); }
        if (q == QU + 1) { c.oB = c.oB == limB ? 0u : c.oB; PINS(c.oB); }
        if (q == QU + 2) { lB = lB == lB0 + 4 * L1_BST ? lB0 : lB; PINS(lB); }
        if (sub) {
          if (q == QU + 3) { c.oA += 128; lA += L1_AST; PINS(c.oA); PINS(lA); }
          if (q == QU + 4) { c.oA = c.oA == 1280u ? 0u : c.oA; PINS(c.oA); }
          if (q == QU + 5) { lA = lA == lA0 + 3 * L1_AST ? lA0 : lA; PINS(lA); }
          if (q == QU + 6) { dA = lA == lA0 + L1_AST ? -2 * L1_AST : L1_AST; PINS(dA); }
          if (q == QU + 7) { rare -= 1; PINS(rare); }
        }
        if (!sub && q == QU + 3) { rare2 = LATE ? (rare + 2) & ~2 : rare; PINS(rare2); }
        if (q == 11) setm0(lB);
      }
      if (book && q == 10 && (LATE ? sub : !sub)) { dB = -dB; PINS(dB); }
      if (!LATE && EB > 0) {
        if (k == 4 * NT - 2 - EB) { vmwait(); WAIT_L0(); }
        if (k == 4 * NT - 1 - EB) { __builtin_amdgcn_s_setprio(2); BAR(); }
      }
      SB();
    }
    __builtin_amdgcn_s_setprio(0);
    if (!LATE && EB == 0) { vmwait(); WAIT_L0(); BAR(); }
  };
  constexpr int LM = AC ? 1 : 2;
  if (!LATE) { upB(2, 5); dB = -dB; }
  BAR();
  if (LATE) {
    if (AC) { setm0(lA); SB(); }
    rdB(0, 0); rdB(1, 0); SB();
    if (AC) pieceA(0);
  }
  setm0(lB);
  SB();
  if (LATE) { rdB(2, 0); rdB(3, 0); } else asm volatile("s_nop 0");
  SB();
  constexpr int X = TYPE == 0;
  const int rend = c.bstar - (X ? 7 : 8);
  const u32x4 rw4 = srd(ws, WS_END);
  u32x2 fl = {0u, 0u};
#pragma unroll 1
  do {
    if (X && __builtin_expect(rare == rend + 1, 0)) ld8_hidden(fl, rw4, foff);
    L(0, LM);
    if (__builtin_expect(rare == 0, 0)) stats(0);
    U(0);
    C(0, true, 1, []() { WAIT_V(8); });
    L(1, LM);
    if (__builtin_expect(rare2 == 0, 0)) {
      PINS(rare);
      if (rare == 0) stats(1);
      else if (LATE && c.w == 0 && lane == 0) set_flag(ws, OFF_F1 + (c.Mt * 16 + c.cslot) * 8, tag);
    }
    U(1);
    C(1, true, 1, []() { WAIT_V(8); });
  } while (rare != rend);
  auto check = [&]() {
    PIN(fl);
    if (__builtin_expect(!__all(__builtin_bit_cast(u64, fl) == tag), 0)) wait_flags(ws, foff, tag);
  };
  if (X) {
    L(0, LM);
    WAIT_V(11);
    check();
    pf.items<13, 16>();
    U(0);
    C(0, true, 1, []() { WAIT_V(11); });
    L(1, LM);
    pf.items<16, 19>();
    U(1);
    C(1, true, 1, []() { WAIT_V(14); });
  }
  if (!X) ld8_hidden(fl, rw4, foff);
  L(0, 1);
  if (X) pf.items<19, 22>();
  U(0);
  C(0, true, 2, []() { wait_v<X ? 13 : 8>(); });
  L(1, 0);
  wait_v<X ? 6 : 3>();
  if (!X) check();
  dma_sc1(rw4, lane * 16, OFF_SC + c.cslot * 15360 + c.w * 1024, c.ldsb + L1_GC + c.w * 1024);
  dma_sc1(rw4, lane * 16, OFF_SC + c.cslot * 15360 + c.w * 1024 + 8192, c.ldsb + L1_GC + c.w * 1024 + 8192);
  dma_sc1(rw4, lane * 16, OFF_SR + c.Mt * 10240 + c.w * 1024, c.ldsb + L1_GR + c.w * 1024);
  if (LATE && c.w < 2) dma_sc1(rw4, lane * 16, OFF_SR + c.Mt * 10240 + c.w * 1024 + 8192, c.ldsb + L1_GR + c.w * 1024 + 8192);
  if (X) pf.items<22, 24>();
  U(1);
  C(1, true, 2, []() {});
  L(0, 0);
  if (TYPE != 1) wd_prefetch<0, 4>(rw4, c.ldsb, c.j, c.w, lane);
  if (X) pf.items<24, 26>();
  C(0, true, 0, []() { wait_v<X ? 8 : TYPE != 1 ? 4 : 0>(); });
  L(1, 0);
  {
    __builtin_amdgcn_s_setprio(2);
    if (LATE || c.w < 6) {
      const LAS float* src = (const LAS float*)(size_t)(c.ldsb + L1_GC + (c.w * 64 + lane) * 4);
      float v[10];
#pragma unroll
      for (int p = 0; p < 10; ++p) v[p] = src[p * 384];
      *(LAS float*)(size_t)(c.ldsb + L1_CS + (c.w * 64 + lane) * 4) = (((v[0] + v[1]) + (v[2] + v[3])) + ((v[4] + v[5]) + (v[6] + v[7]))) + (v[8] + v[9]);
    } else {
      const LAS f32x2* src = (const LAS f32x2*)(size_t)(c.ldsb + L1_GR + ((c.w - 6) * 64 + lane) * 8);
      f32x2 v[10];
#pragma unroll
      for (int p = 0; p < 10; ++p) v[p] = src[p * 128];
      const f32x2 s = ((((v[0] + v[1]) + (v[2] + v[3])) + ((v[4] + v[5]) + (v[6] + v[7]))) + (v[8] + v[9])) * (1.0f / kD);
      LAS float* d = (LAS float*)(size_t)(c.ldsb + L1_ST + ((c.w - 6) * 64 + lane) * 4);
      d[0] = s[0]; d[128] = __builtin_amdgcn_rsqf(fmaxf(__builtin_fmaf(-s[0], s[0], s[1]), 0.f) + 1e-5f);
    }
    __builtin_amdgcn_s_setprio(0);
  }
  SB();
  if (TYPE != 1) wd_prefetch<4, 6>(rw4, c.ldsb, c.j, c.w, lane);
  if (X) pf.items<26, 28>();
  SB();
  C(1, false, 0, []() {});
}

template <int N, class F> DEVI void static_for(F&& f) {
  if constexpr (N > 0) { static_for<N - 1>(f); f(std::integral_constant<int, N - 1>{}); }
}
DEVI u32x4 pair16(u32x2 a, u32x2 b) {
  const auto s0 = __builtin_amdgcn_permlane32_swap(a[0], b[0], false, false);
  const auto s1 = __builtin_amdgcn_permlane32_swap(a[1], b[1], false, false);
  return u32x4{s0[0], s1[0], s0[1], s1[1]};
}
struct Watch {
  LAS unsigned* lf; char* ws; u64 tag; int mt2, j, w, lane;
  unsigned status;
  DEVI u64 fetch() {
    int l2 = lane;
    PIN(l2);
    const unsigned f0 = l2 < 32 ? OFF_FH + mt2 * 256 + (j >> 2) * 128 + (l2 < 11 ? l2 : 0) * 8 : OFF_F3 + (blockIdx.x & 7) * 128 + (l2 & 15) * 8;
    return __hip_atomic_load((GAS u64*)(ws + f0), RLX_AGENT);
  }
  DEVI bool eval(u64 v, bool giveup) {
    const u64 no = __ballot(v != tag);
    unsigned s = ((no & 0x1ffull) ? 0u : S_H) | ((no & 0x600ull) ? 0u : S_Q) | ((no >> 32) ? 0u : S_M);
    if (giveup) s = 7u;
    if (s == status) return false;
    status = s;
    __hip_atomic_store(lf + LF_STATUS, s, RLX_WG);
    return true;
  }
  DEVI bool poll(bool giveup) { return eval(fetch(), giveup); }
  DEVI bool has(unsigned bits) { return (~(unsigned)__builtin_amdgcn_readfirstlane(__hip_atomic_load(lf + LF_STATUS, RLX_WG)) & bits) == 0u; }
  int pcnt, plast, preps; unsigned pflag;
  DEVI void publish() {
    if ((int)lds_inc(lf + LF_PUB + pcnt, lane) == plast && lane < preps) set_flag(ws, pflag + lane * 128, tag);
    preps = 0;
  }
  DEVI void need(unsigned bits, int watcher = 7) {
    if (w != watcher) { lds_wait_bits(lf + LF_STATUS, bits); return; }
#pragma unroll 1
    for (unsigned spins = 0; (~status & bits) != 0u; ++spins)
      if (!poll(spins >= (1u << 20))) __builtin_amdgcn_s_sleep(1);
    asm volatile("" ::: "memory");
  }
};

#define EPI_N 12
DEVI void epi_ffn(LAS char* lds, f32x4 (&acc)[4][6], char* ws, rsrc_t rws, Watch& wt, const P1& c, PF2& pf, int lane) {
  const int r = lane & 15, g = lane >> 4;
  pf.items<0, 2>();
  float nm[4], k1[4], k2[4], iv[4];
#pragma unroll
  for (int mi = 0; mi < 4; ++mi) {
    const LAS float* sp = (const LAS float*)(lds + L1_ST) + c.mw * 64 + mi * 16 + r;
    const float rs = sp[128], c0 = -2.0f * 0.7978845608028654f * 1.4426950408889634f;
    nm[mi] = -sp[0]; k1[mi] = c0 * rs; k2[mi] = c0 * 0.044715f * rs * rs * rs; iv[mi] = __builtin_amdgcn_rcpf(rs * rs);
  }
  const unsigned hb = OFF_H + ((c.Mt * 2 + c.mw) * 54 + 2 * c.cslot + (c.nw >> 1)) * 4096 + (c.nw & 1) * 512 + (lane & 31) * 16 + (lane >> 5) * 1024;
  u32x4 v[6];
  static_for<3>([&](auto I) {
    constexpr int i = I;
    const f32x4 cg = *(LAS f32x4*)(lds + L1_CS + ((c.nw * 6 + i) * 16 + 4 * g) * 4);
    const f32x4 cu = *(LAS f32x4*)(lds + L1_CS + ((c.nw * 6 + 3 + i) * 16 + 4 * g) * 4);
    u32x2 o[4];
    static_for<4>([&](auto MI) {
      constexpr int mi = MI, n = 4 * i + mi;
      if constexpr (2 + 2 * n < EPI_N) pf.template items<2 + 2 * n, 4 + 2 * n>();
#pragma unroll
      for (int hh = 0; hh < 2; ++hh) {
        const f32x2 G = fma2(bc2(nm[mi]), hh ? hi2(cg) : lo2(cg), hh ? hi2(acc[mi][i]) : lo2(acc[mi][i]));
        const f32x2 U = fma2(bc2(nm[mi]), hh ? hi2(cu) : lo2(cu), hh ? hi2(acc[mi][3 + i]) : lo2(acc[mi][3 + i]));
        const f32x2 z = G * fma2(G * G, bc2(k2[mi]), bc2(k1[mi]));
        const f32x2 d = fma2(f32x2{__builtin_amdgcn_exp2f(z[0]), __builtin_amdgcn_exp2f(z[1])}, bc2(iv[mi]), bc2(iv[mi]));
        o[mi][hh] = pk2(G * U * f32x2{__builtin_amdgcn_rcpf(d[0]), __builtin_amdgcn_rcpf(d[1])});
      }
      PIN(o[mi]);
      SB();
#define STN 4
#define STS 4
      if constexpr (n == STN || n == STN + STS) {
        constexpr int p = (n - STN) / STS;
        st16(rws, hb, p * 18 * 4096, v[2 * p]);
        st16(rws, hb, p * 18 * 4096 + 2048, v[2 * p + 1]);
        SB();
      }
    });
    v[2 * i] = pair16(o[0], o[1]); v[2 * i + 1] = pair16(o[2], o[3]);
    PIN(v[2 * i]); PIN(v[2 * i + 1]);
  });
  st16(rws, hb, 36 * 4096, v[4]);
  st16(rws, hb, 36 * 4096 + 2048, v[5]);
  STAMP(4);
  BAR();
  WAIT_V(0);
  wt.pcnt = c.mw; wt.plast = 3; wt.preps = 2; wt.pflag = OFF_FH + (c.Mt * 2 + c.mw) * 256 + c.cslot * 8;
  wt.publish();
  STAMP(5);
  pf.items<EPI_N, 13>();
  WAIT_V(0);
}

DEVI f32x4 sweep8(rsrc_t rws, unsigned voff, unsigned soff, unsigned stride, unsigned tag) {
  u32x4 gr[8];
#pragma unroll 1
  for (unsigned spins = 0; spins < (1u << 20); ++spins) {
#pragma unroll
    for (int p = 0; p < 8; ++p) gr[p] = ld16(rws, voff, soff + p * stride);
    SB();
    unsigned bad = 0;
#pragma unroll
    for (int p = 0; p < 8; ++p) bad |= (gr[p][1] ^ tag) | (gr[p][3] ^ tag);
    if (__all(bad == 0u || voff == OOB)) break;
    asm volatile("" ::: "memory");
  }
  f32x2 a[8], b[8];
#pragma unroll
  for (int p = 0; p < 8; ++p) { a[p] = unpk(gr[p][0]); b[p] = unpk(gr[p][2]); }
  const f32x2 sa = ((a[0] + a[1]) + (a[2] + a[3])) + ((a[4] + a[5]) + (a[6] + a[7]));
  const f32x2 sb = ((b[0] + b[1]) + (b[2] + b[3])) + ((b[4] + b[5]) + (b[6] + b[7]));
  return f32x4{sa[0], sa[1], sb[0], sb[1]};
}

DEVI void epi_q(LAS char* lds, f32x4 (&acc)[4][6], char* ws, rsrc_t rws, Watch& wt, const P1& c, PF2& pf, int lane) {
  const int r = lane & 15, g = lane >> 4, qi = c.cslot - 13;
  f32x4 cs[5];
#pragma unroll
  for (int i = 0; i < 5; ++i) cs[i] = *(LAS f32x4*)(lds + L1_CS + ((c.nw * 6 + i) * 16 + 4 * g) * 4);
  pf.items<0, 8>();
  static_for<4>([&](auto MI) {
    constexpr int mi = MI;
    const LAS float* sp = (const LAS float*)(lds + L1_ST) + c.mw * 64 + mi * 16 + r;
    const f32x2 nm = bc2(-sp[0]), rs = bc2(sp[128]);
    static_for<5>([&](auto I) {
      constexpr int i = I;
      const f32x2 a = phi2(fma2(nm, lo2(cs[i]), lo2(acc[mi][i])), rs), b = phi2(fma2(nm, hi2(cs[i]), hi2(acc[mi][i])), rs);
      acc[mi][i] = f32x4{a[0], a[1], b[0], b[1]};
      PIN(acc[mi][i]);
      SB();
      pf.template item<8 + mi * 5 + i>();
    });
  });
  STAMP(3);
  LAS unsigned* lf = (LAS unsigned*)(lds + L2_FLAG);
  if (c.w == 4) { wait_flags(ws, OFF_FZ + (blockIdx.x & 7) * 512 + qi * 256 + (lane & 31) * 8, wt.tag); lds_inc(lf + LF_K, lane); }
  else lds_wait(lf + LF_K, 1u);
  STAMP(12);
  {
    const int q4 = (c.w & 1) * 64 + lane;
    const f32x4 zs = sweep8(rws, q4 < 40 ? q4 * 16 : q4 < 80 ? q4 * 16 + 31 * 640 : OOB, OFF_ZP + (qi * 64 + (c.w >> 1) * 8) * 640, 640, (unsigned)wt.tag);
    if (q4 < 80) *(LAS f32x4*)(lds + L2_RED + (c.w >> 1) * 1280 + q4 * 16) = zs;
    WAIT_L0(); BAR();
  }
  u64 fv = 0;
  STAMP(5);
  f32x4 z[5];
#pragma unroll
  for (int i = 0; i < 5; ++i) {
    const LAS char* p = lds + L2_RED + (c.nw * 80 + 20 * (2 * (g & 1) + (g >> 1)) + 4 * i) * 4;
    z[i] = (*(const LAS f32x4*)p + *(const LAS f32x4*)(p + 1280)) + (*(const LAS f32x4*)(p + 2560) + *(const LAS f32x4*)(p + 3840));
  }
  const unsigned qb = OFF_Q + ((c.Mt * 2 + c.mw) * 20 + (2 * qi + (c.nw >> 1)) * 5) * 4096;
  u32x2 keep;
#pragma unroll
  for (int mi = 0; mi < 4; ++mi) {
    f32x2 d0 = lo2(acc[mi][0]) * lo2(z[0]), d1 = hi2(acc[mi][0]) * hi2(z[0]);
#pragma unroll
    for (int i = 1; i < 5; ++i) { d0 = fma2(lo2(acc[mi][i]), lo2(z[i]), d0); d1 = fma2(hi2(acc[mi][i]), hi2(z[i]), d1); }
    d0 += d1;
    const f32x2 inv = bc2(__builtin_amdgcn_rcpf(d0[0] + d0[1] + 1e-6f));
    unsigned o[10];
#pragma unroll
    for (int i = 0; i < 5; ++i) { o[2 * i] = pk2(lo2(acc[mi][i]) * inv); o[2 * i + 1] = pk2(hi2(acc[mi][i]) * inv); }
    st16(rws, lane * 16 + mi * 1024, qb + (c.nw & 1) * 12288, u32x4{o[0], o[1], o[2], o[3]});
    st16(rws, lane * 16 + mi * 1024, qb + (c.nw & 1) * 12288 + 4096, u32x4{o[4], o[5], o[6], o[7]});
    if (mi & 1) st16(rws, (lane & 31) * 16 + (lane >> 5) * 1024 + (mi - 1) * 1024, qb + 8192 + (c.nw & 1) * 512, pair16(keep, u32x2{o[8], o[9]}));
    else keep = u32x2{o[8], o[9]};
    if (mi == 0 && c.w == 7) { SB(); fv = wt.fetch(); SB(); }
  }
  STAMP(4);
  SB();
  if (c.w == 7) wt.eval(fv, false);
  WAIT_L0(); BAR();
  wt.pcnt = c.mw; wt.plast = 3; wt.preps = 2; wt.pflag = OFF_FH + (c.Mt * 2 + c.mw) * 256 + (9 + qi) * 8;
}

DEVI void epi_kv(LAS char* lds, f32x4 (&acc)[4][6], char* ws, rsrc_t rws, u64 tag, const P1& c, int lane) {
  const int cl = lane & 15, g = lane >> 4, kj = c.cslot - 9;
  float cs[5];
#pragma unroll
  for (int i = 0; i < 5; ++i) cs[i] = -*(LAS float*)(lds + L1_CS + ((c.nw * 6 + i) * 16 + cl) * 4);
  f32x4 mean[4], rstd[4];
#pragma unroll
  for (int mi = 0; mi < 4; ++mi) {
    mean[mi] = *(LAS f32x4*)(lds + L1_ST + (c.mw * 64 + mi * 16 + 4 * g) * 4);
    rstd[mi] = *(LAS f32x4*)(lds + L1_ST + 512 + (c.mw * 64 + mi * 16 + 4 * g) * 4);
  }
  u32x4 fk[2][3], fv[2][3];
  auto center = [&](int mi, int i, int hh) { return fma2(hh ? hi2(mean[mi]) : lo2(mean[mi]), bc2(cs[i]), hh ? hi2(acc[mi][i]) : lo2(acc[mi][i])); };
#pragma unroll
  for (int mi = 0; mi < 4; ++mi)
#pragma unroll
    for (int f = 0; f < 3; ++f)
#pragma unroll
      for (int hh = 0; hh < 2; ++hh)
        fk[mi >> 1][f][(mi & 1) * 2 + hh] = pk2(phi2(center(mi, f < 2 ? f : 4, hh), hh ? hi2(rstd[mi]) : lo2(rstd[mi])));
  const int uh = cl < 4 ? 16 + cl : 32 + cl;
  const f32x4 zero = {0.f, 0.f, 0.f, 0.f};
  {
    const u32x4 one = {0x3f803f80u, 0x3f803f80u, 0x3f803f80u, 0x3f803f80u};
    f32x4 z[3];
#pragma unroll
    for (int f = 0; f < 3; ++f) z[f] = mfma(one, fk[1][f], mfma(one, fk[0][f], zero));
    const float v = g == 0 ? z[0][0] : g == 1 ? z[1][0] : z[2][0];
    const float vn = __builtin_bit_cast(float, __builtin_amdgcn_mov_dpp(__builtin_bit_cast(int, v), 0xB1, 0xf, 0xf, true));
    st8(rws, (cl & 1) ? OOB : g < 2 ? (g * 20 + cl) * 4 : (g == 2 && cl < 8) ? uh * 4 : OOB,
        OFF_ZP + (kj * 32 + c.Mt * 2 + c.mw) * 640 + c.nw * 160, u32x2{pk2(v, vn), (unsigned)tag});
    if (lds_inc((LAS unsigned*)(lds + L2_FLAG) + LF_PUB, lane) == 7u && lane < 8) set_flag(ws, OFF_FZ + lane * 512 + kj * 128 + c.Mt * 8, tag);
    SB();
  }
#pragma unroll
  for (int mi = 0; mi < 4; ++mi)
#pragma unroll
    for (int f = 0; f < 3; ++f)
#pragma unroll
      for (int hh = 0; hh < 2; ++hh)
        fv[mi >> 1][f][(mi & 1) * 2 + hh] = pk2(center(mi, 2 + f, hh) * (hh ? hi2(rstd[mi]) : lo2(rstd[mi])));
  constexpr int KI[7] = {0, 0, 2, 1, 1, 2, 2}, VI[7] = {0, 2, 0, 1, 2, 1, 2};
  f32x4 p[7];
#pragma unroll
  for (int q = 0; q < 7; ++q) p[q] = mfma(fv[1][VI[q]], fk[1][KI[q]], mfma(fv[0][VI[q]], fk[0][KI[q]], zero));
  LAS char* ex = lds + L1_EX + c.w * 3584;
  auto put = [&](bool ok, int u, int e, f32x4 v) { *(LAS f32x4*)(ex + (ok ? (u * 20 + e) * 4 : 3200)) = v; };
  put(true, cl, 4 * g, p[0]);
  put(true, 20 + cl, 4 * g, p[3]);
  put(g >= 2, (g & 1) * 20 + cl, 16, g == 2 ? p[1] : p[4]);
  put(cl < 8, uh, 4 * g, cl < 4 ? p[2] : p[5]);
  put(cl < 8 && g == 2 + (cl >> 2), uh, 16, p[6]);
  WAIT_L0(); BAR();
#pragma unroll
  for (int q = 0; q < 2; ++q) {
    const int o = (c.mw * 128 + q * 64 + lane) * 16;
    const LAS char* src = lds + L1_EX + c.nw * 3584 + (o < 3200 ? o : 0);
    const f32x4 v = *(const LAS f32x4*)src + *(const LAS f32x4*)(src + 4 * 3584);
    st16(rws, o < 3200 ? o : OOB, OFF_KVP + ((kj * 4 + c.nw) * 16 + c.Mt) * 3456,
         u32x4{pk2(lo2(v)), (unsigned)tag, pk2(hi2(v)), (unsigned)tag});
  }
  SB();
  STAMP(4);
  LAS unsigned* lf = (LAS unsigned*)(lds + L2_FLAG);
  if (lds_inc(lf + LF_PUB + 1, lane) == 7u && lane < 8) set_flag(ws, OFF_FK + lane * 512 + kj * 128 + c.Mt * 8, tag);
  BAR();
}

DEVI void retile(const char* wd, rsrc_t rws, unsigned ldsw, int id, int w, int lane) {
  const u32x4 rd = srd(wd, kF * 1280);
  const int jj = lane >> 3, g = lane >> 4;
  const unsigned vo = ((jj >> 2) * 8 + (jj & 3)) * 1280 + (lane & 7) * 16;
  const unsigned ra = ldsw + g * 1024 + ((lane & 15) >> 2) * 128 + (lane & 3) * 8;
  auto fetch = [&](int n) {
    const int it = id * 8 + w + n * 128, rb = it / 10, ln = it - rb * 10;
#pragma unroll
    for (int q = 0; q < 4; ++q)
      dma<0>(rd, vo, it < 540 ? (rb * 32 + 16 * (q >> 1) + 4 * (q & 1)) * 1280 + ln * 128 : OOB, ldsw + (n & 3) * 4096 + q * 1024);
  };
  auto put = [&](int n) {
    const int it = id * 8 + w + n * 128, rb = it / 10, ln = it - rb * 10, cg = rb / 6, uu = rb - cg * 6;
    const int ks = 18 * (uu >> 1) + 2 * cg + (uu & 1);
#pragma unroll
    for (int q = 0; q < 4; ++q) {
      const int nt = ln * 4 + q, j = nt / 5, t = nt - j * 5;
      st16(rws, lane * 16, it < 540 ? OFF_WD + ((j * 54 + ks) * 5 + t) * 1024 : OOB, lds_trfrag(ra + (n & 3) * 4096 + q * 32));
    }
  };
#pragma unroll 1
  for (int n = 0; n < 4; ++n) fetch(n);
  WAIT_V(0);
  put(0);
  WAIT_L0();
  fetch(4);
#pragma unroll 1
  for (int n = 1; n < 5; ++n) {
    if (n == 4) WAIT_V(0);
    put(n);
  }
}

DEVI void fold_kv(LAS char* lds, char* ws, rsrc_t rws, Watch& wt, const u32x4 (&wf)[2][5], int hp, int w, int lane) {
  const u64 tag = wt.tag;
  const int cl = lane & 15, g = lane >> 4;
  const int o = ((w & 3) * 64 + lane) * 16;
  unsigned po = o < 3200 ? o : OOB, ko[6][2];
#pragma unroll
  for (int f = 0; f < 6; ++f) {
    const int u = 20 * (f & 1) + (f < 4 ? (cl & 4) * 2 + 4 * (f >> 1) : 16) + (cl & 3);
    const bool mine = f >= 4 || (cl >> 3) == (f & 1);
    ko[f][0] = (unsigned)(size_t)lds + L2_RED + (mine && g < 3 ? u * 80 + g * 32 : 3360);
    ko[f][1] = (unsigned)(size_t)lds + L2_RED + (mine && g < 2 ? u * 80 + g * 32 + 16 : 3360);
    PIN(ko[f][0]); PIN(ko[f][1]);
  }
  unsigned vo = OFF_MP + (w * 20 + (hp >> 2) * 5) * 5120 + (3 * ((hp >> 1) & 1) + (g & 1)) * 5120 + (((hp & 1) + (g & 2)) * 16 + cl) * 16;
  unsigned vo2 = OFF_MP + (w * 20 + (hp >> 2) * 5 + 2) * 5120 + g * 1024 + ((hp & 3) * 16 + cl) * 16;
  PIN(po); PIN(vo); PIN(vo2);
  WAIT_V(0);
  if (w == 0) wait_flags(ws, OFF_FK + (blockIdx.x & 7) * 512 + (hp >> 2) * 128 + cl * 8, tag);
  BAR();
  STAMP(2);
  {
    *(LAS f32x4*)(lds + L2_RED + (w >> 2) * 4096 + o) = sweep8(rws, po, OFF_KVP + (hp * 16 + (w >> 2) * 8) * 3456, 3456, (unsigned)tag);
    WAIT_L0(); BAR();
  }
  STAMP(12);
  u64 fv = 0;
  if (w == 7) fv = wt.fetch();
  SB();
  u32x4 kf[6];
#pragma unroll
  for (int f = 0; f < 6; ++f) {
    const f32x4 lo = *(LAS f32x4*)(size_t)ko[f][0] + *(LAS f32x4*)(size_t)(ko[f][0] + 4096);
    const f32x4 hi = *(LAS f32x4*)(size_t)ko[f][1] + *(LAS f32x4*)(size_t)(ko[f][1] + 4096);
    kf[f] = u32x4{pk2(lo2(lo)), pk2(hi2(lo)), pk2(lo2(hi)), pk2(hi2(hi))};
  }
  const f32x4 zero = {0.f, 0.f, 0.f, 0.f};
  u32x4 r1;
#pragma unroll
  for (int i = 0; i < 5; ++i) {
    const f32x4 d1 = mfma(kf[1], wf[1][i], mfma(kf[0], wf[0][i], zero)), d2 = mfma(kf[3], wf[1][i], mfma(kf[2], wf[0][i], zero));
    const f32x4 d3 = mfma(kf[4], wf[0][i], zero), d4 = mfma(kf[5], wf[1][i], zero);
    st16(rws, vo, i * 1024, u32x4{pk2(lo2(d1)), pk2(hi2(d1)), pk2(lo2(d2)), pk2(hi2(d2))});
    const u32x4 t = {pk2(lo2(d3)), pk2(hi2(d3)), pk2(lo2(d4)), pk2(hi2(d4))};
    if (i == 0 || i == 4) r1 = t; else r1 = g == i ? t : r1;
    if (i == 3) st16(rws, vo2, 0, r1);
    if (i == 4) st16(rws, g == 0 ? vo2 + 4096 : OOB, 0, r1);
  }
  STAMP(3);
  SB();
  if (w == 7) wt.eval(fv, false);
  WAIT_L0(); BAR();
  wt.pcnt = 0; wt.plast = 7; wt.preps = 8; wt.pflag = OFF_F3 + hp * 8;
}

#define ASLEEP 0
#define FPRE 3
template <bool ATT>
DEVI void p2_steps(LAS char* lds, rsrc_t rws, Watch& wt, const u32x4 (&br)[3][5], f32x4 (&acc)[4][5], const u32x2 (&xr)[5],
                   int mt2, int j, int w, int lane) {
  LAS unsigned* lf = wt.lf;
  const unsigned ab = OFF_H + (mt2 * 54 + w) * 4096;
  u32x4 fa[12][4], fb[12][5];
  auto ldA = [&](int n) {
    const unsigned so = n >= 7 ? OFF_Q + (mt2 * 20 + (w - 4) * 5 + n - 7) * 4096
                               : n == 6 && w >= 6 ? OOB : ab + (n < 3 ? 30 + 8 * ((n + (mt2 >> 1)) % 3) : 8 * n - 24) * 4096;
#pragma unroll
    for (int m = 0; m < 4; ++m) fa[n][m] = ld16(rws, lane * 16, so + ((m + w) & 3) * 1024);
    SB();
  };
  unsigned lb = (unsigned)(size_t)lds + w * 5120 + lane * 16;
  auto rdB = [&](int n) {
    if (n == 5) { lb += 81920; PIN(lb); }
    if (ATT && n == 6) { lb -= w >= 6 ? 40960 : 0; PIN(lb); }
#pragma unroll
    for (int t = 0; t < 5; ++t) fb[n][t] = lds_b128(lb + ((n - 3) & 1) * 40960 + t * 1024);
    SB();
  };
  auto ld1 = [&](int kind, int n, int i) {
    if (kind == 0) {
      const unsigned so = n >= 7 ? OFF_Q + (mt2 * 20 + (w - 4) * 5 + n - 7) * 4096
                                 : n == 6 && w >= 6 ? OOB : ab + (n < 3 ? 30 + 8 * ((n + (mt2 >> 1)) % 3) : 8 * n - 24) * 4096;
      fa[n][i] = ld16(rws, lane * 16, so + ((i + w) & 3) * 1024);
    } else fb[n][i] = ld16(rws, lane * 16, OFF_MP + (j * 20 + (w - 4) * 5 + n - 7) * 5120 + i * 1024);
    SB();
  };
  LAS char* ex = lds + w * 5120 + lane * 16;
  constexpr int PRE = ATT ? 6 : FPRE;
  if (ATT && ASLEEP && wt.preps == 0) __builtin_amdgcn_s_sleep(ASLEEP);
#pragma unroll
  for (int n = 0; n < PRE; ++n) {
    ldA(n);
    if (n == 1 && wt.preps != 0) { WAIT_V(8); wt.publish(); STAMP(13); }
  }
  if (ATT) {
#pragma unroll
    for (int t = 0; t < 5; ++t) {
      const f32x2 x0 = unpk(xr[t][0]), x1 = unpk(xr[t][1]);
      acc[0][t] = f32x4{x0[0], x0[1], x1[0], x1[1]};
      PIN(acc[0][t]);
    }
    SB();
    asm volatile("s_nop 1" ::: "memory");
  }
  constexpr int A1[12] = {ATT ? -1 : FPRE, ATT ? 6 : FPRE == 3 ? 5 : FPRE == 4 ? 6 : -1, ATT ? 7 : -1, -1, ATT ? 8 : -1, -1, ATT ? 9 : -1, 10, 11, -1, -1, -1};
  constexpr int A2[12] = {ATT ? -1 : FPRE + 1, ATT || FPRE > 3 ? -1 : 6, -1, -1, -1, -1, -1, -1, -1, -1, -1, -1};
  constexpr int M1[12] = {-1, -1, -1, ATT ? 7 : -1, -1, ATT ? 8 : -1, ATT ? 9 : -1, 10, 11, -1, -1, -1};
  u64 fv = 0;
  static_for<(ATT ? 12 : 7)>([&](auto N) {
    constexpr int n = N;
    if (n == 2) lds_wait(lf + LF_DMA, 8u);
    if (ATT && n == 2) lds_wait_bits(lf + LF_STATUS, S_Q);
    if (ATT && n == 3) { lds_wait_bits(lf + LF_STATUS, S_M); STAMP(7); }
    if (!ATT && (n == 3 || n == 5) && w == 3) { if (n == 5) wt.eval(fv, false); fv = wt.fetch(); SB(); }
    if (n >= 2 && n < 6) rdB(n + 1);
    if (n == (ATT ? 11 : 6)) lds_wait(lf + LF_BC + ATT, 4u);
#pragma unroll
    for (int q = 0; q < 20; ++q) {
      constexpr int LASTN = ATT ? 11 : 6;
      const int m = n == LASTN ? (q / 5 + 1) & 3 : n < 7 ? q / 5 : q & 3, t = n < 7 || n == LASTN ? q % 5 : q >> 2;
      if (n == 0 && (m > 0 || !ATT)) mfma_new(acc[m][t], br[0][t], fa[0][m]);
      else mfma_acc(acc[m][t], n < 3 ? br[n < 3 ? n : 0][t] : fb[n][t], fa[n][m]);
      if (n == LASTN && q >= 10) {
        SB();
        *(LAS f32x4*)(ex + ((q - 10) / 5) * 40960 + (q % 5) * 1024) = acc[(q - 10) / 5 + 1][q % 5];
        SB();
      }
      if (q & 1) {
        const int p = q >> 1, na = (A1[n] >= 0 ? 4 : 0) + (A2[n] >= 0 ? 4 : 0);
        if (p < na) ld1(0, p < 4 ? A1[n] : A2[n], p & 3);
        else if (M1[n] >= 0 && p - na < 5) ld1(1, M1[n], p - na);
      }
    }
    SB();
    if (n == 0) STAMP(9);
    if (n == 6) STAMP(11);
    if (n == 5) { WAIT_L0(); lds_inc(lf + LF_BC + ATT, lane); }
  });
  if (!ATT && w == 3) wt.eval(fv, false);
}

DEVI void phase2(LAS char* lds, const char* x, char* ws, rsrc_t rws, char* out, Watch& wt, u32x4 (&br)[3][5],
                 int mt2, int j, int w, int lane) {
  const int a = w & 3;
  LAS unsigned* lf = wt.lf;
  const unsigned obase = (mt2 * 64 + a * 16) * 1280 + j * 160;
  f32x4 acc[4][5];
  u32x2 xr[5];
  if (w >= 4) {
    const rsrc_t rx = __builtin_amdgcn_make_buffer_rsrc((void*)x, 0, 2048 * 1280, 0x00020000);
#pragma unroll
    for (int t = 0; t < 5; ++t) xr[t] = __builtin_amdgcn_raw_buffer_load_b64(rx, (lane & 15) * 1280 + (lane >> 4) * 8 + t * 32, obase, 0);
    SB();
  }
  if (wt.preps != 0) { if (!wt.has(S_H)) { if (w == 7) wt.poll(false); WAIT_V(0); wt.publish(); STAMP(13); } }
  else WAIT_V(15);
  lds_inc(lf + LF_DMA, lane);
  wt.need(S_H);
  STAMP(6);
  if (w < 4) {
    __builtin_amdgcn_s_setprio(2);
    p2_steps<false>(lds, rws, wt, br, acc, xr, mt2, j, w, lane);
    __builtin_amdgcn_s_setprio(0);
#pragma unroll
    for (int q = 0; q < 15; ++q) asm volatile("" : "=v"(br[q / 5][q % 5]));
  }
  PINS(w);
  if (w >= 4) { __builtin_amdgcn_s_setprio(1); p2_steps<true>(lds, rws, wt, br, acc, xr, mt2, j, w, lane); }
  asm volatile("s_nop 7" ::: "memory");
  SB();
  STAMP(14);
  LAS char* ex = lds + (w >> 2) * 20480 + lane * 16;
#pragma unroll
  for (int t = 0; t < 5; ++t) *(LAS f32x4*)(ex + 2 * 40960 + (a * 5 + t) * 1024) = acc[3][t];
  WAIT_L0();
  if (w < 4) { lds_inc(lf + LF_X, lane); if (w == 3) wt.need(S_Q | S_M, 3); lds_wait(lf + LF_X, 4u); }
  else BAR();
  STAMP(8);
#pragma unroll
  for (int t = 0; t < 5; ++t)
#pragma unroll
    for (int q = 1; q < 4; ++q) acc[0][t] += *(LAS f32x4*)(ex + (q - 1) * 40960 + (((a - q) & 3) * 5 + t) * 1024);
  LAS char* base = lds + L2_BASE + a * 5120 + lane * 16;
  if (w < 4) {
#pragma unroll
    for (int t = 0; t < 5; ++t) *(LAS f32x4*)(base + t * 1024) = acc[0][t];
    WAIT_L0();
  } else {
    const rsrc_t ro = __builtin_amdgcn_make_buffer_rsrc(out, 0, 2048 * 1280, 0x00020000);
    u32x2 o[5];
#pragma unroll
    for (int t = 0; t < 5; ++t) {
      const f32x4 s = acc[0][t] + *(LAS f32x4*)(base + t * 1024);
      o[t] = u32x2{pk2(lo2(s)), pk2(hi2(s))};
    }
    const unsigned orow = obase + (lane & 15) * 1280;
#pragma unroll
    for (int t = 0; t < 4; t += 2) {
      const auto s0 = __builtin_amdgcn_permlane16_swap(o[t][0], o[t + 1][0], false, false);
      const auto s1 = __builtin_amdgcn_permlane16_swap(o[t][1], o[t + 1][1], false, false);
      __builtin_amdgcn_raw_buffer_store_b128(u32x4{s0[0], s1[0], s0[1], s1[1]}, ro, orow + ((lane >> 4) & 1) * 32 + (lane >> 5) * 16 + t * 32, 0, OUT_AUX);
    }
    __builtin_amdgcn_raw_buffer_store_b64(o[4], ro, orow + (lane >> 4) * 8 + 128, 0, OUT_AUX);
  }
}

__global__ __launch_bounds__(512, 2) void fused(const char* x, const char* wqkv, const char* wo, const char* wg,
                                                const char* wu, const char* wd, char* ws, char* out) {
  extern __shared__ __attribute__((aligned(16))) char smem[];
  const char* pc0;
  asm volatile("s_getpc_b64 %0" : "=s"(pc0));
  asm volatile("" : "+s"(x), "+s"(wqkv), "+s"(wo), "+s"(wg), "+s"(wu), "+s"(wd), "+s"(ws), "+s"(out));
  LAS char* lds = (LAS char*)smem;
  const unsigned ldsb = (unsigned)(size_t)lds;
  const int bid = blockIdx.x, xcd = bid & 7, idx = bid >> 3;
  const int Mt = (xcd & 1) * 8 + (idx & 7), grp = xcd >> 1, pos4 = idx >> 3, slot2 = grp * 4 + pos4;
  const int cslot = (int)(0xCF76BE54AD329810ull >> (4 * (pos4 == 2 ? (slot2 + 4 * (idx >> SPR)) & 15 : slot2))) & 15;
  const int w = __builtin_amdgcn_readfirstlane(threadIdx.x >> 6), lane = threadIdx.x & 63;
  STAMP(0);
  const rsrc_t rws = __builtin_amdgcn_make_buffer_rsrc(ws, 0, WS_END, 0x00020000);
  u64 tag;
  u32x2 epoch = {0u, 0u};
  ld8_hidden(epoch, srd(ws, WS_END), OFF_E);
  Watch wt{(LAS unsigned*)(lds + L2_FLAG), ws, 0ull, Mt * 2 + (slot2 >> 3), slot2 & 7, w, lane, 0u, 0, 0, 0, 0u};
  u32x4 br[3][5];
  PF2 pf{ws, rws, ldsb, slot2 & 7, w, lane, Mt % 3, br};
  if (cslot == 15) {
    if (threadIdx.x < 16) ((LAS unsigned*)(lds + L2_FLAG))[threadIdx.x] = 0u;
    retile(wd, rws, ldsb + w * 16384, Mt, w, lane);
    WAIT_V(0);
    PIN(epoch);
    wt.tag = tag = make_tag(epoch);
    WAIT_L0(); BAR();
    if (threadIdx.x == 0) { set_flag(ws, OFF_FR + Mt * 8, tag); set_flag(ws, OFF_F1 + (Mt * 16 + 15) * 8, tag); }
    STAMP(5);
    u32x4 wf[2][5];
    {
      const u32x4 ro = srd(wo, kD * 1280);
#pragma unroll 1
      for (int q = 0; q < 10; ++q)
        dma<0>(ro, (lane >> 3) * 1280 + (lane & 7) * 16, (Mt * 40 + (q >> 1) * 8) * 1280 + ((w * 160) & ~127) + (q & 1) * 128, ldsb + w * 16384 + q * 1024);
      WAIT_V(0);
#pragma unroll
      for (int hh = 0; hh < 2; ++hh) {
        unsigned ra[2];
#pragma unroll
        for (int h2 = 0; h2 < 2; ++h2) {
          int row = hh * 20 + (lane >> 4) * 8 + h2 * 4 + ((lane & 15) >> 2);
          row = row < 40 ? row : 39;
          ra[h2] = ldsb + w * 16384 + (row >> 3) * 2048 + (row & 7) * 128 + (lane & 3) * 8;
        }
#pragma unroll
        for (int i = 0; i < 5; ++i) {
          const int cb = (w & 3) * 32 + i * 32, o = (cb >> 7) * 1024 + (cb & 127);
          const u32x2 lo = __builtin_bit_cast(u32x2, __builtin_amdgcn_ds_read_tr16_b64_v4i16((LAS s16x4*)(size_t)(ra[0] + o)));
          const u32x2 hi = __builtin_bit_cast(u32x2, __builtin_amdgcn_ds_read_tr16_b64_v4i16((LAS s16x4*)(size_t)(ra[1] + o)));
          wf[hh][i] = u32x4{lo[0], lo[1], hi[0], hi[1]};
        }
      }
    }
    wait_flags(ws, OFF_FR + (lane & 15) * 8, tag);
    WAIT_L0(); BAR();
    wd_prefetch<0, 6>(srd(ws, WS_END), ldsb, slot2 & 7, w, lane);
    pf.items<0, 28>();
    if (Mt == 0 && w == 7) {
#pragma unroll 1
      for (int q = 0; q < 4; ++q) wait_flags(ws, OFF_F1 + (q * 64 + lane) * 8, tag);
      if (lane == 0) set_flag(ws, OFF_E, tag);
    }
    fold_kv(lds, ws, rws, wt, wf, Mt, w, lane);
  } else {
    const int type = cslot < 9 ? 0 : cslot < 13 ? 1 : 2;
    P1 c;
    c.w = w; c.mw = w >> 2; c.nw = w & 3; c.Mt = Mt; c.cslot = cslot; c.j = slot2 & 7;
    c.ldsb = ldsb;
    {
#define ROTM 1
      const int kb = (Mt + cslot) % 10, rot = ROTM == 0 ? (kb >= 5 ? 5 : 0) : ROTM == 1 ? (kb > 7 ? 7 : kb) : (kb / 3) * 3 - (kb == 9 ? 3 : 0);
      c.bstar = kb - rot;
      const int seg = w >> 2;
      c.pitch = type == 0 ? kF * 2 : kQKV * 2;
      const int startb = type == 0 ? cslot * 384
                       : ((type == 1 ? (640 * (1 + seg) + 160 * (cslot - 9)) * 2 : 320 * (2 * (cslot - 13) + seg)) & ~127);
      const char* wb = type == 0 ? (seg ? wu : wg) : wqkv;
      c.rw = srd(wb + startb - 2048, 640 * c.pitch - startb + 2048);
      c.rx = srd(x + (size_t)Mt * 128 * 1280, 128 * 1280);
      const int i = lane >> 3, cp = lane & 7, pg = w & 3;
      c.voffB = (unsigned)(8 * pg + i) * c.pitch + 16 * (cp ^ ((i & 2) | ((pg & 1) << 2))) + 2048;
      c.voffA = (unsigned)(8 * w + i) * 1280 + 16 * (cp ^ ((4 * w + (i >> 1)) & 7));
      c.oB = rot * 64 * c.pitch; c.oA = rot * 128;
      dma<0>(c.rx, c.voffA, c.oA, c.ldsb + L1_A + w * 1024);
    }
    auto pro = [&](int s) {
      const unsigned lB = c.ldsb + L1_B + w * 3072, lA = c.ldsb + L1_A + w * 1024;
      dma<0>(c.rw, c.voffB, c.oB, lB + s * L1_BST);
      dma<128>(c.rw, c.voffB, c.oB, lB + s * L1_BST + 1024);
      dma<256>(c.rw, c.voffB, c.oB, lB + s * L1_BST + 2048);
      c.oB += 32 * c.pitch;
      dma<0>(c.rx, c.voffA, c.oA + ((s + 1) >> 1) * 128 + ((s + 1) & 1) * 81920, lA + (s + 1) * 8192);
      SB();
    };
#define PSPLIT 1
#pragma unroll
    for (int s = 0; s < PSPLIT; ++s) pro(s);
    unsigned vb[5], va0;
    {
      const int g = lane >> 4, q = (lane & 15) >> 2, p = lane & 3, r = lane & 15, nw = c.nw;
      const int swz = (q & 2) | ((g & 1) << 2);
#pragma unroll
      for (int pos = 0; pos < 5; ++pos) {
        const int cf = 16 * nw + 4 * p;
        const int hb = (cslot & 1 ? 0 : 32) + 40 * nw;
        const int ck = pos < 4 ? (pos >> 1) * 192 + hb + 20 * (pos & 1) + 4 * p : (p >> 1) * 192 + hb + 20 * (p & 1) + 16;
        const int cq = (nw >> 1) * 224 + 20 * (4 * (nw & 1) + 2 * (p & 1) + (p >> 1)) + 4 * pos;
        const int col = type == 0 ? cf : type == 1 ? ck : cq;
        const int line = col >> 6, seg = line >= 3;
        vb[pos] = c.ldsb + L1_B + seg * (12288 - 3072) + (g * 3 + line) * 1024 + q * 128 + 16 * (((col & 63) >> 3) ^ swz) + 8 * ((col >> 2) & 1);
      }
      va0 = c.ldsb + L1_A + (c.mw * 64 + r) * 128 + 16 * (g ^ ((r >> 1) & 7));
    }
    if (threadIdx.x < 16) ((LAS unsigned*)(lds + L2_FLAG))[threadIdx.x] = 0u;
    f32x4 acc[4][6];
#pragma unroll
    for (int m = 0; m < 4; ++m)
#pragma unroll
      for (int i = 0; i < 6; ++i) acc[m][i] = f32x4{0.f, 0.f, 0.f, 0.f};
    unsigned foff = lane >= 20 ? OFF_FR + (lane & 15) * 8 : OFF_F1 + (lane < 10 ? lane * 16 + cslot : Mt * 16 + lane - 10) * 8;
    PIN(foff);
#pragma unroll
    for (int i = 0; i < 5; ++i) PIN(vb[i]);
    SB();
#pragma unroll
    for (int s = PSPLIT; s < 3; ++s) pro(s);
    c.oA += 256;
    if (w == 7 && idx < 8) warm_code(pc0, ldsb + L1_GR + 8192, idx, lane);
    SB();
    WAIT_V(8);
    PIN(epoch);
    wt.tag = tag = make_tag(epoch);
#define P1_STAMP
#define P1_RUN(T) do { if (w < 4) p1_loop<T, true>(c, vb, va0, foff, acc, pf, ws, rws, tag, lane); \
                       else p1_loop<T, false>(c, vb, va0, foff, acc, pf, ws, rws, tag, lane); P1_STAMP; } while (0)
    if (type == 0) { P1_RUN(0); epi_ffn(lds, acc, ws, rws, wt, c, pf, lane); }
    else if (type == 2) { P1_RUN(2); epi_q(lds, acc, ws, rws, wt, c, pf, lane); }
    else {
      P1_RUN(1);
      epi_kv(lds, acc, ws, rws, tag, c, lane);
      STAMP(5);
      wd_prefetch<0, 6>(srd(ws, WS_END), ldsb, c.j, w, lane);
      pf.items<0, 28>();
    }
  }
  phase2(lds, x, ws, rws, out, wt, br, Mt * 2 + (slot2 >> 3), slot2 & 7, w, lane);
  STAMP(10);
}

extern "C" void kernel_launch(void* const* d_in, const int* in_sizes, int n_in, void* d_out, int out_size,
                              void* d_ws, size_t ws_size, hipStream_t stream) {
  (void)in_sizes; (void)n_in; (void)out_size; (void)ws_size;
  (void)hipFuncSetAttribute((const void*)fused, hipFuncAttributeMaxDynamicSharedMemorySize, LDS_SIZE);
  fused<<<256, 512, LDS_SIZE, stream>>>((const char*)d_in[0], (const char*)d_in[1], (const char*)d_in[2], (const char*)d_in[4],
                                        (const char*)d_in[5], (const char*)d_in[6], (char*)d_ws, (char*)d_out);
}
```

```cpp
#include <hip/hip_runtime.h>
#include <stdint.h>
#include <type_traits>

#define GAS __attribute__((address_space(1)))
#define LAS __attribute__((address_space(3)))
typedef __attribute__((ext_vector_type(2))) __bf16 bf16x2;
typedef __attribute__((ext_vector_type(8))) __bf16 bf16x8;
typedef __attribute__((ext_vector_type(4))) short s16x4;
typedef __attribute__((ext_vector_type(4))) float f32x4;
typedef __attribute__((ext_vector_type(2))) float f32x2;
typedef __attribute__((ext_vector_type(4))) unsigned u32x4;
typedef __attribute__((ext_vector_type(2))) unsigned u32x2;
typedef unsigned long long u64;
typedef __amdgpu_buffer_rsrc_t rsrc_t;

#define DEVI __device__ __forceinline__
#define WAIT_V_(n) asm volatile("s_waitcnt vmcnt(" #n ")" ::: "memory")
#define WAIT_V(n) WAIT_V_(n)
template <int N> __device__ __forceinline__ void wait_v() { asm volatile("s_waitcnt vmcnt(%0)" :: "i"(N) : "memory"); }
#define WAIT_L0() do { __builtin_amdgcn_s_waitcnt(0xC07F); asm volatile("" ::: "memory"); } while (0)
#define SB() __builtin_amdgcn_sched_barrier(0)
#define PIN(x) asm volatile("" : "+v"(x))
#define PINS(x) asm volatile("" : "+s"(x))
#define BAR() do { asm volatile("" ::: "memory"); __builtin_amdgcn_s_barrier(); asm volatile("" ::: "memory"); } while (0)
#define RLX_AGENT __ATOMIC_RELAXED, __HIP_MEMORY_SCOPE_AGENT
#define RLX_WG __ATOMIC_RELAXED, __HIP_MEMORY_SCOPE_WORKGROUP
#define SC1 16
#define OUT_AUX 0

#define STAMP(i)

constexpr int kD = 640, kF = 1728, kQKV = 1920;
constexpr unsigned OFF_E   = 0;
constexpr unsigned OFF_F1  = 256;
constexpr unsigned OFF_FR  = OFF_F1 + 2048;
constexpr unsigned OFF_FK  = 2560;
constexpr unsigned OFF_F3  = OFF_FK + 4096;
constexpr unsigned OFF_FH  = OFF_F3 + 1024;
constexpr unsigned OFF_FZ  = 16384;
constexpr unsigned OFF_ZP  = OFF_FZ + 4096;
constexpr unsigned OFF_SC  = OFF_ZP + 4 * 32 * 640;
constexpr unsigned OFF_SR  = OFF_SC + 15 * 10 * 384 * 4;
constexpr unsigned OFF_KVP = OFF_SR + 16 * 10 * 256 * 4;
constexpr unsigned OFF_MP  = OFF_KVP + 16 * 16 * 3456;
constexpr unsigned OFF_WD  = OFF_MP + 8 * 20 * 5 * 1024;
constexpr unsigned OFF_Q   = OFF_WD + 8 * 54 * 5 * 1024;
constexpr unsigned OFF_H   = OFF_Q + 32 * 20 * 4096;
constexpr unsigned WS_END  = OFF_H + 32 * 54 * 4096;
constexpr unsigned OOB = 0x80000000u;
static_assert(OFF_FH + 8192 <= OFF_FZ && OFF_MP % 1024 == 0 && OFF_KVP % 128 == 0, "align");

constexpr int L1_B = 0, L1_BST = 24576;
constexpr int L1_A = 4 * L1_BST, L1_AST = 16384;
constexpr int L1_GR = L1_A + 3 * L1_AST;
constexpr int L1_GC = L1_A + L1_AST;
constexpr int L1_ST = 158720;
constexpr int L1_CS = L1_ST + 1024;
constexpr int L1_EX = 72 * 1024;
constexpr int L2_BASE = 120 * 1024;
constexpr int L2_RED = 150 * 1024;
constexpr int L2_FLAG = 162816;
constexpr int LDS_SIZE = L2_FLAG + 64;
enum { LF_STATUS = 0, LF_DMA = 1, LF_K = 2, LF_X = 3, LF_BC = 4, LF_PUB = 8 };
enum { S_H = 1, S_Q = 2, S_M = 4 };

DEVI f32x4 mfma(u32x4 a, u32x4 b, f32x4 c) {
  return __builtin_amdgcn_mfma_f32_16x16x32_bf16(__builtin_bit_cast(bf16x8, a), __builtin_bit_cast(bf16x8, b), c, 0, 0, 0);
}
DEVI void mfma_acc(f32x4& c, u32x4 a, u32x4 b) { asm volatile("v_mfma_f32_16x16x32_bf16 %0, %1, %2, %0" : "+v"(c) : "v"(a), "v"(b)); }
DEVI void mfma_new(f32x4& c, u32x4 a, u32x4 b) { asm volatile("v_mfma_f32_16x16x32_bf16 %0, %1, %2, 0" : "=&v"(c) : "v"(a), "v"(b)); }
DEVI unsigned pk2(f32x2 f) { return __builtin_bit_cast(unsigned, __builtin_convertvector(f, bf16x2)); }
DEVI unsigned pk2(float lo, float hi) { return pk2(f32x2{lo, hi}); }
DEVI f32x2 unpk(unsigned u) { return f32x2{__builtin_bit_cast(float, u << 16), __builtin_bit_cast(float, u & 0xffff0000u)}; }
DEVI f32x2 fma2(f32x2 a, f32x2 b, f32x2 c) { return __builtin_elementwise_fma(a, b, c); }
DEVI f32x2 bc2(float v) { return f32x2{v, v}; }
DEVI f32x2 lo2(f32x4 v) { return f32x2{v[0], v[1]}; }
DEVI f32x2 hi2(f32x4 v) { return f32x2{v[2], v[3]}; }
DEVI f32x2 phi2(f32x2 g, f32x2 rs) {
  const f32x2 t = fma2(g, rs, bc2(1e-6f)), l = g * (rs * 1.4426950408889634f);
  return f32x2{fmaxf(t[0], 1e-6f), fmaxf(t[1], 1e-6f)} +
         f32x2{__builtin_amdgcn_fmed3f(__builtin_amdgcn_exp2f(l[0]), 0.f, 1.f), __builtin_amdgcn_fmed3f(__builtin_amdgcn_exp2f(l[1]), 0.f, 1.f)};
}
DEVI u32x4 lds_b128(unsigned a) { return *(LAS u32x4*)(size_t)a; }
DEVI u32x4 lds_trfrag(unsigned a) {
  const u32x2 lo = __builtin_bit_cast(u32x2, __builtin_amdgcn_ds_read_tr16_b64_v4i16((LAS s16x4*)(size_t)a));
  const u32x2 hi = __builtin_bit_cast(u32x2, __builtin_amdgcn_ds_read_tr16_b64_v4i16((LAS s16x4*)(size_t)(a + 512)));
  return u32x4{lo[0], lo[1], hi[0], hi[1]};
}
DEVI u32x4 srd(const void* p, unsigned bytes) {
  const u64 a = (u64)p;
  return u32x4{(unsigned)a, (unsigned)(a >> 32) & 0xffffu, bytes, 0x00020000u};
}
DEVI void setm0(unsigned v) { asm volatile("s_mov_b32 m0, %0" :: "s"(v) : "memory"); }
template <int IMM>
DEVI void dma_n(u32x4 r, unsigned voff, unsigned soff) {
  asm volatile("buffer_load_dwordx4 %0, %1, %2 offen offset:%3 lds" :: "v"(voff), "s"(r), "s"(soff), "i"(IMM) : "memory");
}
template <int IMM>
DEVI void dma(u32x4 r, unsigned voff, unsigned soff, unsigned lds) {
  asm volatile("s_mov_b32 m0, %4\n\ts_nop 0\n\tbuffer_load_dwordx4 %0, %1, %2 offen offset:%3 lds"
               :: "v"(voff), "s"(r), "s"(soff), "i"(IMM), "s"(lds - IMM) : "memory");
}
DEVI void dma_sc1(u32x4 r, unsigned voff, unsigned soff, unsigned lds) {
  asm volatile("s_mov_b32 m0, %3\n\ts_nop 0\n\tbuffer_load_dwordx4 %0, %1, %2 offen sc1 lds"
               :: "v"(voff), "s"(r), "s"(soff), "s"(lds) : "memory");
}
DEVI void ld8_hidden(u32x2& v, u32x4 r, unsigned voff) {
  asm volatile("buffer_load_dwordx2 %0, %1, %2, 0 offen sc1" : "+v"(v) : "v"(voff), "s"(r) : "memory");
}
template <int N0, int N1>
DEVI void wd_prefetch(u32x4 rws, unsigned ldsb, int j, int w, int lane) {
#pragma unroll
  for (int n = N0; n < N1; ++n) {
    const unsigned a = (unsigned)(w + 8 * n) * 1024;
    if (n < 18 || w < 6) dma_sc1(rws, lane * 16, OFF_WD + j * 276480 + a, ldsb + a);
  }
}
DEVI void wait_flags(char* ws, unsigned off, u64 tag) {
#pragma unroll 1
  for (unsigned spins = 0; spins < (1u << 20); ++spins) {
    const u64 v = __hip_atomic_load((GAS u64*)(ws + off), RLX_AGENT);
    if (__all(v == tag)) break;
    __builtin_amdgcn_s_sleep(1);
  }
  asm volatile("" ::: "memory");
}
DEVI u64 make_tag(u32x2 e) {
  const u64 v = (u64)(unsigned)__builtin_amdgcn_readfirstlane(e[0]) | (u64)(unsigned)__builtin_amdgcn_readfirstlane(e[1]) << 32;
  return v * 0x9E3779B97F4A7C15ull + 0xD1B54A32D192ED03ull;
}
DEVI void set_flag(char* ws, unsigned off, u64 tag) { __hip_atomic_store((GAS u64*)(ws + off), tag, RLX_AGENT); }
DEVI unsigned lds_inc(LAS unsigned* p, int lane) {
  unsigned o = 0;
  if (lane == 0) o = __hip_atomic_fetch_add(p, 1u, RLX_WG);
  return __builtin_amdgcn_readfirstlane(o);
}
DEVI void lds_wait_bits(LAS unsigned* p, unsigned bits) {
  while ((~(unsigned)__builtin_amdgcn_readfirstlane(__hip_atomic_load(p, RLX_WG)) & bits) != 0u) __builtin_amdgcn_s_sleep(1);
  asm volatile("" ::: "memory");
}
DEVI void lds_wait(LAS unsigned* p, unsigned want) {
  while ((unsigned)__builtin_amdgcn_readfirstlane(__hip_atomic_load(p, RLX_WG)) < want) __builtin_amdgcn_s_sleep(1);
  asm volatile("" ::: "memory");
}
DEVI u32x4 ld16(rsrc_t r, unsigned voff, unsigned soff) { return __builtin_amdgcn_raw_buffer_load_b128(r, voff, soff, SC1); }
DEVI void st16(rsrc_t r, unsigned voff, unsigned soff, u32x4 v) { __builtin_amdgcn_raw_buffer_store_b128(v, r, voff + soff, 0, SC1); }
DEVI void st8(rsrc_t r, unsigned voff, unsigned soff, u32x2 v) { __builtin_amdgcn_raw_buffer_store_b64(v, r, voff, soff, SC1); }

constexpr unsigned CODE_BYTES = 60 * 1024;
DEVI void warm_code(const char* pc0, unsigned junk, int chunk, int lane) {
  asm volatile("s_mov_b32 m0, %3\n\ts_nop 0\n\tbuffer_load_dword %0, %1, %2 offen lds"
               :: "v"(lane << 7), "s"(srd(pc0, CODE_BYTES)), "s"(chunk * 8192), "s"(junk) : "memory");
}

struct P1 {
  u32x4 rw, rx;
  unsigned ldsb, pitch;
  unsigned voffB, voffA;
  unsigned oB, oA;
  int w, mw, nw, bstar;
  int Mt, cslot, j;
};

struct PF2 {
  char* ws; rsrc_t rws; unsigned ldsb; int j, w, lane, r0;
  u32x4 (&br)[3][5];
  template <int N> DEVI void item() {
    if constexpr (N < 13) wd_prefetch<6 + N, 7 + N>(srd(ws, WS_END), ldsb, j, w, lane);
    else br[(N - 13) / 5][(N - 13) % 5] = ld16(rws, lane * 16, OFF_WD + (j * 54 + 30 + w + 8 * (((N - 13) / 5 + r0) % 3)) * 5120 + ((N - 13) % 5) * 1024);
    SB();
  }
  template <int N0, int N1> DEVI void items() { if constexpr (N0 < N1) { item<N0>(); items<N0 + 1, N1>(); } }
};

#define SPR 1
#define EB 3
#define AINC 1
template <int TYPE, bool LATE>
DEVI void p1_loop(P1& c, unsigned (&vb)[5], unsigned va0, unsigned foff, f32x4 (&acc)[4][6], PF2& pf, char* ws, rsrc_t rws, u64 tag, int lane) {
  constexpr int NT = TYPE == 0 ? 6 : 5;
  constexpr bool AC = !LATE || AINC;
  u32x4 fa[4], fb[NT];
  unsigned va1 = va0 ^ 64;
  const unsigned stepB = 32 * c.pitch, limB = 640 * c.pitch;
  const unsigned lB0 = c.ldsb + L1_B + c.w * 3072, lA0 = c.ldsb + L1_A + c.w * 1024;
  unsigned lB = lB0 + 3 * L1_BST, lA = lA0 + 2 * L1_AST;
  unsigned vB1 = c.voffB - 896, vB2 = c.voffB - 1792, vA1 = c.voffA + 81920;
  PIN(vB1); PIN(vB2); PIN(vA1);
  int dA = L1_AST, dB = 2 * L1_BST, rare = c.bstar, rare2 = 1;
  f32x4 st[5];
  auto adB = [&](int i) { return TYPE == 0 ? vb[!LATE && i >= 2 ? 1 : 0] + (i / 3) * 12288 + (i % 3) * 1024 : vb[i < 5 ? i : 4]; };
  if (TYPE == 0) { vb[1] = vb[0]; PIN(vb[1]); }
  auto upB = [&](int i0, int i1) {
    if (TYPE == 0) { vb[i0 ? 1 : 0] += dB; PIN(vb[i0 ? 1 : 0]); }
    else {
#pragma unroll
      for (int i = i0; i < i1; ++i) { vb[i] += dB; PIN(vb[i]); }
    }
  };
  auto rdA = [&](int i, int sub) { fa[i] = lds_b128((sub ? va1 : va0) + i * 2048); };
  auto rdB = [&](int i, int sub) { fb[i] = lds_trfrag(adB(i) + sub * L1_BST); };
  auto rdBh = [&](int i, int hh, int sub) {
    const u32x2 v = __builtin_bit_cast(u32x2, __builtin_amdgcn_ds_read_tr16_b64_v4i16((LAS s16x4*)(size_t)(adB(i) + sub * L1_BST + hh * 512)));
    fb[i][2 * hh] = v[0]; fb[i][2 * hh + 1] = v[1];
  };
  auto pieceA = [&](int sub) { dma_n<0>(c.rx, sub ? vA1 : c.voffA, c.oA); };
  auto L = [&](int sub, int mode) {
    if (mode) dma_n<0>(c.rw, c.voffB, c.oB);
    SB(); rdA(0, sub); rdA(1, sub); SB();
    if (mode) dma_n<1024>(c.rw, vB1, c.oB);
    SB(); rdA(2, sub); rdA(3, sub); SB();
    if (mode) dma_n<2048>(c.rw, vB2, c.oB);
    SB(); rdB(LATE ? 4 : 0, sub); SB();
    if (mode == 2) setm0(lA + sub * 8192);
    SB(); if (!LATE) rdB(1, sub); else if (NT == 6) rdB(NT - 1, sub); else if (mode == 2) asm volatile("s_nop 0"); SB();
    if (mode == 2) pieceA(sub);
    if (!LATE && !sub) upB(2, 5);
    SB();
  };
  auto U = [&](int sub) {
    if (sub) { va1 += dA; PIN(va1); upB(0, LATE ? 5 : 2); }
    else { va0 += dA; PIN(va0); }
    SB();
  };
  auto stats = [&](int sub) {
    const u32x4 one = {0x3f803f80u, 0x3f803f80u, 0x3f803f80u, 0x3f803f80u};
    const u32x4 sa = lds_b128((sub ? va1 : va0) + c.nw * 2048);
    u32x4 sb[3];
#pragma unroll
    for (int i = 0; i < 3; ++i) { unsigned a = adB(LATE ? i : 3 + i); PIN(a); sb[i] = lds_trfrag(a + sub * L1_BST); }
    SB();
    if (sub == 0) {
#pragma unroll
      for (int i = 0; i < 5; ++i) st[i] = f32x4{0.f, 0.f, 0.f, 0.f};
    }
    st[0] = mfma(one, sa, st[0]);
    st[1] = mfma(sa, sa, st[1]);
#pragma unroll
    for (int i = 0; i < 3; ++i) st[2 + i] = mfma(one, sb[i], st[2 + i]);
    if (sub == 1) {
      const int cl = lane & 15, g = lane >> 4;
      const float sq = (cl & 2) ? ((cl & 1) ? st[1][3] : st[1][2]) : ((cl & 1) ? st[1][1] : st[1][0]);
      st8(rws, (cl >> 2) == g ? (c.mw * 64 + c.nw * 16 + cl) * 8 : OOB, c.cslot < 10 ? OFF_SR + (c.Mt * 10 + c.cslot) * 1024 : WS_END,
          u32x2{__builtin_bit_cast(unsigned, st[0][0]), __builtin_bit_cast(unsigned, sq)});
      const float v = g == 0 ? st[2][0] : g == 1 ? st[3][0] : st[4][0];
      __builtin_amdgcn_raw_buffer_store_b32(__builtin_bit_cast(unsigned, v), rws, g < 3 ? ((c.nw * 6 + c.mw * 3) * 16 + lane) * 4 : OOB,
                                            c.Mt < 10 ? OFF_SC + (c.cslot * 10 + c.Mt) * 1536 : WS_END, SC1);
    }
    SB();
  };
#define SEG(i)
  auto C = [&](int sub, bool more, int book, auto vmwait) {
    SEG(0);
    __builtin_amdgcn_s_setprio(1);
    if (LATE) { vmwait(); WAIT_L0(); BAR(); }
    SB();
#pragma unroll
    for (int k = 0; k < 4 * NT; ++k) {
      const int i = k >> 2, m = k & 3, ms = (i & 1) ? 3 - m : m;
      acc[ms][i] = TYPE == 1 ? mfma(fa[ms], fb[i], acc[ms][i]) : mfma(fb[i], fa[ms], acc[ms][i]);
      if (LATE) { if (more && i >= 1 && i < 5 && m < 2) rdBh(i - 1, m, sub ^ 1); }
      else if (k < 2 * (NT - 2)) rdBh(2 + (k >> 1), k & 1, sub);
      const int q = LATE ? (k < 2 ? k : m >= 2 && i < 5 ? 2 * i + m : -1) : k - 2 * (NT - 2);
      constexpr int QA = LATE ? 8 : 0, QU = LATE ? 0 : 3;
      if (book == 1) {
        if (AC) {
          if (q == QA) setm0(lA + (LATE ? sub ^ 1 : sub) * 8192);
          if (q == QA + 1) pieceA(LATE ? sub ^ 1 : sub);
        }
        if (q == QU) { c.oB += stepB; lB += L1_BST; PINS(c.oB); PINS(lB); }
        if (q == QU + 1) { c.oB = c.oB == limB ? 0u : c.oB; PINS(c.oB); }
        if (q == QU + 2) { lB = lB == lB0 + 4 * L1_BST ? lB0 : lB; PINS(lB); }
        if (sub) {
          if (q == QU + 3) { c.oA += 128; lA += L1_AST; PINS(c.oA); PINS(lA); }
          if (q == QU + 4) { c.oA = c.oA == 1280u ? 0u : c.oA; PINS(c.oA); }
          if (q == QU + 5) { lA = lA == lA0 + 3 * L1_AST ? lA0 : lA; PINS(lA); }
          if (q == QU + 6) { dA = lA == lA0 + L1_AST ? -2 * L1_AST : L1_AST; PINS(dA); }
          if (q == QU + 7) { rare -= 1; PINS(rare); }
        }
        if (!sub && q == QU + 3) { rare2 = LATE ? (rare + 2) & ~2 : rare; PINS(rare2); }
        if (q == 11) setm0(lB);
      }
      if (book && q == 10 && (LATE ? sub : !sub)) { dB = -dB; PINS(dB); }
      if (!LATE && EB > 0) {
        if (k == 4 * NT - 2 - EB) { vmwait(); WAIT_L0(); }
        if (k == 4 * NT - 1 - EB) { __builtin_amdgcn_s_setprio(2); BAR(); }
      }
      SB();
    }
    __builtin_amdgcn_s_setprio(0);
    if (!LATE && EB == 0) { vmwait(); WAIT_L0(); BAR(); }
  };
  constexpr int LM = AC ? 1 : 2;
  if (!LATE) { upB(2, 5); dB = -dB; }
  BAR();
  if (LATE) {
    if (AC) { setm0(lA); SB(); }
    rdB(0, 0); rdB(1, 0); SB();
    if (AC) pieceA(0);
  }
  setm0(lB);
  SB();
  if (LATE) { rdB(2, 0); rdB(3, 0); } else asm volatile("s_nop 0");
  SB();
  constexpr int X = TYPE == 0;
  const int rend = c.bstar - (X ? 7 : 8);
  const u32x4 rw4 = srd(ws, WS_END);
  u32x2 fl = {0u, 0u};
#pragma unroll 1
  do {
    if (X && __builtin_expect(rare == rend + 1, 0)) ld8_hidden(fl, rw4, foff);
    L(0, LM);
    if (__builtin_expect(rare == 0, 0)) stats(0);
    U(0);
    C(0, true, 1, []() { WAIT_V(8); });
    L(1, LM);
    if (__builtin_expect(rare2 == 0, 0)) {
      PINS(rare);
      if (rare == 0) stats(1);
      else if (LATE && c.w == 0 && lane == 0) set_flag(ws, OFF_F1 + (c.Mt * 16 + c.cslot) * 8, tag);
    }
    U(1);
    C(1, true, 1, []() { WAIT_V(8); });
  } while (rare != rend);
  auto check = [&]() {
    PIN(fl);
    if (__builtin_expect(!__all(__builtin_bit_cast(u64, fl) == tag), 0)) wait_flags(ws, foff, tag);
  };
  if (X) {
    L(0, LM);
    WAIT_V(11);
    check();
    pf.items<13, 16>();
    U(0);
    C(0, true, 1, []() { WAIT_V(11); });
    L(1, LM);
    pf.items<16, 19>();
    U(1);
    C(1, true, 1, []() { WAIT_V(14); });
  }
  if (!X) ld8_hidden(fl, rw4, foff);
  L(0, 1);
  if (X) pf.items<19, 22>();
  U(0);
  C(0, true, 2, []() { wait_v<X ? 13 : 8>(); });
  L(1, 0);
  wait_v<X ? 6 : 3>();
  if (!X) check();
  dma_sc1(rw4, lane * 16, OFF_SC + c.cslot * 15360 + c.w * 1024, c.ldsb + L1_GC + c.w * 1024);
  dma_sc1(rw4, lane * 16, OFF_SC + c.cslot * 15360 + c.w * 1024 + 8192, c.ldsb + L1_GC + c.w * 1024 + 8192);
  dma_sc1(rw4, lane * 16, OFF_SR + c.Mt * 10240 + c.w * 1024, c.ldsb + L1_GR + c.w * 1024);
  if (LATE && c.w < 2) dma_sc1(rw4, lane * 16, OFF_SR + c.Mt * 10240 + c.w * 1024 + 8192, c.ldsb + L1_GR + c.w * 1024 + 8192);
  if (X) pf.items<22, 24>();
  U(1);
  C(1, true, 2, []() {});
  L(0, 0);
  if (TYPE != 1) wd_prefetch<0, 4>(rw4, c.ldsb, c.j, c.w, lane);
  if (X) pf.items<24, 26>();
  C(0, true, 0, []() { wait_v<X ? 8 : TYPE != 1 ? 4 : 0>(); });
  L(1, 0);
  {
    __builtin_amdgcn_s_setprio(2);
    if (LATE || c.w < 6) {
      const LAS float* src = (const LAS float*)(size_t)(c.ldsb + L1_GC + (c.w * 64 + lane) * 4);
      float v[10];
#pragma unroll
      for (int p = 0; p < 10; ++p) v[p] = src[p * 384];
      *(LAS float*)(size_t)(c.ldsb + L1_CS + (c.w * 64 + lane) * 4) = (((v[0] + v[1]) + (v[2] + v[3])) + ((v[4] + v[5]) + (v[6] + v[7]))) + (v[8] + v[9]);
    } else {
      const LAS f32x2* src = (const LAS f32x2*)(size_t)(c.ldsb + L1_GR + ((c.w - 6) * 64 + lane) * 8);
      f32x2 v[10];
#pragma unroll
      for (int p = 0; p < 10; ++p) v[p] = src[p * 128];
      const f32x2 s = ((((v[0] + v[1]) + (v[2] + v[3])) + ((v[4] + v[5]) + (v[6] + v[7]))) + (v[8] + v[9])) * (1.0f / kD);
      LAS float* d = (LAS float*)(size_t)(c.ldsb + L1_ST + ((c.w - 6) * 64 + lane) * 4);
      d[0] = s[0]; d[128] = __builtin_amdgcn_rsqf(fmaxf(__builtin_fmaf(-s[0], s[0], s[1]), 0.f) + 1e-5f);
    }
    __builtin_amdgcn_s_setprio(0);
  }
  SB();
  if (TYPE != 1) wd_prefetch<4, 6>(rw4, c.ldsb, c.j, c.w, lane);
  if (X) pf.items<26, 28>();
  SB();
  C(1, false, 0, []() {});
}

template <int N, class F> DEVI void static_for(F&& f) {
  if constexpr (N > 0) { static_for<N - 1>(f); f(std::integral_constant<int, N - 1>{}); }
}
DEVI u32x4 pair16(u32x2 a, u32x2 b) {
  const auto s0 = __builtin_amdgcn_permlane32_swap(a[0], b[0], false, false);
  const auto s1 = __builtin_amdgcn_permlane32_swap(a[1], b[1], false, false);
  return u32x4{s0[0], s1[0], s0[1], s1[1]};
}
struct Watch {
  LAS unsigned* lf; char* ws; u64 tag; int mt2, j, w, lane;
  unsigned status;
  DEVI u64 fetch() {
    int l2 = lane;
    PIN(l2);
    const unsigned f0 = l2 < 32 ? OFF_FH + mt2 * 256 + (j >> 2) * 128 + (l2 < 11 ? l2 : 0) * 8 : OFF_F3 + (blockIdx.x & 7) * 128 + (l2 & 15) * 8;
    return __hip_atomic_load((GAS u64*)(ws + f0), RLX_AGENT);
  }
  DEVI bool eval(u64 v, bool giveup) {
    const u64 no = __ballot(v != tag);
    unsigned s = ((no & 0x1ffull) ? 0u : S_H) | ((no & 0x600ull) ? 0u : S_Q) | ((no >> 32) ? 0u : S_M);
    if (giveup) s = 7u;
    if (s == status) return false;
    status = s;
    __hip_atomic_store(lf + LF_STATUS, s, RLX_WG);
    return true;
  }
  DEVI bool poll(bool giveup) { return eval(fetch(), giveup); }
  DEVI bool has(unsigned bits) { return (~(unsigned)__builtin_amdgcn_readfirstlane(__hip_atomic_load(lf + LF_STATUS, RLX_WG)) & bits) == 0u; }
  int pcnt, plast, preps; unsigned pflag;
  DEVI void publish() {
    if ((int)lds_inc(lf + LF_PUB + pcnt, lane) == plast && lane < preps) set_flag(ws, pflag + lane * 128, tag);
    preps = 0;
  }
  DEVI void need(unsigned bits, int watcher = 7) {
    if (w != watcher) { lds_wait_bits(lf + LF_STATUS, bits); return; }
#pragma unroll 1
    for (unsigned spins = 0; (~status & bits) != 0u; ++spins)
      if (!poll(spins >= (1u << 20))) __builtin_amdgcn_s_sleep(1);
    asm volatile("" ::: "memory");
  }
};

#define EPI_N 12
DEVI void epi_ffn(LAS char* lds, f32x4 (&acc)[4][6], char* ws, rsrc_t rws, Watch& wt, const P1& c, PF2& pf, int lane) {
  const int r = lane & 15, g = lane >> 4;
  pf.items<0, 2>();
  float nm[4], k1[4], k2[4], iv[4];
#pragma unroll
  for (int mi = 0; mi < 4; ++mi) {
    const LAS float* sp = (const LAS float*)(lds + L1_ST) + c.mw * 64 + mi * 16 + r;
    const float rs = sp[128], c0 = -2.0f * 0.7978845608028654f * 1.4426950408889634f;
    nm[mi] = -sp[0]; k1[mi] = c0 * rs; k2[mi] = c0 * 0.044715f * rs * rs * rs; iv[mi] = __builtin_amdgcn_rcpf(rs * rs);
  }
  const unsigned hb = OFF_H + ((c.Mt * 2 + c.mw) * 54 + 2 * c.cslot + (c.nw >> 1)) * 4096 + (c.nw & 1) * 512 + (lane & 31) * 16 + (lane >> 5) * 1024;
  u32x4 v[6];
  static_for<3>([&](auto I) {
    constexpr int i = I;
    const f32x4 cg = *(LAS f32x4*)(lds + L1_CS + ((c.nw * 6 + i) * 16 + 4 * g) * 4);
    const f32x4 cu = *(LAS f32x4*)(lds + L1_CS + ((c.nw * 6 + 3 + i) * 16 + 4 * g) * 4);
    u32x2 o[4];
    static_for<4>([&](auto MI) {
      constexpr int mi = MI, n = 4 * i + mi;
      if constexpr (2 + 2 * n < EPI_N) pf.template items<2 + 2 * n, 4 + 2 * n>();
#pragma unroll
      for (int hh = 0; hh < 2; ++hh) {
        const f32x2 G = fma2(bc2(nm[mi]), hh ? hi2(cg) : lo2(cg), hh ? hi2(acc[mi][i]) : lo2(acc[mi][i]));
        const f32x2 U = fma2(bc2(nm[mi]), hh ? hi2(cu) : lo2(cu), hh ? hi2(acc[mi][3 + i]) : lo2(acc[mi][3 + i]));
        const f32x2 z = G * fma2(G * G, bc2(k2[mi]), bc2(k1[mi]));
        const f32x2 d = fma2(f32x2{__builtin_amdgcn_exp2f(z[0]), __builtin_amdgcn_exp2f(z[1])}, bc2(iv[mi]), bc2(iv[mi]));
        o[mi][hh] = pk2(G * U * f32x2{__builtin_amdgcn_rcpf(d[0]), __builtin_amdgcn_rcpf(d[1])});
      }
      PIN(o[mi]);
      SB();
#define STN 4
#define STS 4
      if constexpr (n == STN || n == STN + STS) {
        constexpr int p = (n - STN) / STS;
        st16(rws, hb, p * 18 * 4096, v[2 * p]);
        st16(rws, hb, p * 18 * 4096 + 2048, v[2 * p + 1]);
        SB();
      }
    });
    v[2 * i] = pair16(o[0], o[1]); v[2 * i + 1] = pair16(o[2], o[3]);
    PIN(v[2 * i]); PIN(v[2 * i + 1]);
  });
  st16(rws, hb, 36 * 4096, v[4]);
  st16(rws, hb, 36 * 4096 + 2048, v[5]);
  STAMP(4);
  BAR();
  WAIT_V(0);
  wt.pcnt = c.mw; wt.plast = 3; wt.preps = 2; wt.pflag = OFF_FH + (c.Mt * 2 + c.mw) * 256 + c.cslot * 8;
  wt.publish();
  STAMP(5);
  pf.items<EPI_N, 13>();
  WAIT_V(0);
}

DEVI f32x4 sweep8(rsrc_t rws, unsigned voff, unsigned soff, unsigned stride, unsigned tag) {
  u32x4 gr[8];
#pragma unroll 1
  for (unsigned spins = 0; spins < (1u << 20); ++spins) {
#pragma unroll
    for (int p = 0; p < 8; ++p) gr[p] = ld16(rws, voff, soff + p * stride);
    SB();
    unsigned bad = 0;
#pragma unroll
    for (int p = 0; p < 8; ++p) bad |= (gr[p][1] ^ tag) | (gr[p][3] ^ tag);
    PIN(bad);
    if (__all(bad == 0u || voff == OOB)) break;
    asm volatile("" ::: "memory");
  }
  f32x2 a[8], b[8];
#pragma unroll
  for (int p = 0; p < 8; ++p) { a[p] = unpk(gr[p][0]); b[p] = unpk(gr[p][2]); }
  const f32x2 sa = ((a[0] + a[1]) + (a[2] + a[3])) + ((a[4] + a[5]) + (a[6] + a[7]));
  const f32x2 sb = ((b[0] + b[1]) + (b[2] + b[3])) + ((b[4] + b[5]) + (b[6] + b[7]));
  return f32x4{sa[0], sa[1], sb[0], sb[1]};
}

DEVI void epi_q(LAS char* lds, f32x4 (&acc)[4][6], char* ws, rsrc_t rws, Watch& wt, const P1& c, PF2& pf, int lane) {
  const int r = lane & 15, g = lane >> 4, qi = c.cslot - 13;
  f32x4 cs[5];
#pragma unroll
  for (int i = 0; i < 5; ++i) cs[i] = *(LAS f32x4*)(lds + L1_CS + ((c.nw * 6 + i) * 16 + 4 * g) * 4);
  pf.items<0, 8>();
  static_for<4>([&](auto MI) {
    constexpr int mi = MI;
    const LAS float* sp = (const LAS float*)(lds + L1_ST) + c.mw * 64 + mi * 16 + r;
    const f32x2 nm = bc2(-sp[0]), rs = bc2(sp[128]);
    static_for<5>([&](auto I) {
      constexpr int i = I;
      const f32x2 a = phi2(fma2(nm, lo2(cs[i]), lo2(acc[mi][i])), rs), b = phi2(fma2(nm, hi2(cs[i]), hi2(acc[mi][i])), rs);
      acc[mi][i] = f32x4{a[0], a[1], b[0], b[1]};
      PIN(acc[mi][i]);
      SB();
      pf.template item<8 + mi * 5 + i>();
    });
  });
  STAMP(3);
  LAS unsigned* lf = (LAS unsigned*)(lds + L2_FLAG);
  if (c.w == 4) { wait_flags(ws, OFF_FZ + (blockIdx.x & 7) * 512 + qi * 256 + (lane & 31) * 8, wt.tag); lds_inc(lf + LF_K, lane); }
  else lds_wait(lf + LF_K, 1u);
  STAMP(12);
  {
    const int q4 = (c.w & 1) * 64 + lane;
    const f32x4 zs = sweep8(rws, q4 < 40 ? q4 * 16 : q4 < 80 ? q4 * 16 + 31 * 640 : OOB, OFF_ZP + (qi * 64 + (c.w >> 1) * 8) * 640, 640, (unsigned)wt.tag);
    if (q4 < 80) *(LAS f32x4*)(lds + L2_RED + (c.w >> 1) * 1280 + q4 * 16) = zs;
    WAIT_L0(); BAR();
  }
  u64 fv = 0;
  STAMP(5);
  f32x4 z[5];
#pragma unroll
  for (int i = 0; i < 5; ++i) {
    const LAS char* p = lds + L2_RED + (c.nw * 80 + 20 * (2 * (g & 1) + (g >> 1)) + 4 * i) * 4;
    z[i] = (*(const LAS f32x4*)p + *(const LAS f32x4*)(p + 1280)) + (*(const LAS f32x4*)(p + 2560) + *(const LAS f32x4*)(p + 3840));
  }
  const unsigned qb = OFF_Q + ((c.Mt * 2 + c.mw) * 20 + (2 * qi + (c.nw >> 1)) * 5) * 4096;
  u32x2 keep;
#pragma unroll
  for (int mi = 0; mi < 4; ++mi) {
    f32x2 d0 = lo2(acc[mi][0]) * lo2(z[0]), d1 = hi2(acc[mi][0]) * hi2(z[0]);
#pragma unroll
    for (int i = 1; i < 5; ++i) { d0 = fma2(lo2(acc[mi][i]), lo2(z[i]), d0); d1 = fma2(hi2(acc[mi][i]), hi2(z[i]), d1); }
    d0 += d1;
    const f32x2 inv = bc2(__builtin_amdgcn_rcpf(d0[0] + d0[1] + 1e-6f));
    unsigned o[10];
#pragma unroll
    for (int i = 0; i < 5; ++i) { o[2 * i] = pk2(lo2(acc[mi][i]) * inv); o[2 * i + 1] = pk2(hi2(acc[mi][i]) * inv); }
    st16(rws, lane * 16 + mi * 1024, qb + (c.nw & 1) * 12288, u32x4{o[0], o[1], o[2], o[3]});
    st16(rws, lane * 16 + mi * 1024, qb + (c.nw & 1) * 12288 + 4096, u32x4{o[4], o[5], o[6], o[7]});
    if (mi & 1) st16(rws, (lane & 31) * 16 + (lane >> 5) * 1024 + (mi - 1) * 1024, qb + 8192 + (c.nw & 1) * 512, pair16(keep, u32x2{o[8], o[9]}));
    else keep = u32x2{o[8], o[9]};
    if (mi == 0 && c.w == 7) { SB(); fv = wt.fetch(); SB(); }
  }
  STAMP(4);
  SB();
  if (c.w == 7) wt.eval(fv, false);
  WAIT_L0(); BAR();
  wt.pcnt = c.mw; wt.plast = 3; wt.preps = 2; wt.pflag = OFF_FH + (c.Mt * 2 + c.mw) * 256 + (9 + qi) * 8;
}

DEVI void epi_kv(LAS char* lds, f32x4 (&acc)[4][6], char* ws, rsrc_t rws, u64 tag, const P1& c, int lane) {
  const int cl = lane & 15, g = lane >> 4, kj = c.cslot - 9;
  float cs[5];
#pragma unroll
  for (int i = 0; i < 5; ++i) cs[i] = -*(LAS float*)(lds + L1_CS + ((c.nw * 6 + i) * 16 + cl) * 4);
  f32x4 mean[4], rstd[4];
#pragma unroll
  for (int mi = 0; mi < 4; ++mi) {
    mean[mi] = *(LAS f32x4*)(lds + L1_ST + (c.mw * 64 + mi * 16 + 4 * g) * 4);
    rstd[mi] = *(LAS f32x4*)(lds + L1_ST + 512 + (c.mw * 64 + mi * 16 + 4 * g) * 4);
  }
  u32x4 fk[2][3], fv[2][3];
  auto center = [&](int mi, int i, int hh) { return fma2(hh ? hi2(mean[mi]) : lo2(mean[mi]), bc2(cs[i]), hh ? hi2(acc[mi][i]) : lo2(acc[mi][i])); };
#pragma unroll
  for (int mi = 0; mi < 4; ++mi)
#pragma unroll
    for (int f = 0; f < 3; ++f)
#pragma unroll
      for (int hh = 0; hh < 2; ++hh)
        fk[mi >> 1][f][(mi & 1) * 2 + hh] = pk2(phi2(center(mi, f < 2 ? f : 4, hh), hh ? hi2(rstd[mi]) : lo2(rstd[mi])));
  const int uh = cl < 4 ? 16 + cl : 32 + cl;
  const f32x4 zero = {0.f, 0.f, 0.f, 0.f};
  {
    const u32x4 one = {0x3f803f80u, 0x3f803f80u, 0x3f803f80u, 0x3f803f80u};
    f32x4 z[3];
#pragma unroll
    for (int f = 0; f < 3; ++f) z[f] = mfma(one, fk[1][f], mfma(one, fk[0][f], zero));
    const float v = g == 0 ? z[0][0] : g == 1 ? z[1][0] : z[2][0];
    const float vn = __builtin_bit_cast(float, __builtin_amdgcn_mov_dpp(__builtin_bit_cast(int, v), 0xB1, 0xf, 0xf, true));
    st8(rws, (cl & 1) ? OOB : g < 2 ? (g * 20 + cl) * 4 : (g == 2 && cl < 8) ? uh * 4 : OOB,
        OFF_ZP + (kj * 32 + c.Mt * 2 + c.mw) * 640 + c.nw * 160, u32x2{pk2(v, vn), (unsigned)tag});
    if (lds_inc((LAS unsigned*)(lds + L2_FLAG) + LF_PUB, lane) == 7u && lane < 8) set_flag(ws, OFF_FZ + lane * 512 + kj * 128 + c.Mt * 8, tag);
    SB();
  }
#pragma unroll
  for (int mi = 0; mi < 4; ++mi)
#pragma unroll
    for (int f = 0; f < 3; ++f)
#pragma unroll
      for (int hh = 0; hh < 2; ++hh)
        fv[mi >> 1][f][(mi & 1) * 2 + hh] = pk2(center(mi, 2 + f, hh) * (hh ? hi2(rstd[mi]) : lo2(rstd[mi])));
  constexpr int KI[7] = {0, 0, 2, 1, 1, 2, 2}, VI[7] = {0, 2, 0, 1, 2, 1, 2};
  f32x4 p[7];
#pragma unroll
  for (int q = 0; q < 7; ++q) p[q] = mfma(fv[1][VI[q]], fk[1][KI[q]], mfma(fv[0][VI[q]], fk[0][KI[q]], zero));
  LAS char* ex = lds + L1_EX + c.w * 3584;
  auto put = [&](bool ok, int u, int e, f32x4 v) { *(LAS f32x4*)(ex + (ok ? (u * 20 + e) * 4 : 3200)) = v; };
  put(true, cl, 4 * g, p[0]);
  put(true, 20 + cl, 4 * g, p[3]);
  put(g >= 2, (g & 1) * 20 + cl, 16, g == 2 ? p[1] : p[4]);
  put(cl < 8, uh, 4 * g, cl < 4 ? p[2] : p[5]);
  put(cl < 8 && g == 2 + (cl >> 2), uh, 16, p[6]);
  WAIT_L0(); BAR();
#pragma unroll
  for (int q = 0; q < 2; ++q) {
    const int o = (c.mw * 128 + q * 64 + lane) * 16;
    const LAS char* src = lds + L1_EX + c.nw * 3584 + (o < 3200 ? o : 0);
    const f32x4 v = *(const LAS f32x4*)src + *(const LAS f32x4*)(src + 4 * 3584);
    st16(rws, o < 3200 ? o : OOB, OFF_KVP + ((kj * 4 + c.nw) * 16 + c.Mt) * 3456,
         u32x4{pk2(lo2(v)), (unsigned)tag, pk2(hi2(v)), (unsigned)tag});
  }
  SB();
  STAMP(4);
  LAS unsigned* lf = (LAS unsigned*)(lds + L2_FLAG);
  if (lds_inc(lf + LF_PUB + 1, lane) == 7u && lane < 8) set_flag(ws, OFF_FK + lane * 512 + kj * 128 + c.Mt * 8, tag);
  BAR();
}

DEVI void retile(const char* wd, rsrc_t rws, unsigned ldsw, int id, int w, int lane) {
  const u32x4 rd = srd(wd, kF * 1280);
  const int jj = lane >> 3, g = lane >> 4;
  const unsigned vo = ((jj >> 2) * 8 + (jj & 3)) * 1280 + (lane & 7) * 16;
  const unsigned ra = ldsw + g * 1024 + ((lane & 15) >> 2) * 128 + (lane & 3) * 8;
  auto fetch = [&](int n) {
    const int it = id * 8 + w + n * 128, rb = it / 10, ln = it - rb * 10;
#pragma unroll
    for (int q = 0; q < 4; ++q)
      dma<0>(rd, vo, it < 540 ? (rb * 32 + 16 * (q >> 1) + 4 * (q & 1)) * 1280 + ln * 128 : OOB, ldsw + (n & 3) * 4096 + q * 1024);
  };
  auto put = [&](int n) {
    const int it = id * 8 + w + n * 128, rb = it / 10, ln = it - rb * 10, cg = rb / 6, uu = rb - cg * 6;
    const int ks = 18 * (uu >> 1) + 2 * cg + (uu & 1);
#pragma unroll
    for (int q = 0; q < 4; ++q) {
      const int nt = ln * 4 + q, j = nt / 5, t = nt - j * 5;
      st16(rws, lane * 16, it < 540 ? OFF_WD + ((j * 54 + ks) * 5 + t) * 1024 : OOB, lds_trfrag(ra + (n & 3) * 4096 + q * 32));
    }
  };
#pragma unroll 1
  for (int n = 0; n < 4; ++n) fetch(n);
  WAIT_V(0);
  put(0);
  WAIT_L0();
  fetch(4);
#pragma unroll 1
  for (int n = 1; n < 5; ++n) {
    if (n == 4) WAIT_V(0);
    put(n);
  }
}

DEVI void fold_kv(LAS char* lds, char* ws, rsrc_t rws, Watch& wt, const u32x4 (&wf)[2][5], int hp, int w, int lane) {
  const u64 tag = wt.tag;
  const int cl = lane & 15, g = lane >> 4;
  const int o = ((w & 3) * 64 + lane) * 16;
  unsigned po = o < 3200 ? o : OOB, ko[6][2];
#pragma unroll
  for (int f = 0; f < 6; ++f) {
    const int u = 20 * (f & 1) + (f < 4 ? (cl & 4) * 2 + 4 * (f >> 1) : 16) + (cl & 3);
    const bool mine = f >= 4 || (cl >> 3) == (f & 1);
    ko[f][0] = (unsigned)(size_t)lds + L2_RED + (mine && g < 3 ? u * 80 + g * 32 : 3360);
    ko[f][1] = (unsigned)(size_t)lds + L2_RED + (mine && g < 2 ? u * 80 + g * 32 + 16 : 3360);
    PIN(ko[f][0]); PIN(ko[f][1]);
  }
  unsigned vo = OFF_MP + (w * 20 + (hp >> 2) * 5) * 5120 + (3 * ((hp >> 1) & 1) + (g & 1)) * 5120 + (((hp & 1) + (g & 2)) * 16 + cl) * 16;
  unsigned vo2 = OFF_MP + (w * 20 + (hp >> 2) * 5 + 2) * 5120 + g * 1024 + ((hp & 3) * 16 + cl) * 16;
  PIN(po); PIN(vo); PIN(vo2);
  WAIT_V(0);
  if (w == 0) wait_flags(ws, OFF_FK + (blockIdx.x & 7) * 512 + (hp >> 2) * 128 + cl * 8, tag);
  BAR();
  STAMP(2);
  {
    *(LAS f32x4*)(lds + L2_RED + (w >> 2) * 4096 + o) = sweep8(rws, po, OFF_KVP + (hp * 16 + (w >> 2) * 8) * 3456, 3456, (unsigned)tag);
    WAIT_L0(); BAR();
  }
  STAMP(12);
  u64 fv = 0;
  if (w == 7) fv = wt.fetch();
  SB();
  u32x4 kf[6];
#pragma unroll
  for (int f = 0; f < 6; ++f) {
    const f32x4 lo = *(LAS f32x4*)(size_t)ko[f][0] + *(LAS f32x4*)(size_t)(ko[f][0] + 4096);
    const f32x4 hi = *(LAS f32x4*)(size_t)ko[f][1] + *(LAS f32x4*)(size_t)(ko[f][1] + 4096);
    kf[f] = u32x4{pk2(lo2(lo)), pk2(hi2(lo)), pk2(lo2(hi)), pk2(hi2(hi))};
  }
  const f32x4 zero = {0.f, 0.f, 0.f, 0.f};
  u32x4 r1;
#pragma unroll
  for (int i = 0; i < 5; ++i) {
    const f32x4 d1 = mfma(kf[1], wf[1][i], mfma(kf[0], wf[0][i], zero)), d2 = mfma(kf[3], wf[1][i], mfma(kf[2], wf[0][i], zero));
    const f32x4 d3 = mfma(kf[4], wf[0][i], zero), d4 = mfma(kf[5], wf[1][i], zero);
    st16(rws, vo, i * 1024, u32x4{pk2(lo2(d1)), pk2(hi2(d1)), pk2(lo2(d2)), pk2(hi2(d2))});
    const u32x4 t = {pk2(lo2(d3)), pk2(hi2(d3)), pk2(lo2(d4)), pk2(hi2(d4))};
    if (i == 0 || i == 4) r1 = t; else r1 = g == i ? t : r1;
    if (i == 3) st16(rws, vo2, 0, r1);
    if (i == 4) st16(rws, g == 0 ? vo2 + 4096 : OOB, 0, r1);
  }
  STAMP(3);
  SB();
  if (w == 7) wt.eval(fv, false);
  WAIT_L0(); BAR();
  wt.pcnt = 0; wt.plast = 7; wt.preps = 8; wt.pflag = OFF_F3 + hp * 8;
}

#define FPRIO 2
template <bool ATT>
DEVI void p2_steps(LAS char* lds, rsrc_t rws, Watch& wt, const u32x4 (&br)[3][5], f32x4 (&acc)[4][5], const u32x2 (&xr)[5],
                   int mt2, int j, int w, int lane) {
  LAS unsigned* lf = wt.lf;
  const unsigned ab = OFF_H + (mt2 * 54 + w) * 4096;
  u32x4 fa[12][4], fb[12][5];
  auto ldA = [&](int n) {
    const unsigned so = n >= 7 ? OFF_Q + (mt2 * 20 + (w - 4) * 5 + n - 7) * 4096
                               : n == 6 && w >= 6 ? OOB : ab + (n < 3 ? 30 + 8 * ((n + (mt2 >> 1)) % 3) : 8 * n - 24) * 4096;
#pragma unroll
    for (int m = 0; m < 4; ++m) fa[n][m] = ld16(rws, lane * 16, so + ((m + w) & 3) * 1024);
    SB();
  };
  unsigned lb = (unsigned)(size_t)lds + w * 5120 + lane * 16;
  auto rdB = [&](int n) {
    if (n == 5) { lb += 81920; PIN(lb); }
    if (ATT && n == 6) { lb -= w >= 6 ? 40960 : 0; PIN(lb); }
#pragma unroll
    for (int t = 0; t < 5; ++t) fb[n][t] = lds_b128(lb + ((n - 3) & 1) * 40960 + t * 1024);
    SB();
  };
  auto ld1 = [&](int kind, int n, int i) {
    if (kind == 0) {
      const unsigned so = n >= 7 ? OFF_Q + (mt2 * 20 + (w - 4) * 5 + n - 7) * 4096
                                 : n == 6 && w >= 6 ? OOB : ab + (n < 3 ? 30 + 8 * ((n + (mt2 >> 1)) % 3) : 8 * n - 24) * 4096;
      fa[n][i] = ld16(rws, lane * 16, so + ((i + w) & 3) * 1024);
    } else fb[n][i] = ld16(rws, lane * 16, OFF_MP + (j * 20 + (w - 4) * 5 + n - 7) * 5120 + i * 1024);
    SB();
  };
  LAS char* ex = lds + w * 5120 + lane * 16;
  constexpr int PRE = 3;
#pragma unroll
  for (int n = 0; n < PRE; ++n) {
    ldA(n);
    if (n == 1 && wt.preps != 0) { WAIT_V(8); wt.publish(); STAMP(13); }
  }
  if (ATT) {
#pragma unroll
    for (int t = 0; t < 5; ++t) {
      const f32x2 x0 = unpk(xr[t][0]), x1 = unpk(xr[t][1]);
      acc[0][t] = f32x4{x0[0], x0[1], x1[0], x1[1]};
      PIN(acc[0][t]);
    }
    SB();
    asm volatile("s_nop 1" ::: "memory");
  }
  constexpr int A1[12] = {3, 5, -1, -1, ATT ? 7 : -1, ATT ? 8 : -1, ATT ? 9 : -1, 10, 11, -1, -1, -1};
  constexpr int A2[12] = {4, 6, -1, -1, -1, -1, -1, -1, -1, -1, -1, -1};
  constexpr int M1[12] = {-1, -1, -1, -1, ATT ? 7 : -1, ATT ? 8 : -1, ATT ? 9 : -1, 10, 11, -1, -1, -1};
  u64 fv = 0;
  static_for<(ATT ? 12 : 7)>([&](auto N) {
    constexpr int n = N;
    if (n == 2) lds_wait(lf + LF_DMA, 8u);
    if (ATT && n == 4) lds_wait_bits(lf + LF_STATUS, S_Q | S_M);

    if (!ATT && (n == 2 || n == 4 || n == 5) && w == 3) { if (n > 2) wt.eval(fv, false); fv = wt.fetch(); SB(); }
    if (n >= 2 && n < 6) rdB(n + 1);
    if (n == (ATT ? 11 : 6)) lds_wait(lf + LF_BC + ATT, 4u);
#pragma unroll
    for (int q = 0; q < 20; ++q) {
      constexpr int LASTN = ATT ? 11 : 6;
      const int m = n == LASTN ? (q / 5 + 1) & 3 : n < 7 ? q / 5 : q & 3, t = n < 7 || n == LASTN ? q % 5 : q >> 2;
      if (n == 0 && (m > 0 || !ATT)) mfma_new(acc[m][t], br[0][t], fa[0][m]);
      else mfma_acc(acc[m][t], n < 3 ? br[n < 3 ? n : 0][t] : fb[n][t], fa[n][m]);
      if (n == LASTN && q >= 10) {
        SB();
        *(LAS f32x4*)(ex + ((q - 10) / 5) * 40960 + (q % 5) * 1024) = acc[(q - 10) / 5 + 1][q % 5];
        SB();
      }
      if (q & 1) {
        const int p = q >> 1, na = (A1[n] >= 0 ? 4 : 0) + (A2[n] >= 0 ? 4 : 0);
        if (p < na) ld1(0, p < 4 ? A1[n] : A2[n], p & 3);
        else if (M1[n] >= 0 && p - na < 5) ld1(1, M1[n], p - na);
      }
    }
    SB();
    if (n == 0) STAMP(9);
    if (n == 6) STAMP(11);
    if (n == 5) { WAIT_L0(); lds_inc(lf + LF_BC + ATT, lane); }
  });
  if (!ATT && w == 3) wt.eval(fv, false);
}

DEVI void phase2(LAS char* lds, const char* x, char* ws, rsrc_t rws, char* out, Watch& wt, u32x4 (&br)[3][5],
                 int mt2, int j, int w, int lane) {
  const int a = w & 3;
  LAS unsigned* lf = wt.lf;
  const unsigned obase = (mt2 * 64 + a * 16) * 1280 + j * 160;
  f32x4 acc[4][5];
  u32x2 xr[5];
  if (w >= 4) {
    const rsrc_t rx = __builtin_amdgcn_make_buffer_rsrc((void*)x, 0, 2048 * 1280, 0x00020000);
#pragma unroll
    for (int t = 0; t < 5; ++t) xr[t] = __builtin_amdgcn_raw_buffer_load_b64(rx, (lane & 15) * 1280 + (lane >> 4) * 8 + t * 32, obase, 0);
    SB();
  }
  if (wt.preps != 0) { if (!wt.has(S_H)) { if (w == 7) wt.poll(false); WAIT_V(0); wt.publish(); STAMP(13); } }
  else WAIT_V(15);
  lds_inc(lf + LF_DMA, lane);
  wt.need(S_H);
  STAMP(6);
  if (w < 4) {
    __builtin_amdgcn_s_setprio(FPRIO);
    p2_steps<false>(lds, rws, wt, br, acc, xr, mt2, j, w, lane);
    __builtin_amdgcn_s_setprio(0);
#pragma unroll
    for (int q = 0; q < 15; ++q) asm volatile("" : "=v"(br[q / 5][q % 5]));
  }
  PINS(w);
  if (w >= 4) { __builtin_amdgcn_s_setprio(1); p2_steps<true>(lds, rws, wt, br, acc, xr, mt2, j, w, lane); }
  asm volatile("s_nop 7" ::: "memory");
  SB();
  STAMP(14);
  LAS char* ex = lds + (w >> 2) * 20480 + lane * 16;
#pragma unroll
  for (int t = 0; t < 5; ++t) *(LAS f32x4*)(ex + 2 * 40960 + (a * 5 + t) * 1024) = acc[3][t];
  WAIT_L0();
  if (w < 4) { lds_inc(lf + LF_X, lane); if (w == 3) wt.need(S_Q | S_M, 3); lds_wait(lf + LF_X, 4u); }
  else BAR();
  STAMP(8);
#pragma unroll
  for (int t = 0; t < 5; ++t)
#pragma unroll
    for (int q = 1; q < 4; ++q) acc[0][t] += *(LAS f32x4*)(ex + (q - 1) * 40960 + (((a - q) & 3) * 5 + t) * 1024);
  LAS char* base = lds + L2_BASE + a * 5120 + lane * 16;
  if (w < 4) {
#pragma unroll
    for (int t = 0; t < 5; ++t) *(LAS f32x4*)(base + t * 1024) = acc[0][t];
    WAIT_L0();
  } else {
    const rsrc_t ro = __builtin_amdgcn_make_buffer_rsrc(out, 0, 2048 * 1280, 0x00020000);
    u32x2 o[5];
#pragma unroll
    for (int t = 0; t < 5; ++t) {
      const f32x4 s = acc[0][t] + *(LAS f32x4*)(base + t * 1024);
      o[t] = u32x2{pk2(lo2(s)), pk2(hi2(s))};
    }
    const unsigned orow = obase + (lane & 15) * 1280;
#pragma unroll
    for (int t = 0; t < 4; t += 2) {
      const auto s0 = __builtin_amdgcn_permlane16_swap(o[t][0], o[t + 1][0], false, false);
      const auto s1 = __builtin_amdgcn_permlane16_swap(o[t][1], o[t + 1][1], false, false);
      __builtin_amdgcn_raw_buffer_store_b128(u32x4{s0[0], s1[0], s0[1], s1[1]}, ro, orow + ((lane >> 4) & 1) * 32 + (lane >> 5) * 16 + t * 32, 0, OUT_AUX);
    }
    __builtin_amdgcn_raw_buffer_store_b64(o[4], ro, orow + (lane >> 4) * 8 + 128, 0, OUT_AUX);
  }
}

__global__ __launch_bounds__(512, 2) void fused(const char* x, const char* wqkv, const char* wo, const char* wg,
                                                const char* wu, const char* wd, char* ws, char* out) {
  extern __shared__ __attribute__((aligned(16))) char smem[];
  const char* pc0;
  asm volatile("s_getpc_b64 %0" : "=s"(pc0));
  asm volatile("" : "+s"(x), "+s"(wqkv), "+s"(wo), "+s"(wg), "+s"(wu), "+s"(wd), "+s"(ws), "+s"(out));
  LAS char* lds = (LAS char*)smem;
  const unsigned ldsb = (unsigned)(size_t)lds;
  const int bid = blockIdx.x, xcd = bid & 7, idx = bid >> 3;
  const int Mt = (xcd & 1) * 8 + (idx & 7), grp = xcd >> 1, pos4 = idx >> 3, slot2 = grp * 4 + pos4;
  const int cslot = (int)(0xCF76BE54AD329810ull >> (4 * (pos4 == 2 ? (slot2 + 4 * (idx >> SPR)) & 15 : slot2))) & 15;
  const int w = __builtin_amdgcn_readfirstlane(threadIdx.x >> 6), lane = threadIdx.x & 63;
  STAMP(0);
  const rsrc_t rws = __builtin_amdgcn_make_buffer_rsrc(ws, 0, WS_END, 0x00020000);
  u64 tag;
  u32x2 epoch = {0u, 0u};
  ld8_hidden(epoch, srd(ws, WS_END), OFF_E);
  Watch wt{(LAS unsigned*)(lds + L2_FLAG), ws, 0ull, Mt * 2 + (slot2 >> 3), slot2 & 7, w, lane, 0u, 0, 0, 0, 0u};
  u32x4 br[3][5];
  PF2 pf{ws, rws, ldsb, slot2 & 7, w, lane, Mt % 3, br};
  if (cslot == 15) {
    if (threadIdx.x < 16) ((LAS unsigned*)(lds + L2_FLAG))[threadIdx.x] = 0u;
    retile(wd, rws, ldsb + w * 16384, Mt, w, lane);
    WAIT_V(0);
    PIN(epoch);
    wt.tag = tag = make_tag(epoch);
    WAIT_L0(); BAR();
    if (threadIdx.x == 0) { set_flag(ws, OFF_FR + Mt * 8, tag); set_flag(ws, OFF_F1 + (Mt * 16 + 15) * 8, tag); }
    STAMP(5);
    u32x4 wf[2][5];
    {
      const u32x4 ro = srd(wo, kD * 1280);
#pragma unroll 1
      for (int q = 0; q < 10; ++q)
        dma<0>(ro, (lane >> 3) * 1280 + (lane & 7) * 16, (Mt * 40 + (q >> 1) * 8) * 1280 + ((w * 160) & ~127) + (q & 1) * 128, ldsb + w * 16384 + q * 1024);
      WAIT_V(0);
#pragma unroll
      for (int hh = 0; hh < 2; ++hh) {
        unsigned ra[2];
#pragma unroll
        for (int h2 = 0; h2 < 2; ++h2) {
          int row = hh * 20 + (lane >> 4) * 8 + h2 * 4 + ((lane & 15) >> 2);
          row = row < 40 ? row : 39;
          ra[h2] = ldsb + w * 16384 + (row >> 3) * 2048 + (row & 7) * 128 + (lane & 3) * 8;
        }
#pragma unroll
        for (int i = 0; i < 5; ++i) {
          const int cb = (w & 3) * 32 + i * 32, o = (cb >> 7) * 1024 + (cb & 127);
          const u32x2 lo = __builtin_bit_cast(u32x2, __builtin_amdgcn_ds_read_tr16_b64_v4i16((LAS s16x4*)(size_t)(ra[0] + o)));
          const u32x2 hi = __builtin_bit_cast(u32x2, __builtin_amdgcn_ds_read_tr16_b64_v4i16((LAS s16x4*)(size_t)(ra[1] + o)));
          wf[hh][i] = u32x4{lo[0], lo[1], hi[0], hi[1]};
        }
      }
    }
    wait_flags(ws, OFF_FR + (lane & 15) * 8, tag);
    WAIT_L0(); BAR();
    wd_prefetch<0, 6>(srd(ws, WS_END), ldsb, slot2 & 7, w, lane);
    pf.items<0, 28>();
    if (Mt == 0 && w == 7) {
#pragma unroll 1
      for (int q = 0; q < 4; ++q) wait_flags(ws, OFF_F1 + (q * 64 + lane) * 8, tag);
      if (lane == 0) set_flag(ws, OFF_E, tag);
    }
    fold_kv(lds, ws, rws, wt, wf, Mt, w, lane);
  } else {
    const int type = cslot < 9 ? 0 : cslot < 13 ? 1 : 2;
    P1 c;
    c.w = w; c.mw = w >> 2; c.nw = w & 3; c.Mt = Mt; c.cslot = cslot; c.j = slot2 & 7;
    c.ldsb = ldsb;
    {
#define ROTM 1
      const int kb = (Mt + cslot) % 10, rot = ROTM == 0 ? (kb >= 5 ? 5 : 0) : ROTM == 1 ? (kb > 7 ? 7 : kb) : (kb / 3) * 3 - (kb == 9 ? 3 : 0);
      c.bstar = kb - rot;
      const int seg = w >> 2;
      c.pitch = type == 0 ? kF * 2 : kQKV * 2;
      const int startb = type == 0 ? cslot * 384
                       : ((type == 1 ? (640 * (1 + seg) + 160 * (cslot - 9)) * 2 : 320 * (2 * (cslot - 13) + seg)) & ~127);
      const char* wb = type == 0 ? (seg ? wu : wg) : wqkv;
      c.rw = srd(wb + startb - 2048, 640 * c.pitch - startb + 2048);
      c.rx = srd(x + (size_t)Mt * 128 * 1280, 128 * 1280);
      const int i = lane >> 3, cp = lane & 7, pg = w & 3;
      c.voffB = (unsigned)(8 * pg + i) * c.pitch + 16 * (cp ^ ((i & 2) | ((pg & 1) << 2))) + 2048;
      c.voffA = (unsigned)(8 * w + i) * 1280 + 16 * (cp ^ ((4 * w + (i >> 1)) & 7));
      c.oB = rot * 64 * c.pitch; c.oA = rot * 128;
      dma<0>(c.rx, c.voffA, c.oA, c.ldsb + L1_A + w * 1024);
    }
    auto pro = [&](int s) {
      const unsigned lB = c.ldsb + L1_B + w * 3072, lA = c.ldsb + L1_A + w * 1024;
      dma<0>(c.rw, c.voffB, c.oB, lB + s * L1_BST);
      dma<128>(c.rw, c.voffB, c.oB, lB + s * L1_BST + 1024);
      dma<256>(c.rw, c.voffB, c.oB, lB + s * L1_BST + 2048);
      c.oB += 32 * c.pitch;
      dma<0>(c.rx, c.voffA, c.oA + ((s + 1) >> 1) * 128 + ((s + 1) & 1) * 81920, lA + (s + 1) * 8192);
      SB();
    };
#define PSPLIT 1
#pragma unroll
    for (int s = 0; s < PSPLIT; ++s) pro(s);
    unsigned vb[5], va0;
    {
      const int g = lane >> 4, q = (lane & 15) >> 2, p = lane & 3, r = lane & 15, nw = c.nw;
      const int swz = (q & 2) | ((g & 1) << 2);
#pragma unroll
      for (int pos = 0; pos < 5; ++pos) {
        const int cf = 16 * nw + 4 * p;
        const int hb = (cslot & 1 ? 0 : 32) + 40 * nw;
        const int ck = pos < 4 ? (pos >> 1) * 192 + hb + 20 * (pos & 1) + 4 * p : (p >> 1) * 192 + hb + 20 * (p & 1) + 16;
        const int cq = (nw >> 1) * 224 + 20 * (4 * (nw & 1) + 2 * (p & 1) + (p >> 1)) + 4 * pos;
        const int col = type == 0 ? cf : type == 1 ? ck : cq;
        const int line = col >> 6, seg = line >= 3;
        vb[pos] = c.ldsb + L1_B + seg * (12288 - 3072) + (g * 3 + line) * 1024 + q * 128 + 16 * (((col & 63) >> 3) ^ swz) + 8 * ((col >> 2) & 1);
      }
      va0 = c.ldsb + L1_A + (c.mw * 64 + r) * 128 + 16 * (g ^ ((r >> 1) & 7));
    }
    if (threadIdx.x < 16) ((LAS unsigned*)(lds + L2_FLAG))[threadIdx.x] = 0u;
    f32x4 acc[4][6];
#pragma unroll
    for (int m = 0; m < 4; ++m)
#pragma unroll
      for (int i = 0; i < 6; ++i) acc[m][i] = f32x4{0.f, 0.f, 0.f, 0.f};
    unsigned foff = lane >= 20 ? OFF_FR + (lane & 15) * 8 : OFF_F1 + (lane < 10 ? lane * 16 + cslot : Mt * 16 + lane - 10) * 8;
    PIN(foff);
#pragma unroll
    for (int i = 0; i < 5; ++i) PIN(vb[i]);
    SB();
#pragma unroll
    for (int s = PSPLIT; s < 3; ++s) pro(s);
    c.oA += 256;
    if (w == 7 && idx < 8) warm_code(pc0, ldsb + L1_GR + 8192, idx, lane);
    SB();
    WAIT_V(8);
    PIN(epoch);
    wt.tag = tag = make_tag(epoch);
#define P1_STAMP
#define P1_RUN(T) do { if (w < 4) p1_loop<T, true>(c, vb, va0, foff, acc, pf, ws, rws, tag, lane); \
                       else p1_loop<T, false>(c, vb, va0, foff, acc, pf, ws, rws, tag, lane); P1_STAMP; } while (0)
    if (type == 0) { P1_RUN(0); epi_ffn(lds, acc, ws, rws, wt, c, pf, lane); }
    else if (type == 2) { P1_RUN(2); epi_q(lds, acc, ws, rws, wt, c, pf, lane); }
    else {
      P1_RUN(1);
      epi_kv(lds, acc, ws, rws, tag, c, lane);
      STAMP(5);
      wd_prefetch<0, 6>(srd(ws, WS_END), ldsb, c.j, w, lane);
      pf.items<0, 28>();
    }
  }
  phase2(lds, x, ws, rws, out, wt, br, Mt * 2 + (slot2 >> 3), slot2 & 7, w, lane);
  STAMP(10);
}

extern "C" void kernel_launch(void* const* d_in, const int* in_sizes, int n_in, void* d_out, int out_size,
                              void* d_ws, size_t ws_size, hipStream_t stream) {
  (void)in_sizes; (void)n_in; (void)out_size; (void)ws_size;
  (void)hipFuncSetAttribute((const void*)fused, hipFuncAttributeMaxDynamicSharedMemorySize, LDS_SIZE);
  fused<<<256, 512, LDS_SIZE, stream>>>((const char*)d_in[0], (const char*)d_in[1], (const char*)d_in[2], (const char*)d_in[4],
                                        (const char*)d_in[5], (const char*)d_in[6], (char*)d_ws, (char*)d_out);
}
```

```cpp
#include <hip/hip_runtime.h>
#include <stdint.h>
#include <type_traits>

#define GAS __attribute__((address_space(1)))
#define LAS __attribute__((address_space(3)))
typedef __attribute__((ext_vector_type(2))) __bf16 bf16x2;
typedef __attribute__((ext_vector_type(8))) __bf16 bf16x8;
typedef __attribute__((ext_vector_type(4))) short s16x4;
typedef __attribute__((ext_vector_type(4))) float f32x4;
typedef __attribute__((ext_vector_type(2))) float f32x2;
typedef __attribute__((ext_vector_type(4))) unsigned u32x4;
typedef __attribute__((ext_vector_type(2))) unsigned u32x2;
typedef unsigned long long u64;
typedef __amdgpu_buffer_rsrc_t rsrc_t;

#define DEVI __device__ __forceinline__
#define WAIT_V_(n) asm volatile("s_waitcnt vmcnt(" #n ")" ::: "memory")
#define WAIT_V(n) WAIT_V_(n)
template <int N> __device__ __forceinline__ void wait_v() { asm volatile("s_waitcnt vmcnt(%0)" :: "i"(N) : "memory"); }
#define WAIT_L0() do { __builtin_amdgcn_s_waitcnt(0xC07F); asm volatile("" ::: "memory"); } while (0)
#define SB() __builtin_amdgcn_sched_barrier(0)
#define PIN(x) asm volatile("" : "+v"(x))
#define PINS(x) asm volatile("" : "+s"(x))
#define BAR() do { asm volatile("" ::: "memory"); __builtin_amdgcn_s_barrier(); asm volatile("" ::: "memory"); } while (0)
#define RLX_AGENT __ATOMIC_RELAXED, __HIP_MEMORY_SCOPE_AGENT
#define RLX_WG __ATOMIC_RELAXED, __HIP_MEMORY_SCOPE_WORKGROUP
#define SC1 16
#define OUT_AUX 0

#define STAMP(i)

constexpr int kD = 640, kF = 1728, kQKV = 1920;
constexpr unsigned OFF_E   = 0;
constexpr unsigned OFF_F1  = 256;
constexpr unsigned OFF_FR  = OFF_F1 + 2048;
constexpr unsigned OFF_FK  = 2560;
constexpr unsigned OFF_F3  = OFF_FK + 4096;
constexpr unsigned OFF_FH  = OFF_F3 + 1024;
constexpr unsigned OFF_FZ  = 16384;
constexpr unsigned OFF_ZP  = OFF_FZ + 4096;
constexpr unsigned OFF_SC  = OFF_ZP + 4 * 32 * 640;
constexpr unsigned OFF_SR  = OFF_SC + 15 * 10 * 384 * 4;
constexpr unsigned OFF_KVP = OFF_SR + 16 * 10 * 256 * 4;
constexpr unsigned OFF_MP  = OFF_KVP + 16 * 16 * 3456;
constexpr unsigned OFF_WD  = OFF_MP + 8 * 20 * 5 * 1024;
constexpr unsigned OFF_Q   = OFF_WD + 8 * 54 * 5 * 1024;
constexpr unsigned OFF_H   = OFF_Q + 32 * 20 * 4096;
constexpr unsigned WS_END  = OFF_H + 32 * 54 * 4096;
constexpr unsigned OOB = 0x80000000u;
static_assert(OFF_FH + 8192 <= OFF_FZ && OFF_MP % 1024 == 0 && OFF_KVP % 128 == 0, "align");

constexpr int L1_B = 0, L1_BST = 24576;
constexpr int L1_A = 4 * L1_BST, L1_AST = 16384;
constexpr int L1_GR = L1_A + 3 * L1_AST;
constexpr int L1_GC = L1_A + L1_AST;
constexpr int L1_ST = 158720;
constexpr int L1_CS = L1_ST + 1024;
constexpr int L1_EX = 72 * 1024;
constexpr int L2_BASE = 120 * 1024;
constexpr int L2_RED = 150 * 1024;
constexpr int L2_FLAG = 162816;
constexpr int LDS_SIZE = L2_FLAG + 64;
enum { LF_STATUS = 0, LF_DMA = 1, LF_K = 2, LF_X = 3, LF_BC = 4, LF_PUB = 8 };
enum { S_H = 1, S_Q = 2, S_M = 4 };

DEVI f32x4 mfma(u32x4 a, u32x4 b, f32x4 c) {
  return __builtin_amdgcn_mfma_f32_16x16x32_bf16(__builtin_bit_cast(bf16x8, a), __builtin_bit_cast(bf16x8, b), c, 0, 0, 0);
}
DEVI void mfma_acc(f32x4& c, u32x4 a, u32x4 b) { asm volatile("v_mfma_f32_16x16x32_bf16 %0, %1, %2, %0" : "+v"(c) : "v"(a), "v"(b)); }
DEVI void mfma_new(f32x4& c, u32x4 a, u32x4 b) { asm volatile("v_mfma_f32_16x16x32_bf16 %0, %1, %2, 0" : "=&v"(c) : "v"(a), "v"(b)); }
DEVI unsigned pk2(f32x2 f) { return __builtin_bit_cast(unsigned, __builtin_convertvector(f, bf16x2)); }
DEVI unsigned pk2(float lo, float hi) { return pk2(f32x2{lo, hi}); }
DEVI f32x2 unpk(unsigned u) { return f32x2{__builtin_bit_cast(float, u << 16), __builtin_bit_cast(float, u & 0xffff0000u)}; }
DEVI f32x2 fma2(f32x2 a, f32x2 b, f32x2 c) { return __builtin_elementwise_fma(a, b, c); }
DEVI f32x2 bc2(float v) { return f32x2{v, v}; }
DEVI f32x2 lo2(f32x4 v) { return f32x2{v[0], v[1]}; }
DEVI f32x2 hi2(f32x4 v) { return f32x2{v[2], v[3]}; }
DEVI f32x2 phi2(f32x2 g, f32x2 rs) {
  const f32x2 t = fma2(g, rs, bc2(1e-6f)), l = g * (rs * 1.4426950408889634f);
  return f32x2{fmaxf(t[0], 1e-6f), fmaxf(t[1], 1e-6f)} +
         f32x2{__builtin_amdgcn_fmed3f(__builtin_amdgcn_exp2f(l[0]), 0.f, 1.f), __builtin_amdgcn_fmed3f(__builtin_amdgcn_exp2f(l[1]), 0.f, 1.f)};
}
DEVI u32x4 lds_b128(unsigned a) { return *(LAS u32x4*)(size_t)a; }
DEVI u32x4 lds_trfrag(unsigned a) {
  const u32x2 lo = __builtin_bit_cast(u32x2, __builtin_amdgcn_ds_read_tr16_b64_v4i16((LAS s16x4*)(size_t)a));
  const u32x2 hi = __builtin_bit_cast(u32x2, __builtin_amdgcn_ds_read_tr16_b64_v4i16((LAS s16x4*)(size_t)(a + 512)));
  return u32x4{lo[0], lo[1], hi[0], hi[1]};
}
DEVI u32x4 srd(const void* p, unsigned bytes) {
  const u64 a = (u64)p;
  return u32x4{(unsigned)a, (unsigned)(a >> 32) & 0xffffu, bytes, 0x00020000u};
}
DEVI void setm0(unsigned v) { asm volatile("s_mov_b32 m0, %0" :: "s"(v) : "memory"); }
template <int IMM>
DEVI void dma_n(u32x4 r, unsigned voff, unsigned soff) {
  asm volatile("buffer_load_dwordx4 %0, %1, %2 offen offset:%3 lds" :: "v"(voff), "s"(r), "s"(soff), "i"(IMM) : "memory");
}
template <int IMM>
DEVI void dma(u32x4 r, unsigned voff, unsigned soff, unsigned lds) {
  asm volatile("s_mov_b32 m0, %4\n\ts_nop 0\n\tbuffer_load_dwordx4 %0, %1, %2 offen offset:%3 lds"
               :: "v"(voff), "s"(r), "s"(soff), "i"(IMM), "s"(lds - IMM) : "memory");
}
DEVI void dma_sc1(u32x4 r, unsigned voff, unsigned soff, unsigned lds) {
  asm volatile("s_mov_b32 m0, %3\n\ts_nop 0\n\tbuffer_load_dwordx4 %0, %1, %2 offen sc1 lds"
               :: "v"(voff), "s"(r), "s"(soff), "s"(lds) : "memory");
}
DEVI void ld8_hidden(u32x2& v, u32x4 r, unsigned voff) {
  asm volatile("buffer_load_dwordx2 %0, %1, %2, 0 offen sc1" : "+v"(v) : "v"(voff), "s"(r) : "memory");
}
template <int N0, int N1>
DEVI void wd_prefetch(u32x4 rws, unsigned ldsb, int j, int w, int lane) {
#pragma unroll
  for (int n = N0; n < N1; ++n) {
    const unsigned a = (unsigned)(w + 8 * n) * 1024;
    if (n < 18 || w < 6) dma_sc1(rws, lane * 16, OFF_WD + j * 276480 + a, ldsb + a);
  }
}
DEVI void wait_flags(char* ws, unsigned off, u64 tag) {
#pragma unroll 1
  for (unsigned spins = 0; spins < (1u << 20); ++spins) {
    const u64 v = __hip_atomic_load((GAS u64*)(ws + off), RLX_AGENT);
    if (__all(v == tag)) break;
    __builtin_amdgcn_s_sleep(1);
  }
  asm volatile("" ::: "memory");
}
DEVI u64 make_tag(u32x2 e) {
  const u64 v = (u64)(unsigned)__builtin_amdgcn_readfirstlane(e[0]) | (u64)(unsigned)__builtin_amdgcn_readfirstlane(e[1]) << 32;
  return v * 0x9E3779B97F4A7C15ull + 0xD1B54A32D192ED03ull;
}
DEVI void set_flag(char* ws, unsigned off, u64 tag) { __hip_atomic_store((GAS u64*)(ws + off), tag, RLX_AGENT); }
DEVI unsigned lds_inc(LAS unsigned* p, int lane) {
  unsigned o = 0;
  if (lane == 0) o = __hip_atomic_fetch_add(p, 1u, RLX_WG);
  return __builtin_amdgcn_readfirstlane(o);
}
DEVI void lds_wait_bits(LAS unsigned* p, unsigned bits) {
  while ((~(unsigned)__builtin_amdgcn_readfirstlane(__hip_atomic_load(p, RLX_WG)) & bits) != 0u) __builtin_amdgcn_s_sleep(1);
  asm volatile("" ::: "memory");
}
DEVI void lds_wait(LAS unsigned* p, unsigned want) {
  while ((unsigned)__builtin_amdgcn_readfirstlane(__hip_atomic_load(p, RLX_WG)) < want) __builtin_amdgcn_s_sleep(1);
  asm volatile("" ::: "memory");
}
DEVI u32x4 ld16(rsrc_t r, unsigned voff, unsigned soff) { return __builtin_amdgcn_raw_buffer_load_b128(r, voff, soff, SC1); }
DEVI void st16(rsrc_t r, unsigned voff, unsigned soff, u32x4 v) { __builtin_amdgcn_raw_buffer_store_b128(v, r, voff + soff, 0, SC1); }
DEVI void st8(rsrc_t r, unsigned voff, unsigned soff, u32x2 v) { __builtin_amdgcn_raw_buffer_store_b64(v, r, voff, soff, SC1); }

constexpr unsigned CODE_BYTES = 60 * 1024;
DEVI void warm_code(const char* pc0, unsigned junk, int chunk, int lane) {
  asm volatile("s_mov_b32 m0, %3\n\ts_nop 0\n\tbuffer_load_dword %0, %1, %2 offen lds"
               :: "v"(lane << 7), "s"(srd(pc0, CODE_BYTES)), "s"(chunk * 8192), "s"(junk) : "memory");
}

struct P1 {
  u32x4 rw, rx;
  unsigned ldsb, pitch;
  unsigned voffB, voffA;
  unsigned oB, oA;
  int w, mw, nw, bstar;
  int Mt, cslot, j;
};

struct PF2 {
  char* ws; rsrc_t rws; unsigned ldsb; int j, w, lane, r0;
  u32x4 (&br)[3][5];
  template <int N> DEVI void item() {
    if constexpr (N < 13) wd_prefetch<6 + N, 7 + N>(srd(ws, WS_END), ldsb, j, w, lane);
    else br[(N - 13) / 5][(N - 13) % 5] = ld16(rws, lane * 16, OFF_WD + (j * 54 + 30 + w + 8 * (((N - 13) / 5 + r0) % 3)) * 5120 + ((N - 13) % 5) * 1024);
    SB();
  }
  template <int N0, int N1> DEVI void items() { if constexpr (N0 < N1) { item<N0>(); items<N0 + 1, N1>(); } }
};

#define SPR 1
#define EB 3
#define AINC 1
template <int TYPE, bool LATE>
DEVI void p1_loop(P1& c, unsigned (&vb)[5], unsigned va0, unsigned foff, f32x4 (&acc)[4][6], PF2& pf, char* ws, rsrc_t rws, u64 tag, int lane) {
  constexpr int NT = TYPE == 0 ? 6 : 5;
  constexpr bool AC = !LATE || AINC;
  u32x4 fa[4], fb[NT];
  unsigned va1 = va0 ^ 64;
  const unsigned stepB = 32 * c.pitch, limB = 640 * c.pitch;
  const unsigned lB0 = c.ldsb + L1_B + c.w * 3072, lA0 = c.ldsb + L1_A + c.w * 1024;
  unsigned lB = lB0 + 3 * L1_BST, lA = lA0 + 2 * L1_AST;
  unsigned vB1 = c.voffB - 896, vB2 = c.voffB - 1792, vA1 = c.voffA + 81920;
  PIN(vB1); PIN(vB2); PIN(vA1);
  int dA = L1_AST, dB = 2 * L1_BST, rare = c.bstar, rare2 = 1;
  f32x4 st[5];
  auto adB = [&](int i) { return TYPE == 0 ? vb[!LATE && i >= 2 ? 1 : 0] + (i / 3) * 12288 + (i % 3) * 1024 : vb[i < 5 ? i : 4]; };
  if (TYPE == 0) { vb[1] = vb[0]; PIN(vb[1]); }
  auto upB = [&](int i0, int i1) {
    if (TYPE == 0) { vb[i0 ? 1 : 0] += dB; PIN(vb[i0 ? 1 : 0]); }
    else {
#pragma unroll
      for (int i = i0; i < i1; ++i) { vb[i] += dB; PIN(vb[i]); }
    }
  };
  auto rdA = [&](int i, int sub) { fa[i] = lds_b128((sub ? va1 : va0) + i * 2048); };
  auto rdB = [&](int i, int sub) { fb[i] = lds_trfrag(adB(i) + sub * L1_BST); };
  auto rdBh = [&](int i, int hh, int sub) {
    const u32x2 v = __builtin_bit_cast(u32x2, __builtin_amdgcn_ds_read_tr16_b64_v4i16((LAS s16x4*)(size_t)(adB(i) + sub * L1_BST + hh * 512)));
    fb[i][2 * hh] = v[0]; fb[i][2 * hh + 1] = v[1];
  };
  auto pieceA = [&](int sub) { dma_n<0>(c.rx, sub ? vA1 : c.voffA, c.oA); };
  auto L = [&](int sub, int mode) {
    if (mode) dma_n<0>(c.rw, c.voffB, c.oB);
    SB(); rdA(0, sub); rdA(1, sub); SB();
    if (mode) dma_n<1024>(c.rw, vB1, c.oB);
    SB(); rdA(2, sub); rdA(3, sub); SB();
    if (mode) dma_n<2048>(c.rw, vB2, c.oB);
    SB(); rdB(LATE ? 4 : 0, sub); SB();
    if (mode == 2) setm0(lA + sub * 8192);
    SB(); if (!LATE) rdB(1, sub); else if (NT == 6) rdB(NT - 1, sub); else if (mode == 2) asm volatile("s_nop 0"); SB();
    if (mode == 2) pieceA(sub);
    if (!LATE && !sub) upB(2, 5);
    SB();
  };
  auto U = [&](int sub) {
    if (sub) { va1 += dA; PIN(va1); upB(0, LATE ? 5 : 2); }
    else { va0 += dA; PIN(va0); }
    SB();
  };
  auto stats = [&](int sub) {
    const u32x4 one = {0x3f803f80u, 0x3f803f80u, 0x3f803f80u, 0x3f803f80u};
    const u32x4 sa = lds_b128((sub ? va1 : va0) + c.nw * 2048);
    u32x4 sb[3];
#pragma unroll
    for (int i = 0; i < 3; ++i) { unsigned a = adB(LATE ? i : 3 + i); PIN(a); sb[i] = lds_trfrag(a + sub * L1_BST); }
    SB();
    if (sub == 0) {
#pragma unroll
      for (int i = 0; i < 5; ++i) st[i] = f32x4{0.f, 0.f, 0.f, 0.f};
    }
    st[0] = mfma(one, sa, st[0]);
    st[1] = mfma(sa, sa, st[1]);
#pragma unroll
    for (int i = 0; i < 3; ++i) st[2 + i] = mfma(one, sb[i], st[2 + i]);
    if (sub == 1) {
      const int cl = lane & 15, g = lane >> 4;
      const float sq = (cl & 2) ? ((cl & 1) ? st[1][3] : st[1][2]) : ((cl & 1) ? st[1][1] : st[1][0]);
      st8(rws, (cl >> 2) == g ? (c.mw * 64 + c.nw * 16 + cl) * 8 : OOB, c.cslot < 10 ? OFF_SR + (c.Mt * 10 + c.cslot) * 1024 : WS_END,
          u32x2{__builtin_bit_cast(unsigned, st[0][0]), __builtin_bit_cast(unsigned, sq)});
      const float v = g == 0 ? st[2][0] : g == 1 ? st[3][0] : st[4][0];
      __builtin_amdgcn_raw_buffer_store_b32(__builtin_bit_cast(unsigned, v), rws, g < 3 ? ((c.nw * 6 + c.mw * 3) * 16 + lane) * 4 : OOB,
                                            c.Mt < 10 ? OFF_SC + (c.cslot * 10 + c.Mt) * 1536 : WS_END, SC1);
    }
    SB();
  };
#define SEG(i)
  auto C = [&](int sub, bool more, int book, auto vmwait) {
    SEG(0);
    __builtin_amdgcn_s_setprio(1);
    if (LATE) { vmwait(); WAIT_L0(); BAR(); }
    SB();
#pragma unroll
    for (int k = 0; k < 4 * NT; ++k) {
      const int i = k >> 2, m = k & 3, ms = (i & 1) ? 3 - m : m;
      acc[ms][i] = TYPE == 1 ? mfma(fa[ms], fb[i], acc[ms][i]) : mfma(fb[i], fa[ms], acc[ms][i]);
      if (LATE) { if (more && i >= 1 && i < 5 && m < 2) rdBh(i - 1, m, sub ^ 1); }
      else if (k < 2 * (NT - 2)) rdBh(2 + (k >> 1), k & 1, sub);
      const int q = LATE ? (k < 2 ? k : m >= 2 && i < 5 ? 2 * i + m : -1) : k - 2 * (NT - 2);
      constexpr int QA = LATE ? 8 : 0, QU = LATE ? 0 : 3;
      if (book == 1) {
        if (AC) {
          if (q == QA) setm0(lA + (LATE ? sub ^ 1 : sub) * 8192);
          if (q == QA + 1) pieceA(LATE ? sub ^ 1 : sub);
        }
        if (q == QU) { c.oB += stepB; lB += L1_BST; PINS(c.oB); PINS(lB); }
        if (q == QU + 1) { c.oB = c.oB == limB ? 0u : c.oB; PINS(c.oB); }
        if (q == QU + 2) { lB = lB == lB0 + 4 * L1_BST ? lB0 : lB; PINS(lB); }
        if (sub) {
          if (q == QU + 3) { c.oA += 128; lA += L1_AST; PINS(c.oA); PINS(lA); }
          if (q == QU + 4) { c.oA = c.oA == 1280u ? 0u : c.oA; PINS(c.oA); }
          if (q == QU + 5) { lA = lA == lA0 + 3 * L1_AST ? lA0 : lA; PINS(lA); }
          if (q == QU + 6) { dA = lA == lA0 + L1_AST ? -2 * L1_AST : L1_AST; PINS(dA); }
          if (q == QU + 7) { rare -= 1; PINS(rare); }
        }
        if (!sub && q == QU + 3) { rare2 = LATE ? (rare + 2) & ~2 : rare; PINS(rare2); }
        if (q == 11) setm0(lB);
      }
      if (book && q == 10 && (LATE ? sub : !sub)) { dB = -dB; PINS(dB); }
      if (!LATE && EB > 0) {
        if (k == 4 * NT - 2 - EB) { vmwait(); WAIT_L0(); }
        if (k == 4 * NT - 1 - EB) { __builtin_amdgcn_s_setprio(2); BAR(); }
      }
      SB();
    }
    __builtin_amdgcn_s_setprio(0);
    if (!LATE && EB == 0) { vmwait(); WAIT_L0(); BAR(); }
  };
  constexpr int LM = AC ? 1 : 2;
  if (!LATE) { upB(2, 5); dB = -dB; }
  BAR();
  if (LATE) {
    if (AC) { setm0(lA); SB(); }
    rdB(0, 0); rdB(1, 0); SB();
    if (AC) pieceA(0);
  }
  setm0(lB);
  SB();
  if (LATE) { rdB(2, 0); rdB(3, 0); } else asm volatile("s_nop 0");
  SB();
  constexpr int X = TYPE == 0;
  const int rend = c.bstar - (X ? 7 : 8);
  const u32x4 rw4 = srd(ws, WS_END);
  u32x2 fl = {0u, 0u};
#pragma unroll 1
  do {
    if (X && __builtin_expect(rare == rend + 1, 0)) ld8_hidden(fl, rw4, foff);
    L(0, LM);
    if (__builtin_expect(rare == 0, 0)) stats(0);
    U(0);
    C(0, true, 1, []() { WAIT_V(8); });
    L(1, LM);
    if (__builtin_expect(rare2 == 0, 0)) {
      PINS(rare);
      if (rare == 0) stats(1);
      else if (LATE && c.w == 0 && lane == 0) set_flag(ws, OFF_F1 + (c.Mt * 16 + c.cslot) * 8, tag);
    }
    U(1);
    C(1, true, 1, []() { WAIT_V(8); });
  } while (rare != rend);
  auto check = [&]() {
    PIN(fl);
    if (__builtin_expect(!__all(__builtin_bit_cast(u64, fl) == tag), 0)) wait_flags(ws, foff, tag);
  };
  if (X) {
    L(0, LM);
    WAIT_V(11);
    check();
    pf.items<13, 16>();
    U(0);
    C(0, true, 1, []() { WAIT_V(11); });
    L(1, LM);
    pf.items<16, 19>();
    U(1);
    C(1, true, 1, []() { WAIT_V(14); });
  }
  if (!X) ld8_hidden(fl, rw4, foff);
  L(0, 1);
  if (X) pf.items<19, 22>();
  U(0);
  C(0, true, 2, []() { wait_v<X ? 13 : 8>(); });
  L(1, 0);
  wait_v<X ? 6 : 3>();
  if (!X) check();
  dma_sc1(rw4, lane * 16, OFF_SC + c.cslot * 15360 + c.w * 1024, c.ldsb + L1_GC + c.w * 1024);
  dma_sc1(rw4, lane * 16, OFF_SC + c.cslot * 15360 + c.w * 1024 + 8192, c.ldsb + L1_GC + c.w * 1024 + 8192);
  dma_sc1(rw4, lane * 16, OFF_SR + c.Mt * 10240 + c.w * 1024, c.ldsb + L1_GR + c.w * 1024);
  if (LATE && c.w < 2) dma_sc1(rw4, lane * 16, OFF_SR + c.Mt * 10240 + c.w * 1024 + 8192, c.ldsb + L1_GR + c.w * 1024 + 8192);
  if (X) pf.items<22, 24>();
  U(1);
  C(1, true, 2, []() {});
  L(0, 0);
  if (TYPE != 1) wd_prefetch<0, 4>(rw4, c.ldsb, c.j, c.w, lane);
  if (X) pf.items<24, 26>();
  C(0, true, 0, []() { wait_v<X ? 8 : TYPE != 1 ? 4 : 0>(); });
  L(1, 0);
  {
    __builtin_amdgcn_s_setprio(2);
    if (LATE || c.w < 6) {
      const LAS float* src = (const LAS float*)(size_t)(c.ldsb + L1_GC + (c.w * 64 + lane) * 4);
      float v[10];
#pragma unroll
      for (int p = 0; p < 10; ++p) v[p] = src[p * 384];
      *(LAS float*)(size_t)(c.ldsb + L1_CS + (c.w * 64 + lane) * 4) = (((v[0] + v[1]) + (v[2] + v[3])) + ((v[4] + v[5]) + (v[6] + v[7]))) + (v[8] + v[9]);
    } else {
      const LAS f32x2* src = (const LAS f32x2*)(size_t)(c.ldsb + L1_GR + ((c.w - 6) * 64 + lane) * 8);
      f32x2 v[10];
#pragma unroll
      for (int p = 0; p < 10; ++p) v[p] = src[p * 128];
      const f32x2 s = ((((v[0] + v[1]) + (v[2] + v[3])) + ((v[4] + v[5]) + (v[6] + v[7]))) + (v[8] + v[9])) * (1.0f / kD);
      LAS float* d = (LAS float*)(size_t)(c.ldsb + L1_ST + ((c.w - 6) * 64 + lane) * 4);
      d[0] = s[0]; d[128] = __builtin_amdgcn_rsqf(fmaxf(__builtin_fmaf(-s[0], s[0], s[1]), 0.f) + 1e-5f);
    }
    __builtin_amdgcn_s_setprio(0);
  }
  SB();
  if (TYPE != 1) wd_prefetch<4, 6>(rw4, c.ldsb, c.j, c.w, lane);
  if (X) pf.items<26, 28>();
  SB();
  C(1, false, 0, []() {});
}

template <int N, class F> DEVI void static_for(F&& f) {
  if constexpr (N > 0) { static_for<N - 1>(f); f(std::integral_constant<int, N - 1>{}); }
}
DEVI u32x4 pair16(u32x2 a, u32x2 b) {
  const auto s0 = __builtin_amdgcn_permlane32_swap(a[0], b[0], false, false);
  const auto s1 = __builtin_amdgcn_permlane32_swap(a[1], b[1], false, false);
  return u32x4{s0[0], s1[0], s0[1], s1[1]};
}
struct Watch {
  LAS unsigned* lf; char* ws; u64 tag; int mt2, j, w, lane;
  unsigned status;
  DEVI u64 fetch() {
    int l2 = lane;
    PIN(l2);
    const unsigned f0 = l2 < 32 ? OFF_FH + mt2 * 256 + (j >> 2) * 128 + (l2 < 11 ? l2 : 0) * 8 : OFF_F3 + (blockIdx.x & 7) * 128 + (l2 & 15) * 8;
    return __hip_atomic_load((GAS u64*)(ws + f0), RLX_AGENT);
  }
  DEVI bool eval(u64 v, bool giveup) {
    const u64 no = __ballot(v != tag);
    unsigned s = ((no & 0x1ffull) ? 0u : S_H) | ((no & 0x600ull) ? 0u : S_Q) | ((no >> 32) ? 0u : S_M);
    if (giveup) s = 7u;
    if (s == status) return false;
    status = s;
    __hip_atomic_store(lf + LF_STATUS, s, RLX_WG);
    return true;
  }
  DEVI bool poll(bool giveup) { return eval(fetch(), giveup); }
  DEVI bool has(unsigned bits) { return (~(unsigned)__builtin_amdgcn_readfirstlane(__hip_atomic_load(lf + LF_STATUS, RLX_WG)) & bits) == 0u; }
  int pcnt, plast, preps; unsigned pflag;
  DEVI void publish() {
    if ((int)lds_inc(lf + LF_PUB + pcnt, lane) == plast && lane < preps) set_flag(ws, pflag + lane * 128, tag);
    preps = 0;
  }
  DEVI void need(unsigned bits, int watcher = 7) {
    if (w != watcher) { lds_wait_bits(lf + LF_STATUS, bits); return; }
#pragma unroll 1
    for (unsigned spins = 0; (~status & bits) != 0u; ++spins)
      if (!poll(spins >= (1u << 20))) __builtin_amdgcn_s_sleep(1);
    asm volatile("" ::: "memory");
  }
};

#define EPI_N 12
DEVI void epi_ffn(LAS char* lds, f32x4 (&acc)[4][6], char* ws, rsrc_t rws, Watch& wt, const P1& c, PF2& pf, int lane) {
  const int r = lane & 15, g = lane >> 4;
  pf.items<0, 2>();
  float nm[4], k1[4], k2[4], iv[4];
#pragma unroll
  for (int mi = 0; mi < 4; ++mi) {
    const LAS float* sp = (const LAS float*)(lds + L1_ST) + c.mw * 64 + mi * 16 + r;
    const float rs = sp[128], c0 = -2.0f * 0.7978845608028654f * 1.4426950408889634f;
    nm[mi] = -sp[0]; k1[mi] = c0 * rs; k2[mi] = c0 * 0.044715f * rs * rs * rs; iv[mi] = __builtin_amdgcn_rcpf(rs * rs);
  }
  const unsigned hb = OFF_H + ((c.Mt * 2 + c.mw) * 54 + 2 * c.cslot + (c.nw >> 1)) * 4096 + (c.nw & 1) * 512 + (lane & 31) * 16 + (lane >> 5) * 1024;
  u32x4 v[6];
  static_for<3>([&](auto I) {
    constexpr int i = I;
    const f32x4 cg = *(LAS f32x4*)(lds + L1_CS + ((c.nw * 6 + i) * 16 + 4 * g) * 4);
    const f32x4 cu = *(LAS f32x4*)(lds + L1_CS + ((c.nw * 6 + 3 + i) * 16 + 4 * g) * 4);
    u32x2 o[4];
    static_for<4>([&](auto MI) {
      constexpr int mi = MI, n = 4 * i + mi;
      if constexpr (2 + 2 * n < EPI_N) pf.template items<2 + 2 * n, 4 + 2 * n>();
#pragma unroll
      for (int hh = 0; hh < 2; ++hh) {
        const f32x2 G = fma2(bc2(nm[mi]), hh ? hi2(cg) : lo2(cg), hh ? hi2(acc[mi][i]) : lo2(acc[mi][i]));
        const f32x2 U = fma2(bc2(nm[mi]), hh ? hi2(cu) : lo2(cu), hh ? hi2(acc[mi][3 + i]) : lo2(acc[mi][3 + i]));
        const f32x2 z = G * fma2(G * G, bc2(k2[mi]), bc2(k1[mi]));
        const f32x2 d = fma2(f32x2{__builtin_amdgcn_exp2f(z[0]), __builtin_amdgcn_exp2f(z[1])}, bc2(iv[mi]), bc2(iv[mi]));
        o[mi][hh] = pk2(G * U * f32x2{__builtin_amdgcn_rcpf(d[0]), __builtin_amdgcn_rcpf(d[1])});
      }
      PIN(o[mi]);
      SB();
#define STN 4
#define STS 4
      if constexpr (n == STN || n == STN + STS) {
        constexpr int p = (n - STN) / STS;
        st16(rws, hb, p * 18 * 4096, v[2 * p]);
        st16(rws, hb, p * 18 * 4096 + 2048, v[2 * p + 1]);
        SB();
      }
    });
    v[2 * i] = pair16(o[0], o[1]); v[2 * i + 1] = pair16(o[2], o[3]);
    PIN(v[2 * i]); PIN(v[2 * i + 1]);
  });
  st16(rws, hb, 36 * 4096, v[4]);
  st16(rws, hb, 36 * 4096 + 2048, v[5]);
  STAMP(4);
  BAR();
  WAIT_V(0);
  wt.pcnt = c.mw; wt.plast = 3; wt.preps = 2; wt.pflag = OFF_FH + (c.Mt * 2 + c.mw) * 256 + c.cslot * 8;
  wt.publish();
  STAMP(5);
  pf.items<EPI_N, 13>();
  WAIT_V(0);
}

DEVI f32x4 sweep8(rsrc_t rws, unsigned voff, unsigned soff, unsigned stride, unsigned tag) {
  u32x4 gr[8];
#pragma unroll 1
  for (unsigned spins = 0; spins < (1u << 20); ++spins) {
#pragma unroll
    for (int p = 0; p < 8; ++p) gr[p] = ld16(rws, voff, soff + p * stride);
    SB();
    unsigned bad = 0;
#pragma unroll
    for (int p = 0; p < 8; ++p) bad |= (gr[p][1] ^ tag) | (gr[p][3] ^ tag);
    PIN(bad);
    if (__all(bad == 0u || voff == OOB)) break;
    asm volatile("" ::: "memory");
  }
  f32x2 a[8], b[8];
#pragma unroll
  for (int p = 0; p < 8; ++p) { a[p] = unpk(gr[p][0]); b[p] = unpk(gr[p][2]); }
  const f32x2 sa = ((a[0] + a[1]) + (a[2] + a[3])) + ((a[4] + a[5]) + (a[6] + a[7]));
  const f32x2 sb = ((b[0] + b[1]) + (b[2] + b[3])) + ((b[4] + b[5]) + (b[6] + b[7]));
  return f32x4{sa[0], sa[1], sb[0], sb[1]};
}

DEVI void epi_q(LAS char* lds, f32x4 (&acc)[4][6], char* ws, rsrc_t rws, Watch& wt, const P1& c, PF2& pf, int lane) {
  const int r = lane & 15, g = lane >> 4, qi = c.cslot - 13;
  f32x4 cs[5];
#pragma unroll
  for (int i = 0; i < 5; ++i) cs[i] = *(LAS f32x4*)(lds + L1_CS + ((c.nw * 6 + i) * 16 + 4 * g) * 4);
  pf.items<0, 8>();
  static_for<4>([&](auto MI) {
    constexpr int mi = MI;
    const LAS float* sp = (const LAS float*)(lds + L1_ST) + c.mw * 64 + mi * 16 + r;
    const f32x2 nm = bc2(-sp[0]), rs = bc2(sp[128]);
    static_for<5>([&](auto I) {
      constexpr int i = I;
      const f32x2 a = phi2(fma2(nm, lo2(cs[i]), lo2(acc[mi][i])), rs), b = phi2(fma2(nm, hi2(cs[i]), hi2(acc[mi][i])), rs);
      acc[mi][i] = f32x4{a[0], a[1], b[0], b[1]};
      PIN(acc[mi][i]);
      SB();
      pf.template item<8 + mi * 5 + i>();
    });
  });
  STAMP(3);
  LAS unsigned* lf = (LAS unsigned*)(lds + L2_FLAG);
  if (c.w == 4) { wait_flags(ws, OFF_FZ + (blockIdx.x & 7) * 512 + qi * 256 + (lane & 31) * 8, wt.tag); lds_inc(lf + LF_K, lane); }
  else lds_wait(lf + LF_K, 1u);
  STAMP(12);
  {
    const int q4 = (c.w & 1) * 64 + lane;
    const f32x4 zs = sweep8(rws, q4 < 40 ? q4 * 16 : q4 < 80 ? q4 * 16 + 31 * 640 : OOB, OFF_ZP + (qi * 64 + (c.w >> 1) * 8) * 640, 640, (unsigned)wt.tag);
    if (q4 < 80) *(LAS f32x4*)(lds + L2_RED + (c.w >> 1) * 1280 + q4 * 16) = zs;
    WAIT_L0(); BAR();
  }
  u64 fv = 0;
  STAMP(5);
  f32x4 z[5];
#pragma unroll
  for (int i = 0; i < 5; ++i) {
    const LAS char* p = lds + L2_RED + (c.nw * 80 + 20 * (2 * (g & 1) + (g >> 1)) + 4 * i) * 4;
    z[i] = (*(const LAS f32x4*)p + *(const LAS f32x4*)(p + 1280)) + (*(const LAS f32x4*)(p + 2560) + *(const LAS f32x4*)(p + 3840));
  }
  const unsigned qb = OFF_Q + ((c.Mt * 2 + c.mw) * 20 + (2 * qi + (c.nw >> 1)) * 5) * 4096;
  u32x2 keep;
#pragma unroll
  for (int mi = 0; mi < 4; ++mi) {
    f32x2 d0 = lo2(acc[mi][0]) * lo2(z[0]), d1 = hi2(acc[mi][0]) * hi2(z[0]);
#pragma unroll
    for (int i = 1; i < 5; ++i) { d0 = fma2(lo2(acc[mi][i]), lo2(z[i]), d0); d1 = fma2(hi2(acc[mi][i]), hi2(z[i]), d1); }
    d0 += d1;
    const f32x2 inv = bc2(__builtin_amdgcn_rcpf(d0[0] + d0[1] + 1e-6f));
    unsigned o[10];
#pragma unroll
    for (int i = 0; i < 5; ++i) { o[2 * i] = pk2(lo2(acc[mi][i]) * inv); o[2 * i + 1] = pk2(hi2(acc[mi][i]) * inv); }
    st16(rws, lane * 16 + mi * 1024, qb + (c.nw & 1) * 12288, u32x4{o[0], o[1], o[2], o[3]});
    st16(rws, lane * 16 + mi * 1024, qb + (c.nw & 1) * 12288 + 4096, u32x4{o[4], o[5], o[6], o[7]});
    if (mi & 1) st16(rws, (lane & 31) * 16 + (lane >> 5) * 1024 + (mi - 1) * 1024, qb + 8192 + (c.nw & 1) * 512, pair16(keep, u32x2{o[8], o[9]}));
    else keep = u32x2{o[8], o[9]};
    if (mi == 0 && c.w == 7) { SB(); fv = wt.fetch(); SB(); }
  }
  STAMP(4);
  SB();
  if (c.w == 7) wt.eval(fv, false);
  WAIT_L0(); BAR();
  wt.pcnt = c.mw; wt.plast = 3; wt.preps = 2; wt.pflag = OFF_FH + (c.Mt * 2 + c.mw) * 256 + (9 + qi) * 8;
}

DEVI void epi_kv(LAS char* lds, f32x4 (&acc)[4][6], char* ws, rsrc_t rws, u64 tag, const P1& c, int lane) {
  const int cl = lane & 15, g = lane >> 4, kj = c.cslot - 9;
  float cs[5];
#pragma unroll
  for (int i = 0; i < 5; ++i) cs[i] = -*(LAS float*)(lds + L1_CS + ((c.nw * 6 + i) * 16 + cl) * 4);
  f32x4 mean[4], rstd[4];
#pragma unroll
  for (int mi = 0; mi < 4; ++mi) {
    mean[mi] = *(LAS f32x4*)(lds + L1_ST + (c.mw * 64 + mi * 16 + 4 * g) * 4);
    rstd[mi] = *(LAS f32x4*)(lds + L1_ST + 512 + (c.mw * 64 + mi * 16 + 4 * g) * 4);
  }
  u32x4 fk[2][3], fv[2][3];
  auto center = [&](int mi, int i, int hh) { return fma2(hh ? hi2(mean[mi]) : lo2(mean[mi]), bc2(cs[i]), hh ? hi2(acc[mi][i]) : lo2(acc[mi][i])); };
#pragma unroll
  for (int mi = 0; mi < 4; ++mi)
#pragma unroll
    for (int f = 0; f < 3; ++f)
#pragma unroll
      for (int hh = 0; hh < 2; ++hh)
        fk[mi >> 1][f][(mi & 1) * 2 + hh] = pk2(phi2(center(mi, f < 2 ? f : 4, hh), hh ? hi2(rstd[mi]) : lo2(rstd[mi])));
  const int uh = cl < 4 ? 16 + cl : 32 + cl;
  const f32x4 zero = {0.f, 0.f, 0.f, 0.f};
  {
    const u32x4 one = {0x3f803f80u, 0x3f803f80u, 0x3f803f80u, 0x3f803f80u};
    f32x4 z[3];
#pragma unroll
    for (int f = 0; f < 3; ++f) z[f] = mfma(one, fk[1][f], mfma(one, fk[0][f], zero));
    const float v = g == 0 ? z[0][0] : g == 1 ? z[1][0] : z[2][0];
    const float vn = __builtin_bit_cast(float, __builtin_amdgcn_mov_dpp(__builtin_bit_cast(int, v), 0xB1, 0xf, 0xf, true));
    st8(rws, (cl & 1) ? OOB : g < 2 ? (g * 20 + cl) * 4 : (g == 2 && cl < 8) ? uh * 4 : OOB,
        OFF_ZP + (kj * 32 + c.Mt * 2 + c.mw) * 640 + c.nw * 160, u32x2{pk2(v, vn), (unsigned)tag});
    if (lds_inc((LAS unsigned*)(lds + L2_FLAG) + LF_PUB, lane) == 7u && lane < 8) set_flag(ws, OFF_FZ + lane * 512 + kj * 128 + c.Mt * 8, tag);
    SB();
  }
#pragma unroll
  for (int mi = 0; mi < 4; ++mi)
#pragma unroll
    for (int f = 0; f < 3; ++f)
#pragma unroll
      for (int hh = 0; hh < 2; ++hh)
        fv[mi >> 1][f][(mi & 1) * 2 + hh] = pk2(center(mi, 2 + f, hh) * (hh ? hi2(rstd[mi]) : lo2(rstd[mi])));
  constexpr int KI[7] = {0, 0, 2, 1, 1, 2, 2}, VI[7] = {0, 2, 0, 1, 2, 1, 2};
  f32x4 p[7];
#pragma unroll
  for (int q = 0; q < 7; ++q) p[q] = mfma(fv[1][VI[q]], fk[1][KI[q]], mfma(fv[0][VI[q]], fk[0][KI[q]], zero));
  LAS char* ex = lds + L1_EX + c.w * 3584;
  auto put = [&](bool ok, int u, int e, f32x4 v) { *(LAS f32x4*)(ex + (ok ? (u * 20 + e) * 4 : 3200)) = v; };
  put(true, cl, 4 * g, p[0]);
  put(true, 20 + cl, 4 * g, p[3]);
  put(g >= 2, (g & 1) * 20 + cl, 16, g == 2 ? p[1] : p[4]);
  put(cl < 8, uh, 4 * g, cl < 4 ? p[2] : p[5]);
  put(cl < 8 && g == 2 + (cl >> 2), uh, 16, p[6]);
  WAIT_L0(); BAR();
#pragma unroll
  for (int q = 0; q < 2; ++q) {
    const int o = (c.mw * 128 + q * 64 + lane) * 16;
    const LAS char* src = lds + L1_EX + c.nw * 3584 + (o < 3200 ? o : 0);
    const f32x4 v = *(const LAS f32x4*)src + *(const LAS f32x4*)(src + 4 * 3584);
    st16(rws, o < 3200 ? o : OOB, OFF_KVP + ((kj * 4 + c.nw) * 16 + c.Mt) * 3456,
         u32x4{pk2(lo2(v)), (unsigned)tag, pk2(hi2(v)), (unsigned)tag});
  }
  SB();
  STAMP(4);
  LAS unsigned* lf = (LAS unsigned*)(lds + L2_FLAG);
  if (lds_inc(lf + LF_PUB + 1, lane) == 7u && lane < 8) set_flag(ws, OFF_FK + lane * 512 + kj * 128 + c.Mt * 8, tag);
  BAR();
}

DEVI void retile(const char* wd, rsrc_t rws, unsigned ldsw, int id, int w, int lane) {
  const u32x4 rd = srd(wd, kF * 1280);
  const int jj = lane >> 3, g = lane >> 4;
  const unsigned vo = ((jj >> 2) * 8 + (jj & 3)) * 1280 + (lane & 7) * 16;
  const unsigned ra = ldsw + g * 1024 + ((lane & 15) >> 2) * 128 + (lane & 3) * 8;
  auto fetch = [&](int n) {
    const int it = id * 8 + w + n * 128, rb = it / 10, ln = it - rb * 10;
#pragma unroll
    for (int q = 0; q < 4; ++q)
      dma<0>(rd, vo, it < 540 ? (rb * 32 + 16 * (q >> 1) + 4 * (q & 1)) * 1280 + ln * 128 : OOB, ldsw + (n & 3) * 4096 + q * 1024);
  };
  auto put = [&](int n) {
    const int it = id * 8 + w + n * 128, rb = it / 10, ln = it - rb * 10, cg = rb / 6, uu = rb - cg * 6;
    const int ks = 18 * (uu >> 1) + 2 * cg + (uu & 1);
#pragma unroll
    for (int q = 0; q < 4; ++q) {
      const int nt = ln * 4 + q, j = nt / 5, t = nt - j * 5;
      st16(rws, lane * 16, it < 540 ? OFF_WD + ((j * 54 + ks) * 5 + t) * 1024 : OOB, lds_trfrag(ra + (n & 3) * 4096 + q * 32));
    }
  };
#pragma unroll 1
  for (int n = 0; n < 4; ++n) fetch(n);
  WAIT_V(0);
  put(0);
  WAIT_L0();
  fetch(4);
#pragma unroll 1
  for (int n = 1; n < 5; ++n) {
    if (n == 4) WAIT_V(0);
    put(n);
  }
}

DEVI void fold_kv(LAS char* lds, char* ws, rsrc_t rws, Watch& wt, const u32x4 (&wf)[2][5], int hp, int w, int lane) {
  const u64 tag = wt.tag;
  const int cl = lane & 15, g = lane >> 4;
  const int o = ((w & 3) * 64 + lane) * 16;
  unsigned po = o < 3200 ? o : OOB, ko[6][2];
#pragma unroll
  for (int f = 0; f < 6; ++f) {
    const int u = 20 * (f & 1) + (f < 4 ? (cl & 4) * 2 + 4 * (f >> 1) : 16) + (cl & 3);
    const bool mine = f >= 4 || (cl >> 3) == (f & 1);
    ko[f][0] = (unsigned)(size_t)lds + L2_RED + (mine && g < 3 ? u * 80 + g * 32 : 3360);
    ko[f][1] = (unsigned)(size_t)lds + L2_RED + (mine && g < 2 ? u * 80 + g * 32 + 16 : 3360);
    PIN(ko[f][0]); PIN(ko[f][1]);
  }
  unsigned vo = OFF_MP + (w * 20 + (hp >> 2) * 5) * 5120 + (3 * ((hp >> 1) & 1) + (g & 1)) * 5120 + (((hp & 1) + (g & 2)) * 16 + cl) * 16;
  unsigned vo2 = OFF_MP + (w * 20 + (hp >> 2) * 5 + 2) * 5120 + g * 1024 + ((hp & 3) * 16 + cl) * 16;
  PIN(po); PIN(vo); PIN(vo2);
  WAIT_V(0);
  if (w == 0) wait_flags(ws, OFF_FK + (blockIdx.x & 7) * 512 + (hp >> 2) * 128 + cl * 8, tag);
  BAR();
  STAMP(2);
  {
    *(LAS f32x4*)(lds + L2_RED + (w >> 2) * 4096 + o) = sweep8(rws, po, OFF_KVP + (hp * 16 + (w >> 2) * 8) * 3456, 3456, (unsigned)tag);
    WAIT_L0(); BAR();
  }
  STAMP(12);
  u64 fv = 0;
  if (w == 7) fv = wt.fetch();
  SB();
  u32x4 kf[6];
#pragma unroll
  for (int f = 0; f < 6; ++f) {
    const f32x4 lo = *(LAS f32x4*)(size_t)ko[f][0] + *(LAS f32x4*)(size_t)(ko[f][0] + 4096);
    const f32x4 hi = *(LAS f32x4*)(size_t)ko[f][1] + *(LAS f32x4*)(size_t)(ko[f][1] + 4096);
    kf[f] = u32x4{pk2(lo2(lo)), pk2(hi2(lo)), pk2(lo2(hi)), pk2(hi2(hi))};
  }
  const f32x4 zero = {0.f, 0.f, 0.f, 0.f};
  u32x4 r1;
#pragma unroll
  for (int i = 0; i < 5; ++i) {
    const f32x4 d1 = mfma(kf[1], wf[1][i], mfma(kf[0], wf[0][i], zero)), d2 = mfma(kf[3], wf[1][i], mfma(kf[2], wf[0][i], zero));
    const f32x4 d3 = mfma(kf[4], wf[0][i], zero), d4 = mfma(kf[5], wf[1][i], zero);
    st16(rws, vo, i * 1024, u32x4{pk2(lo2(d1)), pk2(hi2(d1)), pk2(lo2(d2)), pk2(hi2(d2))});
    const u32x4 t = {pk2(lo2(d3)), pk2(hi2(d3)), pk2(lo2(d4)), pk2(hi2(d4))};
    if (i == 0 || i == 4) r1 = t; else r1 = g == i ? t : r1;
    if (i == 3) st16(rws, vo2, 0, r1);
    if (i == 4) st16(rws, g == 0 ? vo2 + 4096 : OOB, 0, r1);
  }
  STAMP(3);
  SB();
  if (w == 7) wt.eval(fv, false);
  WAIT_L0(); BAR();
  wt.pcnt = 0; wt.plast = 7; wt.preps = 8; wt.pflag = OFF_F3 + hp * 8;
}

#define ASLEEP 0
#define FPRE 3
template <bool ATT>
DEVI void p2_steps(LAS char* lds, rsrc_t rws, Watch& wt, const u32x4 (&br)[3][5], f32x4 (&acc)[4][5], const u32x2 (&xr)[5],
                   int mt2, int j, int w, int lane) {
  LAS unsigned* lf = wt.lf;
  const unsigned ab = OFF_H + (mt2 * 54 + w) * 4096;
  u32x4 fa[12][4], fb[12][5];
  auto ldA = [&](int n) {
    const unsigned so = n >= 7 ? OFF_Q + (mt2 * 20 + (w - 4) * 5 + n - 7) * 4096
                               : n == 6 && w >= 6 ? OOB : ab + (n < 3 ? 30 + 8 * ((n + (mt2 >> 1)) % 3) : 8 * n - 24) * 4096;
#pragma unroll
    for (int m = 0; m < 4; ++m) fa[n][m] = ld16(rws, lane * 16, so + ((m + w) & 3) * 1024);
    SB();
  };
  unsigned lb = (unsigned)(size_t)lds + w * 5120 + lane * 16;
  auto rdB = [&](int n) {
    if (n == 5) { lb += 81920; PIN(lb); }
    if (ATT && n == 6) { lb -= w >= 6 ? 40960 : 0; PIN(lb); }
#pragma unroll
    for (int t = 0; t < 5; ++t) fb[n][t] = lds_b128(lb + ((n - 3) & 1) * 40960 + t * 1024);
    SB();
  };
  auto ld1 = [&](int kind, int n, int i) {
    if (kind == 0) {
      const unsigned so = n >= 7 ? OFF_Q + (mt2 * 20 + (w - 4) * 5 + n - 7) * 4096
                                 : n == 6 && w >= 6 ? OOB : ab + (n < 3 ? 30 + 8 * ((n + (mt2 >> 1)) % 3) : 8 * n - 24) * 4096;
      fa[n][i] = ld16(rws, lane * 16, so + ((i + w) & 3) * 1024);
    } else fb[n][i] = ld16(rws, lane * 16, OFF_MP + (j * 20 + (w - 4) * 5 + n - 7) * 5120 + i * 1024);
    SB();
  };
  LAS char* ex = lds + w * 5120 + lane * 16;
  constexpr int PRE = ATT ? 6 : FPRE;
  if (ATT && ASLEEP && wt.preps == 0) __builtin_amdgcn_s_sleep(ASLEEP);
#pragma unroll
  for (int n = 0; n < PRE; ++n) {
    ldA(n);
    if (n == 1 && wt.preps != 0) { WAIT_V(8); wt.publish(); STAMP(13); }
  }
  if (ATT) {
#pragma unroll
    for (int t = 0; t < 5; ++t) {
      const f32x2 x0 = unpk(xr[t][0]), x1 = unpk(xr[t][1]);
      acc[0][t] = f32x4{x0[0], x0[1], x1[0], x1[1]};
      PIN(acc[0][t]);
    }
    SB();
    asm volatile("s_nop 1" ::: "memory");
  }
  constexpr int A1[12] = {ATT ? -1 : FPRE, ATT ? 6 : FPRE == 3 ? 5 : FPRE == 4 ? 6 : -1, ATT ? 7 : -1, -1, ATT ? 8 : -1, -1, ATT ? 9 : -1, 10, 11, -1, -1, -1};
  constexpr int A2[12] = {ATT ? -1 : FPRE + 1, ATT || FPRE > 3 ? -1 : 6, -1, -1, -1, -1, -1, -1, -1, -1, -1, -1};
  constexpr int M1[12] = {-1, -1, -1, ATT ? 7 : -1, -1, ATT ? 8 : -1, ATT ? 9 : -1, 10, 11, -1, -1, -1};
  u64 fv = 0;
  static_for<(ATT ? 12 : 7)>([&](auto N) {
    constexpr int n = N;
    if (n == 2) lds_wait(lf + LF_DMA, 8u);
    if (ATT && n == 2) lds_wait_bits(lf + LF_STATUS, S_Q);
    if (ATT && n == 3) { lds_wait_bits(lf + LF_STATUS, S_M); STAMP(7); }
    if (!ATT && (n == 3 || n == 5) && w == 3) { if (n == 5) wt.eval(fv, false); fv = wt.fetch(); SB(); }
    if (n >= 2 && n < 6) rdB(n + 1);
    if (n == (ATT ? 11 : 6)) lds_wait(lf + LF_BC + ATT, 4u);
#pragma unroll
    for (int q = 0; q < 20; ++q) {
      constexpr int LASTN = ATT ? 11 : 6;
      const int m = n == LASTN ? (q / 5 + 1) & 3 : n < 7 ? q / 5 : q & 3, t = n < 7 || n == LASTN ? q % 5 : q >> 2;
      if (n == 0 && (m > 0 || !ATT)) mfma_new(acc[m][t], br[0][t], fa[0][m]);
      else mfma_acc(acc[m][t], n < 3 ? br[n < 3 ? n : 0][t] : fb[n][t], fa[n][m]);
      if (n == LASTN && q >= 10) {
        SB();
        *(LAS f32x4*)(ex + ((q - 10) / 5) * 40960 + (q % 5) * 1024) = acc[(q - 10) / 5 + 1][q % 5];
        SB();
      }
      if (q & 1) {
        const int p = q >> 1, na = (A1[n] >= 0 ? 4 : 0) + (A2[n] >= 0 ? 4 : 0);
        if (p < na) ld1(0, p < 4 ? A1[n] : A2[n], p & 3);
        else if (M1[n] >= 0 && p - na < 5) ld1(1, M1[n], p - na);
      }
    }
    SB();
    if (n == 0) STAMP(9);
    if (n == 6) STAMP(11);
    if (n == 5) { WAIT_L0(); lds_inc(lf + LF_BC + ATT, lane); }
  });
  if (!ATT && w == 3) wt.eval(fv, false);
}

DEVI void phase2(LAS char* lds, const char* x, char* ws, rsrc_t rws, char* out, Watch& wt, u32x4 (&br)[3][5],
                 int mt2, int j, int w, int lane) {
  const int a = w & 3;
  LAS unsigned* lf = wt.lf;
  const unsigned obase = (mt2 * 64 + a * 16) * 1280 + j * 160;
  f32x4 acc[4][5];
  u32x2 xr[5];
  if (wt.preps != 0) { if (w == 7 && !(wt.status & S_H)) wt.poll(false); WAIT_V(0); wt.publish(); STAMP(13); }
  else WAIT_V(15);
  lds_inc(lf + LF_DMA, lane);
  wt.need(S_H);
  STAMP(6);
  if (w >= 4) {
    const rsrc_t rx = __builtin_amdgcn_make_buffer_rsrc((void*)x, 0, 2048 * 1280, 0x00020000);
#pragma unroll
    for (int t = 0; t < 5; ++t) xr[t] = __builtin_amdgcn_raw_buffer_load_b64(rx, (lane & 15) * 1280 + (lane >> 4) * 8 + t * 32, obase, 0);
    SB();
  }
  if (w < 4) {
    __builtin_amdgcn_s_setprio(2);
    p2_steps<false>(lds, rws, wt, br, acc, xr, mt2, j, w, lane);
    __builtin_amdgcn_s_setprio(0);
#pragma unroll
    for (int q = 0; q < 15; ++q) asm volatile("" : "=v"(br[q / 5][q % 5]));
  }
  PINS(w);
  if (w >= 4) { __builtin_amdgcn_s_setprio(1); p2_steps<true>(lds, rws, wt, br, acc, xr, mt2, j, w, lane); }
  asm volatile("s_nop 7" ::: "memory");
  SB();
  STAMP(14);
  LAS char* ex = lds + (w >> 2) * 20480 + lane * 16;
#pragma unroll
  for (int t = 0; t < 5; ++t) *(LAS f32x4*)(ex + 2 * 40960 + (a * 5 + t) * 1024) = acc[3][t];
  WAIT_L0();
  if (w < 4) { lds_inc(lf + LF_X, lane); if (w == 3) wt.need(S_Q | S_M, 3); lds_wait(lf + LF_X, 4u); }
  else BAR();
  STAMP(8);
#pragma unroll
  for (int t = 0; t < 5; ++t)
#pragma unroll
    for (int q = 1; q < 4; ++q) acc[0][t] += *(LAS f32x4*)(ex + (q - 1) * 40960 + (((a - q) & 3) * 5 + t) * 1024);
  LAS char* base = lds + L2_BASE + a * 5120 + lane * 16;
  if (w < 4) {
#pragma unroll
    for (int t = 0; t < 5; ++t) *(LAS f32x4*)(base + t * 1024) = acc[0][t];
    WAIT_L0();
  } else {
    const rsrc_t ro = __builtin_amdgcn_make_buffer_rsrc(out, 0, 2048 * 1280, 0x00020000);
    u32x2 o[5];
#pragma unroll
    for (int t = 0; t < 5; ++t) {
      const f32x4 s = acc[0][t] + *(LAS f32x4*)(base + t * 1024);
      o[t] = u32x2{pk2(lo2(s)), pk2(hi2(s))};
    }
    const unsigned orow = obase + (lane & 15) * 1280;
#pragma unroll
    for (int t = 0; t < 4; t += 2) {
      const auto s0 = __builtin_amdgcn_permlane16_swap(o[t][0], o[t + 1][0], false, false);
      const auto s1 = __builtin_amdgcn_permlane16_swap(o[t][1], o[t + 1][1], false, false);
      __builtin_amdgcn_raw_buffer_store_b128(u32x4{s0[0], s1[0], s0[1], s1[1]}, ro, orow + ((lane >> 4) & 1) * 32 + (lane >> 5) * 16 + t * 32, 0, OUT_AUX);
    }
    __builtin_amdgcn_raw_buffer_store_b64(o[4], ro, orow + (lane >> 4) * 8 + 128, 0, OUT_AUX);
  }
}

__global__ __launch_bounds__(512, 2) void fused(const char* x, const char* wqkv, const char* wo, const char* wg,
                                                const char* wu, const char* wd, char* ws, char* out) {
  extern __shared__ __attribute__((aligned(16))) char smem[];
  const char* pc0;
  asm volatile("s_getpc_b64 %0" : "=s"(pc0));
  asm volatile("" : "+s"(x), "+s"(wqkv), "+s"(wo), "+s"(wg), "+s"(wu), "+s"(wd), "+s"(ws), "+s"(out));
  LAS char* lds = (LAS char*)smem;
  const unsigned ldsb = (unsigned)(size_t)lds;
  const int bid = blockIdx.x, xcd = bid & 7, idx = bid >> 3;
  const int Mt = (xcd & 1) * 8 + (idx & 7), grp = xcd >> 1, pos4 = idx >> 3, slot2 = grp * 4 + pos4;
  const int cslot = (int)(0xCF76BE54AD329810ull >> (4 * (pos4 == 2 ? (slot2 + 4 * (idx >> SPR)) & 15 : slot2))) & 15;
  const int w = __builtin_amdgcn_readfirstlane(threadIdx.x >> 6), lane = threadIdx.x & 63;
  STAMP(0);
  const rsrc_t rws = __builtin_amdgcn_make_buffer_rsrc(ws, 0, WS_END, 0x00020000);
  u64 tag;
  u32x2 epoch = {0u, 0u};
  ld8_hidden(epoch, srd(ws, WS_END), OFF_E);
  Watch wt{(LAS unsigned*)(lds + L2_FLAG), ws, 0ull, Mt * 2 + (slot2 >> 3), slot2 & 7, w, lane, 0u, 0, 0, 0, 0u};
  u32x4 br[3][5];
  PF2 pf{ws, rws, ldsb, slot2 & 7, w, lane, Mt % 3, br};
  if (cslot == 15) {
    if (threadIdx.x < 16) ((LAS unsigned*)(lds + L2_FLAG))[threadIdx.x] = 0u;
    retile(wd, rws, ldsb + w * 16384, Mt, w, lane);
    WAIT_V(0);
    PIN(epoch);
    wt.tag = tag = make_tag(epoch);
    WAIT_L0(); BAR();
    if (threadIdx.x == 0) { set_flag(ws, OFF_FR + Mt * 8, tag); set_flag(ws, OFF_F1 + (Mt * 16 + 15) * 8, tag); }
    STAMP(5);
    u32x4 wf[2][5];
    {
      const u32x4 ro = srd(wo, kD * 1280);
#pragma unroll 1
      for (int q = 0; q < 10; ++q)
        dma<0>(ro, (lane >> 3) * 1280 + (lane & 7) * 16, (Mt * 40 + (q >> 1) * 8) * 1280 + ((w * 160) & ~127) + (q & 1) * 128, ldsb + w * 16384 + q * 1024);
      WAIT_V(0);
#pragma unroll
      for (int hh = 0; hh < 2; ++hh) {
        unsigned ra[2];
#pragma unroll
        for (int h2 = 0; h2 < 2; ++h2) {
          int row = hh * 20 + (lane >> 4) * 8 + h2 * 4 + ((lane & 15) >> 2);
          row = row < 40 ? row : 39;
          ra[h2] = ldsb + w * 16384 + (row >> 3) * 2048 + (row & 7) * 128 + (lane & 3) * 8;
        }
#pragma unroll
        for (int i = 0; i < 5; ++i) {
          const int cb = (w & 3) * 32 + i * 32, o = (cb >> 7) * 1024 + (cb & 127);
          const u32x2 lo = __builtin_bit_cast(u32x2, __builtin_amdgcn_ds_read_tr16_b64_v4i16((LAS s16x4*)(size_t)(ra[0] + o)));
          const u32x2 hi = __builtin_bit_cast(u32x2, __builtin_amdgcn_ds_read_tr16_b64_v4i16((LAS s16x4*)(size_t)(ra[1] + o)));
          wf[hh][i] = u32x4{lo[0], lo[1], hi[0], hi[1]};
        }
      }
    }
    wait_flags(ws, OFF_FR + (lane & 15) * 8, tag);
    WAIT_L0(); BAR();
    wd_prefetch<0, 6>(srd(ws, WS_END), ldsb, slot2 & 7, w, lane);
    pf.items<0, 28>();
    if (Mt == 0 && w == 7) {
#pragma unroll 1
      for (int q = 0; q < 4; ++q) wait_flags(ws, OFF_F1 + (q * 64 + lane) * 8, tag);
      if (lane == 0) set_flag(ws, OFF_E, tag);
    }
    fold_kv(lds, ws, rws, wt, wf, Mt, w, lane);
  } else {
    const int type = cslot < 9 ? 0 : cslot < 13 ? 1 : 2;
    P1 c;
    c.w = w; c.mw = w >> 2; c.nw = w & 3; c.Mt = Mt; c.cslot = cslot; c.j = slot2 & 7;
    c.ldsb = ldsb;
    {
#define ROTM 1
      const int kb = (Mt + cslot) % 10, rot = ROTM == 0 ? (kb >= 5 ? 5 : 0) : ROTM == 1 ? (kb > 7 ? 7 : kb) : (kb / 3) * 3 - (kb == 9 ? 3 : 0);
      c.bstar = kb - rot;
      const int seg = w >> 2;
      c.pitch = type == 0 ? kF * 2 : kQKV * 2;
      const int startb = type == 0 ? cslot * 384
                       : ((type == 1 ? (640 * (1 + seg) + 160 * (cslot - 9)) * 2 : 320 * (2 * (cslot - 13) + seg)) & ~127);
      const char* wb = type == 0 ? (seg ? wu : wg) : wqkv;
      c.rw = srd(wb + startb - 2048, 640 * c.pitch - startb + 2048);
      c.rx = srd(x + (size_t)Mt * 128 * 1280, 128 * 1280);
      const int i = lane >> 3, cp = lane & 7, pg = w & 3;
      c.voffB = (unsigned)(8 * pg + i) * c.pitch + 16 * (cp ^ ((i & 2) | ((pg & 1) << 2))) + 2048;
      c.voffA = (unsigned)(8 * w + i) * 1280 + 16 * (cp ^ ((4 * w + (i >> 1)) & 7));
      c.oB = rot * 64 * c.pitch; c.oA = rot * 128;
      dma<0>(c.rx, c.voffA, c.oA, c.ldsb + L1_A + w * 1024);
    }
    auto pro = [&](int s) {
      const unsigned lB = c.ldsb + L1_B + w * 3072, lA = c.ldsb + L1_A + w * 1024;
      dma<0>(c.rw, c.voffB, c.oB, lB + s * L1_BST);
      dma<128>(c.rw, c.voffB, c.oB, lB + s * L1_BST + 1024);
      dma<256>(c.rw, c.voffB, c.oB, lB + s * L1_BST + 2048);
      c.oB += 32 * c.pitch;
      dma<0>(c.rx, c.voffA, c.oA + ((s + 1) >> 1) * 128 + ((s + 1) & 1) * 81920, lA + (s + 1) * 8192);
      SB();
    };
#define PSPLIT 1
#pragma unroll
    for (int s = 0; s < PSPLIT; ++s) pro(s);
    unsigned vb[5], va0;
    {
      const int g = lane >> 4, q = (lane & 15) >> 2, p = lane & 3, r = lane & 15, nw = c.nw;
      const int swz = (q & 2) | ((g & 1) << 2);
#pragma unroll
      for (int pos = 0; pos < 5; ++pos) {
        const int cf = 16 * nw + 4 * p;
        const int hb = (cslot & 1 ? 0 : 32) + 40 * nw;
        const int ck = pos < 4 ? (pos >> 1) * 192 + hb + 20 * (pos & 1) + 4 * p : (p >> 1) * 192 + hb + 20 * (p & 1) + 16;
        const int cq = (nw >> 1) * 224 + 20 * (4 * (nw & 1) + 2 * (p & 1) + (p >> 1)) + 4 * pos;
        const int col = type == 0 ? cf : type == 1 ? ck : cq;
        const int line = col >> 6, seg = line >= 3;
        vb[pos] = c.ldsb + L1_B + seg * (12288 - 3072) + (g * 3 + line) * 1024 + q * 128 + 16 * (((col & 63) >> 3) ^ swz) + 8 * ((col >> 2) & 1);
      }
      va0 = c.ldsb + L1_A + (c.mw * 64 + r) * 128 + 16 * (g ^ ((r >> 1) & 7));
    }
    if (threadIdx.x < 16) ((LAS unsigned*)(lds + L2_FLAG))[threadIdx.x] = 0u;
    f32x4 acc[4][6];
#pragma unroll
    for (int m = 0; m < 4; ++m)
#pragma unroll
      for (int i = 0; i < 6; ++i) acc[m][i] = f32x4{0.f, 0.f, 0.f, 0.f};
    unsigned foff = lane >= 20 ? OFF_FR + (lane & 15) * 8 : OFF_F1 + (lane < 10 ? lane * 16 + cslot : Mt * 16 + lane - 10) * 8;
    PIN(foff);
#pragma unroll
    for (int i = 0; i < 5; ++i) PIN(vb[i]);
    SB();
#pragma unroll
    for (int s = PSPLIT; s < 3; ++s) pro(s);
    c.oA += 256;
    if (w == 7 && idx < 8) warm_code(pc0, ldsb + L1_GR + 8192, idx, lane);
    SB();
    WAIT_V(8);
    PIN(epoch);
    wt.tag = tag = make_tag(epoch);
#define P1_STAMP
#define P1_RUN(T) do { if (w < 4) p1_loop<T, true>(c, vb, va0, foff, acc, pf, ws, rws, tag, lane); \
                       else p1_loop<T, false>(c, vb, va0, foff, acc, pf, ws, rws, tag, lane); P1_STAMP; } while (0)
    if (type == 0) { P1_RUN(0); epi_ffn(lds, acc, ws, rws, wt, c, pf, lane); }
    else if (type == 2) { P1_RUN(2); epi_q(lds, acc, ws, rws, wt, c, pf, lane); }
    else {
      P1_RUN(1);
      epi_kv(lds, acc, ws, rws, tag, c, lane);
      STAMP(5);
      wd_prefetch<0, 6>(srd(ws, WS_END), ldsb, c.j, w, lane);
      pf.items<0, 28>();
    }
  }
  phase2(lds, x, ws, rws, out, wt, br, Mt * 2 + (slot2 >> 3), slot2 & 7, w, lane);
  STAMP(10);
}

extern "C" void kernel_launch(void* const* d_in, const int* in_sizes, int n_in, void* d_out, int out_size,
                              void* d_ws, size_t ws_size, hipStream_t stream) {
  (void)in_sizes; (void)n_in; (void)out_size; (void)ws_size;
  (void)hipFuncSetAttribute((const void*)fused, hipFuncAttributeMaxDynamicSharedMemorySize, LDS_SIZE);
  fused<<<256, 512, LDS_SIZE, stream>>>((const char*)d_in[0], (const char*)d_in[1], (const char*)d_in[2], (const char*)d_in[4],
                                        (const char*)d_in[5], (const char*)d_in[6], (char*)d_ws, (char*)d_out);
}
```

```cpp
#include <hip/hip_runtime.h>
#include <stdint.h>
#include <type_traits>

#define GAS __attribute__((address_space(1)))
#define LAS __attribute__((address_space(3)))
typedef __attribute__((ext_vector_type(2))) __bf16 bf16x2;
typedef __attribute__((ext_vector_type(8))) __bf16 bf16x8;
typedef __attribute__((ext_vector_type(4))) short s16x4;
typedef __attribute__((ext_vector_type(4))) float f32x4;
typedef __attribute__((ext_vector_type(2))) float f32x2;
typedef __attribute__((ext_vector_type(4))) unsigned u32x4;
typedef __attribute__((ext_vector_type(2))) unsigned u32x2;
typedef unsigned long long u64;
typedef __amdgpu_buffer_rsrc_t rsrc_t;

#define DEVI __device__ __forceinline__
#define WAIT_V_(n) asm volatile("s_waitcnt vmcnt(" #n ")" ::: "memory")
#define WAIT_V(n) WAIT_V_(n)
template <int N> __device__ __forceinline__ void wait_v() { asm volatile("s_waitcnt vmcnt(%0)" :: "i"(N) : "memory"); }
#define WAIT_L0() do { __builtin_amdgcn_s_waitcnt(0xC07F); asm volatile("" ::: "memory"); } while (0)
#define SB() __builtin_amdgcn_sched_barrier(0)
#define PIN(x) asm volatile("" : "+v"(x))
#define PINS(x) asm volatile("" : "+s"(x))
#define BAR() do { asm volatile("" ::: "memory"); __builtin_amdgcn_s_barrier(); asm volatile("" ::: "memory"); } while (0)
#define RLX_AGENT __ATOMIC_RELAXED, __HIP_MEMORY_SCOPE_AGENT
#define RLX_WG __ATOMIC_RELAXED, __HIP_MEMORY_SCOPE_WORKGROUP
#define SC1 16


constexpr int kD = 640, kF = 1728, kQKV = 1920;
constexpr unsigned OFF_E   = 0;
constexpr unsigned OFF_F1  = 256;
constexpr unsigned OFF_FR  = OFF_F1 + 2048;
constexpr unsigned OFF_FK  = 2560;
constexpr unsigned OFF_F3  = OFF_FK + 4096;
constexpr unsigned OFF_FH  = OFF_F3 + 1024;
constexpr unsigned OFF_FZ  = 16384;
constexpr unsigned OFF_ZP  = OFF_FZ + 4096;
constexpr unsigned OFF_SC  = OFF_ZP + 4 * 32 * 640;
constexpr unsigned OFF_SR  = OFF_SC + 15 * 10 * 384 * 4;
constexpr unsigned OFF_KVP = OFF_SR + 16 * 10 * 256 * 4;
constexpr unsigned OFF_MP  = OFF_KVP + 16 * 16 * 3456;
constexpr unsigned OFF_WD  = OFF_MP + 8 * 20 * 5 * 1024;
constexpr unsigned OFF_Q   = OFF_WD + 8 * 54 * 5 * 1024;
constexpr unsigned OFF_H   = OFF_Q + 32 * 20 * 4096;
constexpr unsigned WS_END  = OFF_H + 32 * 54 * 4096;
constexpr unsigned OOB = 0x80000000u;
static_assert(OFF_FH + 8192 <= OFF_FZ && OFF_MP % 1024 == 0 && OFF_KVP % 128 == 0, "align");

constexpr int L1_B = 0, L1_BST = 24576;
constexpr int L1_A = 4 * L1_BST, L1_AST = 16384;
constexpr int L1_GR = L1_A + 3 * L1_AST;
constexpr int L1_GC = L1_A + L1_AST;
constexpr int L1_ST = 158720;
constexpr int L1_CS = L1_ST + 1024;
constexpr int L1_EX = 72 * 1024;
constexpr int L2_BASE = 120 * 1024;
constexpr int L2_RED = 150 * 1024;
constexpr int L2_FLAG = 162816;
constexpr int LDS_SIZE = L2_FLAG + 64;
enum { LF_STATUS = 0, LF_DMA = 1, LF_K = 2, LF_X = 3, LF_PUB = 8 };
enum { S_H = 1, S_Q = 2, S_M = 4 };

DEVI f32x4 mfma(u32x4 a, u32x4 b, f32x4 c) {
  return __builtin_amdgcn_mfma_f32_16x16x32_bf16(__builtin_bit_cast(bf16x8, a), __builtin_bit_cast(bf16x8, b), c, 0, 0, 0);
}
DEVI void mfma_acc(f32x4& c, u32x4 a, u32x4 b) { asm volatile("v_mfma_f32_16x16x32_bf16 %0, %1, %2, %0" : "+v"(c) : "v"(a), "v"(b)); }
DEVI void mfma_new(f32x4& c, u32x4 a, u32x4 b) { asm volatile("v_mfma_f32_16x16x32_bf16 %0, %1, %2, 0" : "=&v"(c) : "v"(a), "v"(b)); }
DEVI unsigned pk2(f32x2 f) { return __builtin_bit_cast(unsigned, __builtin_convertvector(f, bf16x2)); }
DEVI unsigned pk2(float lo, float hi) { return pk2(f32x2{lo, hi}); }
DEVI f32x2 unpk(unsigned u) { return f32x2{__builtin_bit_cast(float, u << 16), __builtin_bit_cast(float, u & 0xffff0000u)}; }
DEVI f32x2 fma2(f32x2 a, f32x2 b, f32x2 c) { return __builtin_elementwise_fma(a, b, c); }
DEVI f32x2 bc2(float v) { return f32x2{v, v}; }
DEVI f32x2 lo2(f32x4 v) { return f32x2{v[0], v[1]}; }
DEVI f32x2 hi2(f32x4 v) { return f32x2{v[2], v[3]}; }
DEVI f32x2 phi2(f32x2 g, f32x2 rs) {
  const f32x2 t = fma2(g, rs, bc2(1e-6f)), l = g * (rs * 1.4426950408889634f);
  return f32x2{fmaxf(t[0], 1e-6f), fmaxf(t[1], 1e-6f)} +
         f32x2{__builtin_amdgcn_fmed3f(__builtin_amdgcn_exp2f(l[0]), 0.f, 1.f), __builtin_amdgcn_fmed3f(__builtin_amdgcn_exp2f(l[1]), 0.f, 1.f)};
}
DEVI u32x4 lds_b128(unsigned a) { return *(LAS u32x4*)(size_t)a; }
DEVI u32x4 lds_trfrag(unsigned a) {
  const u32x2 lo = __builtin_bit_cast(u32x2, __builtin_amdgcn_ds_read_tr16_b64_v4i16((LAS s16x4*)(size_t)a));
  const u32x2 hi = __builtin_bit_cast(u32x2, __builtin_amdgcn_ds_read_tr16_b64_v4i16((LAS s16x4*)(size_t)(a + 512)));
  return u32x4{lo[0], lo[1], hi[0], hi[1]};
}
DEVI u32x4 srd(const void* p, unsigned bytes) {
  const u64 a = (u64)p;
  return u32x4{(unsigned)a, (unsigned)(a >> 32) & 0xffffu, bytes, 0x00020000u};
}
DEVI void setm0(unsigned v) { asm volatile("s_mov_b32 m0, %0" :: "s"(v) : "memory"); }
template <int IMM>
DEVI void dma_n(u32x4 r, unsigned voff, unsigned soff) {
  asm volatile("buffer_load_dwordx4 %0, %1, %2 offen offset:%3 lds" :: "v"(voff), "s"(r), "s"(soff), "i"(IMM) : "memory");
}
template <int IMM>
DEVI void dma(u32x4 r, unsigned voff, unsigned soff, unsigned lds) {
  asm volatile("s_mov_b32 m0, %4\n\ts_nop 0\n\tbuffer_load_dwordx4 %0, %1, %2 offen offset:%3 lds"
               :: "v"(voff), "s"(r), "s"(soff), "i"(IMM), "s"(lds - IMM) : "memory");
}
DEVI void dma_sc1(u32x4 r, unsigned voff, unsigned soff, unsigned lds) {
  asm volatile("s_mov_b32 m0, %3\n\ts_nop 0\n\tbuffer_load_dwordx4 %0, %1, %2 offen sc1 lds"
               :: "v"(voff), "s"(r), "s"(soff), "s"(lds) : "memory");
}
DEVI void ld8_hidden(u32x2& v, u32x4 r, unsigned voff) {
  asm volatile("buffer_load_dwordx2 %0, %1, %2, 0 offen sc1" : "+v"(v) : "v"(voff), "s"(r) : "memory");
}
template <int N0, int N1>
DEVI void wd_prefetch(u32x4 rws, unsigned ldsb, int j, int w, int lane) {
#pragma unroll
  for (int n = N0; n < N1; ++n) {
    const unsigned a = (unsigned)(w + 8 * n) * 1024;
    if (n < 18 || w < 6) dma_sc1(rws, lane * 16, OFF_WD + j * 276480 + a, ldsb + a);
  }
}
DEVI void wait_flags(char* ws, unsigned off, u64 tag) {
#pragma unroll 1
  for (unsigned spins = 0; spins < (1u << 20); ++spins) {
    const u64 v = __hip_atomic_load((GAS u64*)(ws + off), RLX_AGENT);
    if (__all(v == tag)) break;
    __builtin_amdgcn_s_sleep(1);
  }
  asm volatile("" ::: "memory");
}
template <int N>
DEVI u64 make_tag(u32x2 e) {
  unsigned lo, hi;
  asm volatile("s_waitcnt vmcnt(%4)\n\tv_readfirstlane_b32 %0, %2\n\tv_readfirstlane_b32 %1, %3\n\ts_nop 1"
               : "=s"(lo), "=s"(hi) : "v"(e[0]), "v"(e[1]), "i"(N) : "memory");
  return ((u64)lo | (u64)hi << 32) * 0x9E3779B97F4A7C15ull + 0xD1B54A32D192ED03ull;
}
DEVI void set_flag(char* ws, unsigned off, u64 tag) { __hip_atomic_store((GAS u64*)(ws + off), tag, RLX_AGENT); }
DEVI unsigned lds_inc(LAS unsigned* p, int lane) {
  unsigned o = 0;
  if (lane == 0) o = __hip_atomic_fetch_add(p, 1u, RLX_WG);
  return __builtin_amdgcn_readfirstlane(o);
}
DEVI void lds_wait_bits(LAS unsigned* p, unsigned bits) {
  while ((~(unsigned)__builtin_amdgcn_readfirstlane(__hip_atomic_load(p, RLX_WG)) & bits) != 0u) __builtin_amdgcn_s_sleep(1);
  asm volatile("" ::: "memory");
}
DEVI void lds_wait(LAS unsigned* p, unsigned want) {
  while ((unsigned)__builtin_amdgcn_readfirstlane(__hip_atomic_load(p, RLX_WG)) < want) __builtin_amdgcn_s_sleep(1);
  asm volatile("" ::: "memory");
}
DEVI u32x4 ld16(rsrc_t r, unsigned voff, unsigned soff) { return __builtin_amdgcn_raw_buffer_load_b128(r, voff, soff, SC1); }
DEVI void st16(rsrc_t r, unsigned voff, unsigned soff, u32x4 v) { __builtin_amdgcn_raw_buffer_store_b128(v, r, voff + soff, 0, SC1); }
DEVI void st8(rsrc_t r, unsigned voff, unsigned soff, u32x2 v) { __builtin_amdgcn_raw_buffer_store_b64(v, r, voff, soff, SC1); }

constexpr unsigned CODE_BYTES = 67 * 1024;
DEVI void warm_code(unsigned bytes, const char* pc0, unsigned junk, int chunk, int lane) {
  asm volatile("s_mov_b32 m0, %3\n\ts_nop 0\n\tbuffer_load_dword %0, %1, %2 offen lds"
               :: "v"(lane << 7), "s"(srd(pc0, bytes)), "s"(chunk * 8192), "s"(junk) : "memory");
}

struct P1 {
  u32x4 rw, rx;
  unsigned ldsb, pitch;
  unsigned voffB, voffA;
  unsigned oB, oA;
  int w, mw, nw, bstar;
  int Mt, cslot, j;
};

struct PF2 {
  char* ws; rsrc_t rws; unsigned ldsb; int j, w, lane, r0;
  u32x4 (&br)[3][5];
  template <int N> DEVI void item() {
    if constexpr (N < 13) wd_prefetch<6 + N, 7 + N>(srd(ws, WS_END), ldsb, j, w, lane);
    else br[(N - 13) / 5][(N - 13) % 5] = ld16(rws, lane * 16, OFF_WD + (j * 54 + 30 + w + 8 * (((N - 13) / 5 + r0) % 3)) * 5120 + ((N - 13) % 5) * 1024);
    SB();
  }
  template <int N0, int N1> DEVI void items() { if constexpr (N0 < N1) { item<N0>(); items<N0 + 1, N1>(); } }
};

constexpr int EB = 3;
template <int TYPE, bool LATE>
DEVI void p1_loop(P1& c, unsigned (&vb)[5], unsigned va0, unsigned foff, f32x4 (&acc)[4][6], PF2& pf, char* ws, rsrc_t rws, u64 tag, int lane) {
  constexpr int NT = TYPE == 0 ? 6 : 5;
  constexpr bool AC = true;
  u32x4 fa[4], fb[NT];
  unsigned va1 = va0 ^ 64;
  const unsigned stepB = 32 * c.pitch, limB = 640 * c.pitch;
  const unsigned lB0 = c.ldsb + L1_B + c.w * 3072, lA0 = c.ldsb + L1_A + c.w * 1024;
  unsigned lB = lB0 + 3 * L1_BST, lA = lA0 + 2 * L1_AST;
  unsigned vB1 = c.voffB - 896, vB2 = c.voffB - 1792, vA1 = c.voffA + 81920;
  PIN(vB1); PIN(vB2); PIN(vA1);
  int dA = L1_AST, dB = 2 * L1_BST, rare = c.bstar, rare2 = 1;
  f32x4 st[5];
  auto adB = [&](int i) { return TYPE == 0 ? vb[!LATE && i >= 2 ? 1 : 0] + (i / 3) * 12288 + (i % 3) * 1024 : vb[i < 5 ? i : 4]; };
  if (TYPE == 0) { vb[1] = vb[0]; PIN(vb[1]); }
  auto upB = [&](int i0, int i1) {
    if (TYPE == 0) { vb[i0 ? 1 : 0] += dB; PIN(vb[i0 ? 1 : 0]); }
    else {
#pragma unroll
      for (int i = i0; i < i1; ++i) { vb[i] += dB; PIN(vb[i]); }
    }
  };
  auto rdA = [&](int i, int sub) { fa[i] = lds_b128((sub ? va1 : va0) + i * 2048); };
  auto rdB = [&](int i, int sub) { fb[i] = lds_trfrag(adB(i) + sub * L1_BST); };
  auto rdBh = [&](int i, int hh, int sub) {
    const u32x2 v = __builtin_bit_cast(u32x2, __builtin_amdgcn_ds_read_tr16_b64_v4i16((LAS s16x4*)(size_t)(adB(i) + sub * L1_BST + hh * 512)));
    fb[i][2 * hh] = v[0]; fb[i][2 * hh + 1] = v[1];
  };
  auto pieceA = [&](int sub) { dma_n<0>(c.rx, sub ? vA1 : c.voffA, c.oA); };
  auto L = [&](int sub, int mode) {
    if (mode) dma_n<0>(c.rw, c.voffB, c.oB);
    SB(); rdA(0, sub); rdA(1, sub); SB();
    if (mode) dma_n<1024>(c.rw, vB1, c.oB);
    SB(); rdA(2, sub); rdA(3, sub); SB();
    if (mode) dma_n<2048>(c.rw, vB2, c.oB);
    SB(); rdB(LATE ? 4 : 0, sub); SB();
    if (mode == 2) setm0(lA + sub * 8192);
    SB(); if (!LATE) rdB(1, sub); else if (NT == 6) rdB(NT - 1, sub); else if (mode == 2) asm volatile("s_nop 0"); SB();
    if (mode == 2) pieceA(sub);
    if (!LATE && !sub) upB(2, 5);
    SB();
  };
  auto U = [&](int sub) {
    if (sub) { va1 += dA; PIN(va1); upB(0, LATE ? 5 : 2); }
    else { va0 += dA; PIN(va0); }
    SB();
  };
  auto stats = [&](int sub) {
    const u32x4 one = {0x3f803f80u, 0x3f803f80u, 0x3f803f80u, 0x3f803f80u};
    const u32x4 sa = lds_b128((sub ? va1 : va0) + c.nw * 2048);
    u32x4 sb[3];
#pragma unroll
    for (int i = 0; i < 3; ++i) { unsigned a = adB(LATE ? i : 3 + i); PIN(a); sb[i] = lds_trfrag(a + sub * L1_BST); }
    SB();
    if (sub == 0) {
#pragma unroll
      for (int i = 0; i < 5; ++i) st[i] = f32x4{0.f, 0.f, 0.f, 0.f};
    }
    st[0] = mfma(one, sa, st[0]);
    st[1] = mfma(sa, sa, st[1]);
#pragma unroll
    for (int i = 0; i < 3; ++i) st[2 + i] = mfma(one, sb[i], st[2 + i]);
    if (sub == 1) {
      const int cl = lane & 15, g = lane >> 4;
      const float sq = (cl & 2) ? ((cl & 1) ? st[1][3] : st[1][2]) : ((cl & 1) ? st[1][1] : st[1][0]);
      st8(rws, (cl >> 2) == g ? (c.mw * 64 + c.nw * 16 + cl) * 8 : OOB, c.cslot < 10 ? OFF_SR + (c.Mt * 10 + c.cslot) * 1024 : WS_END,
          u32x2{__builtin_bit_cast(unsigned, st[0][0]), __builtin_bit_cast(unsigned, sq)});
      const float v = g == 0 ? st[2][0] : g == 1 ? st[3][0] : st[4][0];
      __builtin_amdgcn_raw_buffer_store_b32(__builtin_bit_cast(unsigned, v), rws, g < 3 ? ((c.nw * 6 + c.mw * 3) * 16 + lane) * 4 : OOB,
                                            c.Mt < 10 ? OFF_SC + (c.cslot * 10 + c.Mt) * 1536 : WS_END, SC1);
    }
    SB();
  };
  auto C = [&](int sub, bool more, int book, auto vmwait) {
    __builtin_amdgcn_s_setprio(1);
    if (LATE) { vmwait(); WAIT_L0(); BAR(); }
    SB();
#pragma unroll
    for (int k = 0; k < 4 * NT; ++k) {
      const int i = k >> 2, m = k & 3, ms = (i & 1) ? 3 - m : m;
      acc[ms][i] = TYPE == 1 ? mfma(fa[ms], fb[i], acc[ms][i]) : mfma(fb[i], fa[ms], acc[ms][i]);
      if (LATE) { if (more && i >= 1 && i < 5 && m < 2) rdBh(i - 1, m, sub ^ 1); }
      else if (k < 2 * (NT - 2)) rdBh(2 + (k >> 1), k & 1, sub);
      const int q = LATE ? (k < 2 ? k : m >= 2 && i < 5 ? 2 * i + m : -1) : k - 2 * (NT - 2);
      constexpr int QA = LATE ? 8 : 0, QU = LATE ? 0 : 3;
      if (book == 1) {
        if (AC) {
          if (q == QA) setm0(lA + (LATE ? sub ^ 1 : sub) * 8192);
          if (q == QA + 1) pieceA(LATE ? sub ^ 1 : sub);
        }
        if (q == QU) { c.oB += stepB; lB += L1_BST; PINS(c.oB); PINS(lB); }
        if (q == QU + 1) { c.oB = c.oB == limB ? 0u : c.oB; PINS(c.oB); }
        if (q == QU + 2) { lB = lB == lB0 + 4 * L1_BST ? lB0 : lB; PINS(lB); }
        if (sub) {
          if (q == QU + 3) { c.oA += 128; lA += L1_AST; PINS(c.oA); PINS(lA); }
          if (q == QU + 4) { c.oA = c.oA == 1280u ? 0u : c.oA; PINS(c.oA); }
          if (q == QU + 5) { lA = lA == lA0 + 3 * L1_AST ? lA0 : lA; PINS(lA); }
          if (q == QU + 6) { dA = lA == lA0 + L1_AST ? -2 * L1_AST : L1_AST; PINS(dA); }
          if (q == QU + 7) { rare -= 1; PINS(rare); }
        }
        if (!sub && q == QU + 3) { rare2 = LATE ? (rare + 2) & ~2 : rare; PINS(rare2); }
        if (q == 11) setm0(lB);
      }
      if (book && q == 10 && (LATE ? sub : !sub)) { dB = -dB; PINS(dB); }
      if (!LATE && EB > 0) {
        if (k == 4 * NT - 2 - EB) { vmwait(); WAIT_L0(); }
        if (k == 4 * NT - 1 - EB) { __builtin_amdgcn_s_setprio(2); BAR(); }
      }
      SB();
    }
    __builtin_amdgcn_s_setprio(0);
    if (!LATE && EB == 0) { vmwait(); WAIT_L0(); BAR(); }
  };
  constexpr int LM = AC ? 1 : 2;
  if (!LATE) { upB(2, 5); dB = -dB; }
  BAR();
  if (LATE) {
    if (AC) { setm0(lA); SB(); }
    rdB(0, 0); rdB(1, 0); SB();
    if (AC) pieceA(0);
  }
  setm0(lB);
  SB();
  if (LATE) { rdB(2, 0); rdB(3, 0); } else asm volatile("s_nop 0");
  SB();
  constexpr int X = TYPE == 0;
  const int rend = c.bstar - (X ? 7 : 8);
  const u32x4 rw4 = srd(ws, WS_END);
  u32x2 fl = {0u, 0u};
#pragma unroll 1
  do {
    if (X && __builtin_expect(rare == rend + 1, 0)) ld8_hidden(fl, rw4, foff);
    L(0, LM);
    if (__builtin_expect(rare == 0, 0)) stats(0);
    U(0);
    C(0, true, 1, []() { WAIT_V(8); });
    L(1, LM);
    if (__builtin_expect(rare2 == 0, 0)) {
      PINS(rare);
      if (rare == 0) stats(1);
      else if (LATE && c.w == 0 && lane == 0) set_flag(ws, OFF_F1 + (c.Mt * 16 + c.cslot) * 8, tag);
    }
    U(1);
    C(1, true, 1, []() { WAIT_V(8); });
  } while (rare != rend);
  auto check = [&]() {
    PIN(fl);
    if (__builtin_expect(!__all(__builtin_bit_cast(u64, fl) == tag), 0)) wait_flags(ws, foff, tag);
  };
  if (X) {
    L(0, LM);
    WAIT_V(11);
    check();
    pf.items<13, 16>();
    U(0);
    C(0, true, 1, []() { WAIT_V(11); });
    L(1, LM);
    pf.items<16, 19>();
    U(1);
    C(1, true, 1, []() { WAIT_V(14); });
  }
  if (!X) ld8_hidden(fl, rw4, foff);
  L(0, 1);
  if (X) pf.items<19, 22>();
  U(0);
  C(0, true, 2, []() { wait_v<X ? 13 : 8>(); });
  L(1, 0);
  wait_v<X ? 6 : 3>();
  if (!X) check();
  dma_sc1(rw4, lane * 16, OFF_SC + c.cslot * 15360 + c.w * 1024, c.ldsb + L1_GC + c.w * 1024);
  dma_sc1(rw4, lane * 16, OFF_SC + c.cslot * 15360 + c.w * 1024 + 8192, c.ldsb + L1_GC + c.w * 1024 + 8192);
  dma_sc1(rw4, lane * 16, OFF_SR + c.Mt * 10240 + c.w * 1024, c.ldsb + L1_GR + c.w * 1024);
  if (LATE && c.w < 2) dma_sc1(rw4, lane * 16, OFF_SR + c.Mt * 10240 + c.w * 1024 + 8192, c.ldsb + L1_GR + c.w * 1024 + 8192);
  if (X) pf.items<22, 24>();
  U(1);
  C(1, true, 2, []() {});
  L(0, 0);
  if (TYPE != 1) wd_prefetch<0, 4>(rw4, c.ldsb, c.j, c.w, lane);
  if (X) pf.items<24, 26>();
  C(0, true, 0, []() { wait_v<X ? 8 : TYPE != 1 ? 4 : 0>(); });
  L(1, 0);
  {
    __builtin_amdgcn_s_setprio(2);
    if (LATE || c.w < 6) {
      const LAS float* src = (const LAS float*)(size_t)(c.ldsb + L1_GC + (c.w * 64 + lane) * 4);
      float v[10];
#pragma unroll
      for (int p = 0; p < 10; ++p) v[p] = src[p * 384];
      *(LAS float*)(size_t)(c.ldsb + L1_CS + (c.w * 64 + lane) * 4) = (((v[0] + v[1]) + (v[2] + v[3])) + ((v[4] + v[5]) + (v[6] + v[7]))) + (v[8] + v[9]);
    } else {
      const LAS f32x2* src = (const LAS f32x2*)(size_t)(c.ldsb + L1_GR + ((c.w - 6) * 64 + lane) * 8);
      f32x2 v[10];
#pragma unroll
      for (int p = 0; p < 10; ++p) v[p] = src[p * 128];
      const f32x2 s = ((((v[0] + v[1]) + (v[2] + v[3])) + ((v[4] + v[5]) + (v[6] + v[7]))) + (v[8] + v[9])) * (1.0f / kD);
      LAS float* d = (LAS float*)(size_t)(c.ldsb + L1_ST + ((c.w - 6) * 64 + lane) * 4);
      d[0] = s[0]; d[128] = __builtin_amdgcn_rsqf(fmaxf(__builtin_fmaf(-s[0], s[0], s[1]), 0.f) + 1e-5f);
    }
    __builtin_amdgcn_s_setprio(0);
  }
  SB();
  if (TYPE != 1) wd_prefetch<4, 6>(rw4, c.ldsb, c.j, c.w, lane);
  if (X) pf.items<26, 28>();
  SB();
  C(1, false, 0, []() {});
}

template <int N, class F> DEVI void static_for(F&& f) {
  if constexpr (N > 0) { static_for<N - 1>(f); f(std::integral_constant<int, N - 1>{}); }
}
DEVI u32x4 pair16(u32x2 a, u32x2 b) {
  const auto s0 = __builtin_amdgcn_permlane32_swap(a[0], b[0], false, false);
  const auto s1 = __builtin_amdgcn_permlane32_swap(a[1], b[1], false, false);
  return u32x4{s0[0], s1[0], s0[1], s1[1]};
}
struct Watch {
  LAS unsigned* lf; char* ws; u64 tag; int mt2, j, w, lane;
  unsigned status;
  DEVI u64 fetch() {
    int l2 = lane;
    PIN(l2);
    const unsigned f0 = l2 < 32 ? OFF_FH + mt2 * 256 + (j >> 2) * 128 + (l2 < 11 ? l2 : 0) * 8 : OFF_F3 + (blockIdx.x & 7) * 128 + (l2 & 15) * 8;
    return __hip_atomic_load((GAS u64*)(ws + f0), RLX_AGENT);
  }
  DEVI bool eval(u64 v, bool giveup) {
    const u64 no = __ballot(v != tag);
    unsigned s = ((no & 0x1ffull) ? 0u : S_H) | ((no & 0x600ull) ? 0u : S_Q) | ((no >> 32) ? 0u : S_M);
    if (giveup) s = 7u;
    if (s == status) return false;
    status = s;
    __hip_atomic_store(lf + LF_STATUS, s, RLX_WG);
    return true;
  }
  DEVI bool poll(bool giveup) { return eval(fetch(), giveup); }
  int pcnt, plast, preps; unsigned pflag;
  DEVI void publish() {
    if ((int)lds_inc(lf + LF_PUB + pcnt, lane) == plast && lane < preps) set_flag(ws, pflag + lane * 128, tag);
    preps = 0;
  }
  DEVI void need(unsigned bits, int watcher = 7) {
    if (w != watcher) { lds_wait_bits(lf + LF_STATUS, bits); return; }
#pragma unroll 1
    for (unsigned spins = 0; (~status & bits) != 0u; ++spins)
      if (!poll(spins >= (1u << 20))) __builtin_amdgcn_s_sleep(1);
    asm volatile("" ::: "memory");
  }
};
DEVI void epi_ffn(LAS char* lds, f32x4 (&acc)[4][6], char* ws, rsrc_t rws, Watch& wt, const P1& c, PF2& pf, int lane) {
  const int r = lane & 15, g = lane >> 4;
  pf.items<12, 13>();
  pf.items<0, 2>();
  float nm[4], k1[4], k2[4], iv[4];
#pragma unroll
  for (int mi = 0; mi < 4; ++mi) {
    const LAS float* sp = (const LAS float*)(lds + L1_ST) + c.mw * 64 + mi * 16 + r;
    const float rs = sp[128], c0 = -2.0f * 0.7978845608028654f * 1.4426950408889634f;
    nm[mi] = -sp[0]; k1[mi] = c0 * rs; k2[mi] = c0 * 0.044715f * rs * rs * rs; iv[mi] = __builtin_amdgcn_rcpf(rs * rs);
  }
  const unsigned hb = OFF_H + ((c.Mt * 2 + c.mw) * 54 + 2 * c.cslot + (c.nw >> 1)) * 4096 + (c.nw & 1) * 512 + (lane & 31) * 16 + (lane >> 5) * 1024;
  u32x4 v[6];
  static_for<3>([&](auto I) {
    constexpr int i = I;
    const f32x4 cg = *(LAS f32x4*)(lds + L1_CS + ((c.nw * 6 + i) * 16 + 4 * g) * 4);
    const f32x4 cu = *(LAS f32x4*)(lds + L1_CS + ((c.nw * 6 + 3 + i) * 16 + 4 * g) * 4);
    u32x2 o[4];
    static_for<4>([&](auto MI) {
      constexpr int mi = MI, n = 4 * i + mi;
      if constexpr (2 + 2 * n < 12) pf.template items<2 + 2 * n, 4 + 2 * n>();
#pragma unroll
      for (int hh = 0; hh < 2; ++hh) {
        const f32x2 G = fma2(bc2(nm[mi]), hh ? hi2(cg) : lo2(cg), hh ? hi2(acc[mi][i]) : lo2(acc[mi][i]));
        const f32x2 U = fma2(bc2(nm[mi]), hh ? hi2(cu) : lo2(cu), hh ? hi2(acc[mi][3 + i]) : lo2(acc[mi][3 + i]));
        const f32x2 z = G * fma2(G * G, bc2(k2[mi]), bc2(k1[mi]));
        const f32x2 d = fma2(f32x2{__builtin_amdgcn_exp2f(z[0]), __builtin_amdgcn_exp2f(z[1])}, bc2(iv[mi]), bc2(iv[mi]));
        o[mi][hh] = pk2(G * U * f32x2{__builtin_amdgcn_rcpf(d[0]), __builtin_amdgcn_rcpf(d[1])});
      }
      PIN(o[mi]);
      SB();
      if constexpr (n == 4 || n == 8) {
        constexpr int p = (n - 4) / 4;
        st16(rws, hb, p * 18 * 4096, v[2 * p]);
        st16(rws, hb, p * 18 * 4096 + 2048, v[2 * p + 1]);
        SB();
      }
    });
    v[2 * i] = pair16(o[0], o[1]); v[2 * i + 1] = pair16(o[2], o[3]);
    PIN(v[2 * i]); PIN(v[2 * i + 1]);
  });
  st16(rws, hb, 36 * 4096, v[4]);
  st16(rws, hb, 36 * 4096 + 2048, v[5]);
  BAR();
  WAIT_V(0);
  wt.pcnt = c.mw; wt.plast = 3; wt.preps = 2; wt.pflag = OFF_FH + (c.Mt * 2 + c.mw) * 256 + c.cslot * 8;
  wt.publish();
}

DEVI f32x4 sweep8(rsrc_t rws, unsigned voff, unsigned soff, unsigned stride, unsigned tag) {
  u32x4 gr[8];
#pragma unroll 1
  for (unsigned spins = 0; spins < (1u << 20); ++spins) {
#pragma unroll
    for (int p = 0; p < 8; ++p) gr[p] = ld16(rws, voff, soff + p * stride);
    SB();
    unsigned bad = 0;
#pragma unroll
    for (int p = 0; p < 8; ++p) bad |= (gr[p][1] ^ tag) | (gr[p][3] ^ tag);
    PIN(bad);
    if (__all(bad == 0u || voff == OOB)) break;
    asm volatile("" ::: "memory");
  }
  f32x2 a[8], b[8];
#pragma unroll
  for (int p = 0; p < 8; ++p) { a[p] = unpk(gr[p][0]); b[p] = unpk(gr[p][2]); }
  const f32x2 sa = ((a[0] + a[1]) + (a[2] + a[3])) + ((a[4] + a[5]) + (a[6] + a[7]));
  const f32x2 sb = ((b[0] + b[1]) + (b[2] + b[3])) + ((b[4] + b[5]) + (b[6] + b[7]));
  return f32x4{sa[0], sa[1], sb[0], sb[1]};
}

DEVI void epi_q(LAS char* lds, f32x4 (&acc)[4][6], char* ws, rsrc_t rws, Watch& wt, const P1& c, PF2& pf, int lane) {
  const int r = lane & 15, g = lane >> 4, qi = c.cslot - 13;
  f32x4 cs[5];
#pragma unroll
  for (int i = 0; i < 5; ++i) cs[i] = *(LAS f32x4*)(lds + L1_CS + ((c.nw * 6 + i) * 16 + 4 * g) * 4);
  pf.items<0, 8>();
  static_for<4>([&](auto MI) {
    constexpr int mi = MI;
    const LAS float* sp = (const LAS float*)(lds + L1_ST) + c.mw * 64 + mi * 16 + r;
    const f32x2 nm = bc2(-sp[0]), rs = bc2(sp[128]);
    static_for<5>([&](auto I) {
      constexpr int i = I;
      const f32x2 a = phi2(fma2(nm, lo2(cs[i]), lo2(acc[mi][i])), rs), b = phi2(fma2(nm, hi2(cs[i]), hi2(acc[mi][i])), rs);
      acc[mi][i] = f32x4{a[0], a[1], b[0], b[1]};
      PIN(acc[mi][i]);
      SB();
      pf.template item<8 + mi * 5 + i>();
    });
  });
  LAS unsigned* lf = (LAS unsigned*)(lds + L2_FLAG);
  if (c.w == 4) { wait_flags(ws, OFF_FZ + (blockIdx.x & 7) * 512 + qi * 256 + (lane & 31) * 8, wt.tag); lds_inc(lf + LF_K, lane); }
  else lds_wait(lf + LF_K, 1u);
  {
    const int q4 = (c.w & 1) * 64 + lane;
    const f32x4 zs = sweep8(rws, q4 < 40 ? q4 * 16 : q4 < 80 ? q4 * 16 + 31 * 640 : OOB, OFF_ZP + (qi * 64 + (c.w >> 1) * 8) * 640, 640, (unsigned)wt.tag);
    if (q4 < 80) *(LAS f32x4*)(lds + L2_RED + (c.w >> 1) * 1280 + q4 * 16) = zs;
    WAIT_L0(); BAR();
  }
  u64 fv = 0;
  f32x4 z[5];
#pragma unroll
  for (int i = 0; i < 5; ++i) {
    const LAS char* p = lds + L2_RED + (c.nw * 80 + 20 * (2 * (g & 1) + (g >> 1)) + 4 * i) * 4;
    z[i] = (*(const LAS f32x4*)p + *(const LAS f32x4*)(p + 1280)) + (*(const LAS f32x4*)(p + 2560) + *(const LAS f32x4*)(p + 3840));
  }
  const unsigned qb = OFF_Q + ((c.Mt * 2 + c.mw) * 20 + (2 * qi + (c.nw >> 1)) * 5) * 4096;
  u32x2 keep;
#pragma unroll
  for (int mi = 0; mi < 4; ++mi) {
    f32x2 d0 = lo2(acc[mi][0]) * lo2(z[0]), d1 = hi2(acc[mi][0]) * hi2(z[0]);
#pragma unroll
    for (int i = 1; i < 5; ++i) { d0 = fma2(lo2(acc[mi][i]), lo2(z[i]), d0); d1 = fma2(hi2(acc[mi][i]), hi2(z[i]), d1); }
    d0 += d1;
    const f32x2 inv = bc2(__builtin_amdgcn_rcpf(d0[0] + d0[1] + 1e-6f));
    unsigned o[10];
#pragma unroll
    for (int i = 0; i < 5; ++i) { o[2 * i] = pk2(lo2(acc[mi][i]) * inv); o[2 * i + 1] = pk2(hi2(acc[mi][i]) * inv); }
    st16(rws, lane * 16 + mi * 1024, qb + (c.nw & 1) * 12288, u32x4{o[0], o[1], o[2], o[3]});
    st16(rws, lane * 16 + mi * 1024, qb + (c.nw & 1) * 12288 + 4096, u32x4{o[4], o[5], o[6], o[7]});
    if (mi & 1) st16(rws, (lane & 31) * 16 + (lane >> 5) * 1024 + (mi - 1) * 1024, qb + 8192 + (c.nw & 1) * 512, pair16(keep, u32x2{o[8], o[9]}));
    else keep = u32x2{o[8], o[9]};
    if (mi == 0 && c.w == 7) { SB(); fv = wt.fetch(); SB(); }
  }
  SB();
  if (c.w == 7) wt.eval(fv, false);
  WAIT_L0(); BAR();
  wt.pcnt = c.mw; wt.plast = 3; wt.preps = 2; wt.pflag = OFF_FH + (c.Mt * 2 + c.mw) * 256 + (9 + qi) * 8;
}

DEVI void epi_kv(LAS char* lds, f32x4 (&acc)[4][6], char* ws, rsrc_t rws, u64 tag, const P1& c, int lane) {
  const int cl = lane & 15, g = lane >> 4, kj = c.cslot - 9;
  __builtin_amdgcn_s_setprio(1);
  float cs[5];
#pragma unroll
  for (int i = 0; i < 5; ++i) cs[i] = -*(LAS float*)(lds + L1_CS + ((c.nw * 6 + i) * 16 + cl) * 4);
  f32x4 mean[4], rstd[4];
#pragma unroll
  for (int mi = 0; mi < 4; ++mi) {
    mean[mi] = *(LAS f32x4*)(lds + L1_ST + (c.mw * 64 + mi * 16 + 4 * g) * 4);
    rstd[mi] = *(LAS f32x4*)(lds + L1_ST + 512 + (c.mw * 64 + mi * 16 + 4 * g) * 4);
  }
  u32x4 fk[2][3], fv[2][3];
  auto center = [&](int mi, int i, int hh) { return fma2(hh ? hi2(mean[mi]) : lo2(mean[mi]), bc2(cs[i]), hh ? hi2(acc[mi][i]) : lo2(acc[mi][i])); };
#pragma unroll
  for (int mi = 0; mi < 4; ++mi)
#pragma unroll
    for (int f = 0; f < 3; ++f)
#pragma unroll
      for (int hh = 0; hh < 2; ++hh)
        fk[mi >> 1][f][(mi & 1) * 2 + hh] = pk2(phi2(center(mi, f < 2 ? f : 4, hh), hh ? hi2(rstd[mi]) : lo2(rstd[mi])));
  const int uh = cl < 4 ? 16 + cl : 32 + cl;
  const f32x4 zero = {0.f, 0.f, 0.f, 0.f};
  {
    const u32x4 one = {0x3f803f80u, 0x3f803f80u, 0x3f803f80u, 0x3f803f80u};
    f32x4 z[3];
#pragma unroll
    for (int f = 0; f < 3; ++f) z[f] = mfma(one, fk[1][f], mfma(one, fk[0][f], zero));
    const float v = g == 0 ? z[0][0] : g == 1 ? z[1][0] : z[2][0];
    const float vn = __builtin_bit_cast(float, __builtin_amdgcn_mov_dpp(__builtin_bit_cast(int, v), 0xB1, 0xf, 0xf, true));
    st8(rws, (cl & 1) ? OOB : g < 2 ? (g * 20 + cl) * 4 : (g == 2 && cl < 8) ? uh * 4 : OOB,
        OFF_ZP + (kj * 32 + c.Mt * 2 + c.mw) * 640 + c.nw * 160, u32x2{pk2(v, vn), (unsigned)tag});
    if (lds_inc((LAS unsigned*)(lds + L2_FLAG) + LF_PUB, lane) == 7u && lane < 8) set_flag(ws, OFF_FZ + lane * 512 + kj * 128 + c.Mt * 8, tag);
    __builtin_amdgcn_s_setprio(0);
    SB();
  }
#pragma unroll
  for (int mi = 0; mi < 4; ++mi)
#pragma unroll
    for (int f = 0; f < 3; ++f)
#pragma unroll
      for (int hh = 0; hh < 2; ++hh)
        fv[mi >> 1][f][(mi & 1) * 2 + hh] = pk2(center(mi, 2 + f, hh) * (hh ? hi2(rstd[mi]) : lo2(rstd[mi])));
  constexpr int KI[7] = {0, 0, 2, 1, 1, 2, 2}, VI[7] = {0, 2, 0, 1, 2, 1, 2};
  f32x4 p[7];
#pragma unroll
  for (int q = 0; q < 7; ++q) p[q] = mfma(fv[1][VI[q]], fk[1][KI[q]], mfma(fv[0][VI[q]], fk[0][KI[q]], zero));
  LAS char* ex = lds + L1_EX + c.w * 3584;
  auto put = [&](bool ok, int u, int e, f32x4 v) { *(LAS f32x4*)(ex + (ok ? (u * 20 + e) * 4 : 3200)) = v; };
  put(true, cl, 4 * g, p[0]);
  put(true, 20 + cl, 4 * g, p[3]);
  put(g >= 2, (g & 1) * 20 + cl, 16, g == 2 ? p[1] : p[4]);
  put(cl < 8, uh, 4 * g, cl < 4 ? p[2] : p[5]);
  put(cl < 8 && g == 2 + (cl >> 2), uh, 16, p[6]);
  WAIT_L0(); BAR();
#pragma unroll
  for (int q = 0; q < 2; ++q) {
    const int o = (c.mw * 128 + q * 64 + lane) * 16;
    const LAS char* src = lds + L1_EX + c.nw * 3584 + (o < 3200 ? o : 0);
    const f32x4 v = *(const LAS f32x4*)src + *(const LAS f32x4*)(src + 4 * 3584);
    st16(rws, o < 3200 ? o : OOB, OFF_KVP + ((kj * 4 + c.nw) * 16 + c.Mt) * 3456,
         u32x4{pk2(lo2(v)), (unsigned)tag, pk2(hi2(v)), (unsigned)tag});
  }
  SB();
  LAS unsigned* lf = (LAS unsigned*)(lds + L2_FLAG);
  if (lds_inc(lf + LF_PUB + 1, lane) == 7u && lane < 8) set_flag(ws, OFF_FK + lane * 512 + kj * 128 + c.Mt * 8, tag);
  BAR();
}

DEVI void retile(const char* wd, rsrc_t rws, unsigned ldsw, int id, int w, int lane) {
  const u32x4 rd = srd(wd, kF * 1280);
  const int jj = lane >> 3, g = lane >> 4;
  const unsigned vo = ((jj >> 2) * 8 + (jj & 3)) * 1280 + (lane & 7) * 16;
  const unsigned ra = ldsw + g * 1024 + ((lane & 15) >> 2) * 128 + (lane & 3) * 8;
  auto fetch = [&](int n) {
    const int it = id * 8 + w + n * 128, rb = it / 10, ln = it - rb * 10;
#pragma unroll
    for (int q = 0; q < 4; ++q)
      dma<0>(rd, vo, it < 540 ? (rb * 32 + 16 * (q >> 1) + 4 * (q & 1)) * 1280 + ln * 128 : OOB, ldsw + (n & 3) * 4096 + q * 1024);
  };
  auto put = [&](int n) {
    const int it = id * 8 + w + n * 128, rb = it / 10, ln = it - rb * 10, cg = rb / 6, uu = rb - cg * 6;
    const int ks = 18 * (uu >> 1) + 2 * cg + (uu & 1);
#pragma unroll
    for (int q = 0; q < 4; ++q) {
      const int nt = ln * 4 + q, j = nt / 5, t = nt - j * 5;
      st16(rws, lane * 16, it < 540 ? OFF_WD + ((j * 54 + ks) * 5 + t) * 1024 : OOB, lds_trfrag(ra + (n & 3) * 4096 + q * 32));
    }
  };
#pragma unroll 1
  for (int n = 0; n < 4; ++n) fetch(n);
  WAIT_V(12);
  put(0);
  WAIT_L0();
  fetch(4);
#pragma unroll 1
  for (int n = 1; n < 5; ++n) {
    if (n == 4) WAIT_V(12); else WAIT_V(16);
    put(n);
    SB();
  }
}

DEVI void fold_kv(LAS char* lds, char* ws, rsrc_t rws, Watch& wt, const u32x4 (&wf)[2][5], int hp, int w, int lane) {
  const u64 tag = wt.tag;
  const int cl = lane & 15, g = lane >> 4;
  const int o = ((w & 3) * 64 + lane) * 16;
  unsigned po = o < 3200 ? o : OOB, ko[6][2];
#pragma unroll
  for (int f = 0; f < 6; ++f) {
    const int u = 20 * (f & 1) + (f < 4 ? (cl & 4) * 2 + 4 * (f >> 1) : 16) + (cl & 3);
    const bool mine = f >= 4 || (cl >> 3) == (f & 1);
    ko[f][0] = (unsigned)(size_t)lds + L2_RED + (mine && g < 3 ? u * 80 + g * 32 : 3360);
    ko[f][1] = (unsigned)(size_t)lds + L2_RED + (mine && g < 2 ? u * 80 + g * 32 + 16 : 3360);
    PIN(ko[f][0]); PIN(ko[f][1]);
  }
  unsigned vo = OFF_MP + (w * 20 + (hp >> 2) * 5) * 5120 + (3 * ((hp >> 1) & 1) + (g & 1)) * 5120 + (((hp & 1) + (g & 2)) * 16 + cl) * 16;
  unsigned vo2 = OFF_MP + (w * 20 + (hp >> 2) * 5 + 2) * 5120 + g * 1024 + ((hp & 3) * 16 + cl) * 16;
  PIN(po); PIN(vo); PIN(vo2);
  WAIT_V(0);
  if (w == 0) wait_flags(ws, OFF_FK + (blockIdx.x & 7) * 512 + (hp >> 2) * 128 + cl * 8, tag);
  BAR();
  {
    *(LAS f32x4*)(lds + L2_RED + (w >> 2) * 4096 + o) = sweep8(rws, po, OFF_KVP + (hp * 16 + (w >> 2) * 8) * 3456, 3456, (unsigned)tag);
    WAIT_L0(); BAR();
  }
  u64 fv = 0;
  if (w == 7) fv = wt.fetch();
  SB();
  u32x4 kf[6];
#pragma unroll
  for (int f = 0; f < 6; ++f) {
    const f32x4 lo = *(LAS f32x4*)(size_t)ko[f][0] + *(LAS f32x4*)(size_t)(ko[f][0] + 4096);
    const f32x4 hi = *(LAS f32x4*)(size_t)ko[f][1] + *(LAS f32x4*)(size_t)(ko[f][1] + 4096);
    kf[f] = u32x4{pk2(lo2(lo)), pk2(hi2(lo)), pk2(lo2(hi)), pk2(hi2(hi))};
  }
  const f32x4 zero = {0.f, 0.f, 0.f, 0.f};
  u32x4 r1;
#pragma unroll
  for (int i = 0; i < 5; ++i) {
    const f32x4 d1 = mfma(kf[1], wf[1][i], mfma(kf[0], wf[0][i], zero)), d2 = mfma(kf[3], wf[1][i], mfma(kf[2], wf[0][i], zero));
    const f32x4 d3 = mfma(kf[4], wf[0][i], zero), d4 = mfma(kf[5], wf[1][i], zero);
    st16(rws, vo, i * 1024, u32x4{pk2(lo2(d1)), pk2(hi2(d1)), pk2(lo2(d2)), pk2(hi2(d2))});
    const u32x4 t = {pk2(lo2(d3)), pk2(hi2(d3)), pk2(lo2(d4)), pk2(hi2(d4))};
    if (i == 0 || i == 4) r1 = t; else r1 = g == i ? t : r1;
    if (i == 3) st16(rws, vo2, 0, r1);
    if (i == 4) st16(rws, g == 0 ? vo2 + 4096 : OOB, 0, r1);
  }
  SB();
  if (w == 7) wt.eval(fv, false);
  WAIT_L0(); BAR();
  wt.pcnt = 0; wt.plast = 7; wt.preps = 8; wt.pflag = OFF_F3 + hp * 8;
}

template <bool ATT>
DEVI void p2_steps(LAS char* lds, rsrc_t rws, Watch& wt, const u32x4 (&br)[3][5], f32x4 (&acc)[4][5], const u32x2 (&xr)[5],
                   int mt2, int j, int w, int lane) {
  LAS unsigned* lf = wt.lf;
  const unsigned ab = OFF_H + (mt2 * 54 + w) * 4096;
  u32x4 fa[12][4], fb[12][5];
  auto ldA = [&](int n) {
    const unsigned so = n >= 7 ? OFF_Q + (mt2 * 20 + (w - 4) * 5 + n - 7) * 4096
                               : n == 6 && w >= 6 ? OOB : ab + (n < 3 ? 30 + 8 * ((n + (mt2 >> 1)) % 3) : 8 * n - 24) * 4096;
#pragma unroll
    for (int m = 0; m < 4; ++m) fa[n][m] = ld16(rws, lane * 16, so + ((m + w) & 3) * 1024);
    SB();
  };
  unsigned lb = (unsigned)(size_t)lds + w * 5120 + lane * 16;
  auto rdB = [&](int n) {
    if (n == 5) { lb += 81920; PIN(lb); }
    if (ATT && n == 6) { lb -= w >= 6 ? 40960 : 0; PIN(lb); }
#pragma unroll
    for (int t = 0; t < 5; ++t) fb[n][t] = lds_b128(lb + ((n - 3) & 1) * 40960 + t * 1024);
    SB();
  };
  auto ld1 = [&](int kind, int n, int i) {
    if (kind == 0) {
      const unsigned so = n >= 7 ? OFF_Q + (mt2 * 20 + (w - 4) * 5 + n - 7) * 4096
                                 : n == 6 && w >= 6 ? OOB : ab + (n < 3 ? 30 + 8 * ((n + (mt2 >> 1)) % 3) : 8 * n - 24) * 4096;
      fa[n][i] = ld16(rws, lane * 16, so + ((i + w) & 3) * 1024);
    } else fb[n][i] = ld16(rws, lane * 16, OFF_MP + (j * 20 + (w - 4) * 5 + n - 7) * 5120 + i * 1024);
    SB();
  };
  LAS char* ex = lds + w * 5120 + lane * 16;
  constexpr int PRE = ATT ? 6 : 3;
#pragma unroll
  for (int n = 0; n < PRE; ++n) ldA(n);
  if (ATT) {
#pragma unroll
    for (int t = 0; t < 5; ++t) {
      const f32x2 x0 = unpk(xr[t][0]), x1 = unpk(xr[t][1]);
      acc[0][t] = f32x4{x0[0], x0[1], x1[0], x1[1]};
      PIN(acc[0][t]);
    }
    SB();
    asm volatile("s_nop 1" ::: "memory");
  }
  constexpr int A1[12] = {ATT ? -1 : 3, ATT ? 6 : 5, -1, ATT ? 7 : -1, -1, -1, ATT ? 9 : -1, 10, 11, -1, -1, -1};
  constexpr int A2[12] = {ATT ? -1 : 4, ATT ? -1 : 6, -1, ATT ? 8 : -1, -1, -1, -1, -1, -1, -1, -1, -1};
  constexpr int M1[12] = {-1, -1, -1, -1, ATT ? 7 : -1, ATT ? 8 : -1, ATT ? 9 : -1, 10, 11, -1, -1, -1};
  u64 fv = 0;
  static_for<(ATT ? 12 : 7)>([&](auto N) {
    constexpr int n = N;
    if (n == 2) lds_wait(lf + LF_DMA, 8u);
    if (ATT && n == 3) lds_wait_bits(lf + LF_STATUS, S_Q);
    if (ATT && n == 4) lds_wait_bits(lf + LF_STATUS, S_M);
    if (!ATT && (n == 3 || n == 5) && w == 3) { if (n == 5) wt.eval(fv, false); fv = wt.fetch(); SB(); }
    if (n >= 2 && n < 6) rdB(n + 1);
#pragma unroll
    for (int q = 0; q < 20; ++q) {
      constexpr int LASTN = ATT ? 11 : 6;
      const int m = n == LASTN ? (q / 5 + 1) & 3 : n < 7 ? q / 5 : q & 3, t = n < 7 || n == LASTN ? q % 5 : q >> 2;
      if (n == 0 && (m > 0 || !ATT)) mfma_new(acc[m][t], br[0][t], fa[0][m]);
      else mfma_acc(acc[m][t], n < 3 ? br[n < 3 ? n : 0][t] : fb[n][t], fa[n][m]);
      if (n == LASTN && q >= 10) {
        SB();
        *(LAS f32x4*)(ex + ((q - 10) / 5) * 40960 + (q % 5) * 1024) = acc[(q - 10) / 5 + 1][q % 5];
        SB();
      }
      if (q & 1) {
        const int p = q >> 1, na = (A1[n] >= 0 ? 4 : 0) + (A2[n] >= 0 ? 4 : 0);
        if (p < na) ld1(0, p < 4 ? A1[n] : A2[n], p & 3);
        else if (M1[n] >= 0 && p - na < 5) ld1(1, M1[n], p - na);
      }
    }
    SB();
  });
  if (!ATT && w == 3) wt.eval(fv, false);
}

DEVI void phase2(LAS char* lds, const char* x, char* ws, rsrc_t rws, char* out, Watch& wt, u32x4 (&br)[3][5],
                 int mt2, int j, int w, int lane) {
  const int a = w & 3;
  LAS unsigned* lf = wt.lf;
  const unsigned obase = (mt2 * 64 + a * 16) * 1280 + j * 160;
  f32x4 acc[4][5];
  u32x2 xr[5];
  if (wt.preps != 0) { if (w == 7 && !(wt.status & S_H)) wt.poll(false); WAIT_V(0); wt.publish(); }
  else WAIT_V(15);
  lds_inc(lf + LF_DMA, lane);
  wt.need(S_H);
  if (w >= 4) {
    const rsrc_t rx = __builtin_amdgcn_make_buffer_rsrc((void*)x, 0, 2048 * 1280, 0x00020000);
#pragma unroll
    for (int t = 0; t < 5; ++t) xr[t] = __builtin_amdgcn_raw_buffer_load_b64(rx, (lane & 15) * 1280 + (lane >> 4) * 8 + t * 32, obase, 0);
    SB();
  }
  if (w < 4) {
    __builtin_amdgcn_s_setprio(2);
    p2_steps<false>(lds, rws, wt, br, acc, xr, mt2, j, w, lane);
    __builtin_amdgcn_s_setprio(0);
#pragma unroll
    for (int q = 0; q < 15; ++q) asm volatile("" : "=v"(br[q / 5][q % 5]));
  }
  PINS(w);
  if (w >= 4) { __builtin_amdgcn_s_setprio(1); p2_steps<true>(lds, rws, wt, br, acc, xr, mt2, j, w, lane); }
  asm volatile("s_nop 7" ::: "memory");
  SB();
  LAS char* ex = lds + (w >> 2) * 20480 + lane * 16;
#pragma unroll
  for (int t = 0; t < 5; ++t) *(LAS f32x4*)(ex + 2 * 40960 + (a * 5 + t) * 1024) = acc[3][t];
  WAIT_L0();
  if (w < 4) { lds_inc(lf + LF_X, lane); if (w == 3) wt.need(S_Q | S_M, 3); lds_wait(lf + LF_X, 4u); }
  else BAR();
#pragma unroll
  for (int t = 0; t < 5; ++t)
#pragma unroll
    for (int q = 1; q < 4; ++q) acc[0][t] += *(LAS f32x4*)(ex + (q - 1) * 40960 + (((a - q) & 3) * 5 + t) * 1024);
  LAS char* base = lds + L2_BASE + a * 5120 + lane * 16;
  if (w < 4) {
#pragma unroll
    for (int t = 0; t < 5; ++t) *(LAS f32x4*)(base + t * 1024) = acc[0][t];
    WAIT_L0();
  } else {
    const rsrc_t ro = __builtin_amdgcn_make_buffer_rsrc(out, 0, 2048 * 1280, 0x00020000);
    u32x2 o[5];
#pragma unroll
    for (int t = 0; t < 5; ++t) {
      const f32x4 s = acc[0][t] + *(LAS f32x4*)(base + t * 1024);
      o[t] = u32x2{pk2(lo2(s)), pk2(hi2(s))};
    }
    const unsigned orow = obase + (lane & 15) * 1280;
#pragma unroll
    for (int t = 0; t < 4; t += 2) {
      const auto s0 = __builtin_amdgcn_permlane16_swap(o[t][0], o[t + 1][0], false, false);
      const auto s1 = __builtin_amdgcn_permlane16_swap(o[t][1], o[t + 1][1], false, false);
      __builtin_amdgcn_raw_buffer_store_b128(u32x4{s0[0], s1[0], s0[1], s1[1]}, ro, orow + ((lane >> 4) & 1) * 32 + (lane >> 5) * 16 + t * 32, 0, 0);
    }
    __builtin_amdgcn_raw_buffer_store_b64(o[4], ro, orow + (lane >> 4) * 8 + 128, 0, 0);
  }
}

__global__ __launch_bounds__(512, 2) void fused(const char* x, const char* wqkv, const char* wo, const char* wg,
                                                const char* wu, const char* wd, char* ws, char* out) {
  extern __shared__ __attribute__((aligned(16))) char smem[];
  const char* pc0;
  asm volatile("s_getpc_b64 %0" : "=s"(pc0));
  LAS char* lds = (LAS char*)smem;
  const unsigned ldsb = (unsigned)(size_t)lds;
  const int bid = blockIdx.x, xcd = bid & 7, idx = bid >> 3;
  const int Mt = (xcd & 1) * 8 + (idx >> 2), grp = xcd >> 1, pos4 = (idx & 3) ^ 2, slot2 = grp * 4 + pos4;
  const int cslot = (int)(0xCF76BE54AD329810ull >> (4 * (pos4 == 2 ? (slot2 + 4 * (Mt >> 1)) & 15 : slot2))) & 15;
  const int w = __builtin_amdgcn_readfirstlane(threadIdx.x >> 6), lane = threadIdx.x & 63;
  warm_code(cslot == 15 ? CODE_BYTES : 0u, pc0, ldsb + L1_GR + 8192, w + 8 * (Mt & 1), lane);
  asm volatile("" : "+s"(x), "+s"(wqkv), "+s"(wo), "+s"(wg), "+s"(wu), "+s"(wd), "+s"(ws), "+s"(out));
  const rsrc_t rws = __builtin_amdgcn_make_buffer_rsrc(ws, 0, WS_END, 0x00020000);
  u64 tag;
  u32x2 epoch = {0u, 0u};
  ld8_hidden(epoch, srd(ws, WS_END), OFF_E);
  Watch wt{(LAS unsigned*)(lds + L2_FLAG), ws, 0ull, Mt * 2 + (slot2 >> 3), slot2 & 7, w, lane, 0u, 0, 0, 0, 0u};
  u32x4 br[3][5];
  PF2 pf{ws, rws, ldsb, slot2 & 7, w, lane, Mt % 3, br};
  if (cslot == 15) {
    if (threadIdx.x < 16) ((LAS unsigned*)(lds + L2_FLAG))[threadIdx.x] = 0u;
    retile(wd, rws, ldsb + w * 16384, Mt, w, lane);
    wt.tag = tag = make_tag<0>(epoch);
    WAIT_L0(); BAR();
    if (threadIdx.x == 0) { set_flag(ws, OFF_FR + Mt * 8, tag); set_flag(ws, OFF_F1 + (Mt * 16 + 15) * 8, tag); }
    u32x4 wf[2][5];
    {
      const u32x4 ro = srd(wo, kD * 1280);
#pragma unroll 1
      for (int q = 0; q < 10; ++q)
        dma<0>(ro, (lane >> 3) * 1280 + (lane & 7) * 16, (Mt * 40 + (q >> 1) * 8) * 1280 + ((w * 160) & ~127) + (q & 1) * 128, ldsb + w * 16384 + q * 1024);
      WAIT_V(0);
#pragma unroll
      for (int hh = 0; hh < 2; ++hh) {
        unsigned ra[2];
#pragma unroll
        for (int h2 = 0; h2 < 2; ++h2) {
          int row = hh * 20 + (lane >> 4) * 8 + h2 * 4 + ((lane & 15) >> 2);
          row = row < 40 ? row : 39;
          ra[h2] = ldsb + w * 16384 + (row >> 3) * 2048 + (row & 7) * 128 + (lane & 3) * 8;
        }
#pragma unroll
        for (int i = 0; i < 5; ++i) {
          const int cb = (w & 3) * 32 + i * 32, o = (cb >> 7) * 1024 + (cb & 127);
          const u32x2 lo = __builtin_bit_cast(u32x2, __builtin_amdgcn_ds_read_tr16_b64_v4i16((LAS s16x4*)(size_t)(ra[0] + o)));
          const u32x2 hi = __builtin_bit_cast(u32x2, __builtin_amdgcn_ds_read_tr16_b64_v4i16((LAS s16x4*)(size_t)(ra[1] + o)));
          wf[hh][i] = u32x4{lo[0], lo[1], hi[0], hi[1]};
        }
      }
    }
    wait_flags(ws, OFF_FR + (lane & 15) * 8, tag);
    WAIT_L0(); BAR();
    wd_prefetch<0, 6>(srd(ws, WS_END), ldsb, slot2 & 7, w, lane);
    pf.items<0, 28>();
    if (Mt == 0 && w == 7) {
#pragma unroll 1
      for (int q = 0; q < 4; ++q) wait_flags(ws, OFF_F1 + (q * 64 + lane) * 8, tag);
      if (lane == 0) set_flag(ws, OFF_E, tag);
    }
    fold_kv(lds, ws, rws, wt, wf, Mt, w, lane);
  } else {
    const int type = cslot < 9 ? 0 : cslot < 13 ? 1 : 2;
    P1 c;
    c.w = w; c.mw = w >> 2; c.nw = w & 3; c.Mt = Mt; c.cslot = cslot; c.j = slot2 & 7;
    c.ldsb = ldsb;
    {
      const int kb = (Mt + cslot) % 10, rot = kb - kb % 3;
      c.bstar = kb - rot;
      const int seg = w >> 2;
      c.pitch = type == 0 ? kF * 2 : kQKV * 2;
      const int startb = type == 0 ? cslot * 384
                       : ((type == 1 ? (640 * (1 + seg) + 160 * (cslot - 9)) * 2 : 320 * (2 * (cslot - 13) + seg)) & ~127);
      const char* wb = type == 0 ? (seg ? wu : wg) : wqkv;
      c.rw = srd(wb + startb - 2048, 640 * c.pitch - startb + 2048);
      c.rx = srd(x + (size_t)Mt * 128 * 1280, 128 * 1280);
      const int i = lane >> 3, cp = lane & 7, pg = w & 3;
      c.voffB = (unsigned)(8 * pg + i) * c.pitch + 16 * (cp ^ ((i & 2) | ((pg & 1) << 2))) + 2048;
      c.voffA = (unsigned)(8 * w + i) * 1280 + 16 * (cp ^ ((4 * w + (i >> 1)) & 7));
      c.oB = rot * 64 * c.pitch; c.oA = rot * 128;
      dma<0>(c.rx, c.voffA, c.oA, c.ldsb + L1_A + w * 1024);
    }
    auto pro = [&](int s) {
      const unsigned lB = c.ldsb + L1_B + w * 3072, lA = c.ldsb + L1_A + w * 1024;
      dma<0>(c.rw, c.voffB, c.oB, lB + s * L1_BST);
      dma<128>(c.rw, c.voffB, c.oB, lB + s * L1_BST + 1024);
      dma<256>(c.rw, c.voffB, c.oB, lB + s * L1_BST + 2048);
      c.oB += 32 * c.pitch;
      if (s == 1) { c.oB = c.oB == 640 * c.pitch ? 0u : c.oB; c.oA = c.oA == 1152u ? 0u : c.oA + 128; }
      dma<0>(c.rx, c.voffA, c.oA + ((s + 1) & 1) * 81920, lA + (s + 1) * 8192);
      SB();
    };
#pragma unroll
    for (int s = 0; s < 1; ++s) pro(s);
    unsigned vb[5], va0;
    {
      const int g = lane >> 4, q = (lane & 15) >> 2, p = lane & 3, r = lane & 15, nw = c.nw;
      const int swz = (q & 2) | ((g & 1) << 2);
#pragma unroll
      for (int pos = 0; pos < 5; ++pos) {
        const int cf = 16 * nw + 4 * p;
        const int hb = (cslot & 1 ? 0 : 32) + 40 * nw;
        const int ck = pos < 4 ? (pos >> 1) * 192 + hb + 20 * (pos & 1) + 4 * p : (p >> 1) * 192 + hb + 20 * (p & 1) + 16;
        const int cq = (nw >> 1) * 224 + 20 * (4 * (nw & 1) + 2 * (p & 1) + (p >> 1)) + 4 * pos;
        const int col = type == 0 ? cf : type == 1 ? ck : cq;
        const int line = col >> 6, seg = line >= 3;
        vb[pos] = c.ldsb + L1_B + seg * (12288 - 3072) + (g * 3 + line) * 1024 + q * 128 + 16 * (((col & 63) >> 3) ^ swz) + 8 * ((col >> 2) & 1);
      }
      va0 = c.ldsb + L1_A + (c.mw * 64 + r) * 128 + 16 * (g ^ ((r >> 1) & 7));
    }
    if (threadIdx.x < 16) ((LAS unsigned*)(lds + L2_FLAG))[threadIdx.x] = 0u;
    f32x4 acc[4][6];
#pragma unroll
    for (int m = 0; m < 4; ++m)
#pragma unroll
      for (int i = 0; i < 6; ++i) acc[m][i] = f32x4{0.f, 0.f, 0.f, 0.f};
    unsigned foff = lane >= 20 ? OFF_FR + (lane & 15) * 8 : OFF_F1 + (lane < 10 ? lane * 16 + cslot : Mt * 16 + lane - 10) * 8;
    PIN(foff);
#pragma unroll
    for (int i = 0; i < 5; ++i) PIN(vb[i]);
    SB();
#pragma unroll
    for (int s = 1; s < 3; ++s) pro(s);
    c.oA = c.oA == 1152u ? 0u : c.oA + 128;
    SB();
    wt.tag = tag = make_tag<8>(epoch);
#define P1_RUN(T) do { if (w < 4) p1_loop<T, true>(c, vb, va0, foff, acc, pf, ws, rws, tag, lane); \
                       else p1_loop<T, false>(c, vb, va0, foff, acc, pf, ws, rws, tag, lane); } while (0)
    if (type == 0) { P1_RUN(0); epi_ffn(lds, acc, ws, rws, wt, c, pf, lane); }
    else if (type == 2) { P1_RUN(2); epi_q(lds, acc, ws, rws, wt, c, pf, lane); }
    else {
      P1_RUN(1);
      epi_kv(lds, acc, ws, rws, tag, c, lane);
      wd_prefetch<0, 6>(srd(ws, WS_END), ldsb, c.j, w, lane);
      pf.items<0, 28>();
    }
  }
  phase2(lds, x, ws, rws, out, wt, br, Mt * 2 + (slot2 >> 3), slot2 & 7, w, lane);
}

extern "C" void kernel_launch(void* const* d_in, const int* in_sizes, int n_in, void* d_out, int out_size,
                              void* d_ws, size_t ws_size, hipStream_t stream) {
  (void)in_sizes; (void)n_in; (void)out_size; (void)ws_size;
  (void)hipFuncSetAttribute((const void*)fused, hipFuncAttributeMaxDynamicSharedMemorySize, LDS_SIZE);
  fused<<<256, 512, LDS_SIZE, stream>>>((const char*)d_in[0], (const char*)d_in[1], (const char*)d_in[2], (const char*)d_in[4],
                                        (const char*)d_in[5], (const char*)d_in[6], (char*)d_ws, (char*)d_out);
}
```
